```python
import torch

def _softmax(x, axis=-1):
    return torch.softmax(x, dim=axis)
def _gelu(x):
    return torch.nn.functional.gelu(x, approximate="tanh")
def _silu(x):
    return torch.nn.functional.silu(x)
def _sigmoid(x):
    return torch.sigmoid(x)
def _layernorm(x, axis=-1, eps=1e-5):
    m = x.mean(dim=axis, keepdim=True)
    v = x.var(dim=axis, keepdim=True, unbiased=False)
    return (x - m) / torch.sqrt(v + eps)
def _scatter_add(tgt, idx, src, axis=0):
    return tgt.clone().index_add_(axis, idx, src)
def _segment_sum(src, seg, n):
    out = torch.zeros((n,) + src.shape[1:], dtype=src.dtype)
    return out.index_add_(0, seg, src)
def _pad(x, pw):
    flat = tuple(v for pair in reversed(pw) for v in pair)
    return torch.nn.functional.pad(x, flat)

kernel_name = 'standalone__mha_block_cached__near_ridge'

def setup_inputs():

    _w = (lambda a: (lambda t: t.to(torch.bfloat16) if t.is_floating_point() else t)(torch.as_tensor(a)))
    B, Sq, D = 16, 112, 640
    H, Sk, Dh = 32, 512, 192
    return {
        "x": _w((lambda *s: torch.randn(*s, dtype=torch.bfloat16) * 0.8)(B, Sq, D)),
        "k_cache": _w((lambda *s: torch.randn(*s, dtype=torch.bfloat16) * 0.8)(B, H, Sk, Dh)),
        "v_cache": _w((lambda *s: torch.randn(*s, dtype=torch.bfloat16) * 0.05)(B, H, Sk, Dh)),
        "wq": _w((lambda *s: torch.randn(*s, dtype=torch.bfloat16) * 0.8)(D, H * Dh) * (D ** -0.5)),
        "wo": _w((lambda *s: torch.randn(*s, dtype=torch.bfloat16) * 0.05)(H * Dh, D) * ((H * Dh) ** -0.5)),
    }

def reference(x, k_cache, v_cache, wq, wo):
    B, Sq, D = x.shape
    H, Dh = 32, 192
    q = (lambda x, *d: x.permute(*d))((x @ wq).reshape(B, Sq, H, Dh), 0, 2, 1, 3)
    scores = torch.einsum("bhqd,bhkd->bhqk", q, k_cache) * (Dh ** -0.5)
    p = _softmax(scores, axis=-1)
    o = torch.einsum("bhqk,bhkd->bhqd", p, v_cache)
    return (lambda x, *d: x.permute(*d))(o, 0, 2, 1, 3).reshape(B, Sq, H * Dh) @ wo
```

```cpp
#include <hip/hip_runtime.h>
#include <hip/hip_bf16.h>
typedef unsigned short u16;
__device__ __forceinline__ float bf2f(u16 x){ return __uint_as_float(((unsigned)x)<<16); }
__device__ __forceinline__ u16 f2bf(float f){ unsigned u=__float_as_uint(f); u += 0x7fff + ((u>>16)&1); return (u16)(u>>16); }
constexpr int B=16,SQ=112,D=640,H=32,SK=512,DH=192;
__global__ void gemm_naive(const u16* A,const u16* W,u16* C,int M,int N,int K){
  int n=blockIdx.x*256+threadIdx.x; int m0=blockIdx.y*8;
  if(n>=N) return;
  float acc[8]={0};
  for(int k=0;k<K;++k){ float w=bf2f(W[(long)k*N+n]); for(int i=0;i<8;++i) acc[i]+=bf2f(A[(long)(m0+i)*K+k])*w; }
  for(int i=0;i<8;++i) C[(long)(m0+i)*N+n]=f2bf(acc[i]);
}
__global__ void attn_naive(const u16* Q,const u16* Kc,const u16* Vc,u16* O){
  int q=blockIdx.x, h=blockIdx.y, b=blockIdx.z, t=threadIdx.x;
  __shared__ float qs[DH]; __shared__ float s[SK]; __shared__ float red[256];
  if(t<DH) qs[t]=bf2f(Q[((long)(b*SQ+q))*(H*DH)+h*DH+t]);
  __syncthreads();
  const u16* Kp=Kc+((long)(b*H+h))*SK*DH; const u16* Vp=Vc+((long)(b*H+h))*SK*DH;
  float mx=-1e30f;
  for(int k=t;k<SK;k+=256){ float a=0; for(int d=0;d<DH;++d) a+=qs[d]*bf2f(Kp[k*DH+d]); a*=0.07216878364870322f; s[k]=a; mx=fmaxf(mx,a);}
  red[t]=mx; __syncthreads();
  for(int o=128;o>0;o>>=1){ if(t<o) red[t]=fmaxf(red[t],red[t+o]); __syncthreads(); }
  mx=red[0]; __syncthreads();
  float sum=0; for(int k=t;k<SK;k+=256){ float e=__expf(s[k]-mx); s[k]=e; sum+=e; }
  red[t]=sum; __syncthreads();
  for(int o=128;o>0;o>>=1){ if(t<o) red[t]+=red[t+o]; __syncthreads(); }
  sum=red[0];
  if(t<DH){ float a=0; for(int k=0;k<SK;++k) a+=s[k]*bf2f(Vp[k*DH+t]); O[((long)(b*SQ+q))*(H*DH)+h*DH+t]=f2bf(a/sum);}
}
extern "C" void kernel_launch(void* const* d_in, const int* in_sizes, int n_in,
                              void* d_out, int out_size, void* d_ws, size_t ws_size,
                              hipStream_t stream) {
  const u16* x=(const u16*)d_in[0]; const u16* k=(const u16*)d_in[1]; const u16* v=(const u16*)d_in[2];
  const u16* wq=(const u16*)d_in[3]; const u16* wo=(const u16*)d_in[4];
  u16* Q=(u16*)d_ws; u16* O=Q+(long)B*SQ*H*DH;
  gemm_naive<<<dim3(H*DH/256,B*SQ/8),256,0,stream>>>(x,wq,Q,B*SQ,H*DH,D);
  attn_naive<<<dim3(SQ,H,B),256,0,stream>>>(Q,k,v,O);
  gemm_naive<<<dim3((D+255)/256,B*SQ/8),256,0,stream>>>(O,wo,(u16*)d_out,B*SQ,D,H*DH);
}
```

```cpp
#include <hip/hip_runtime.h>

__global__ __launch_bounds__(256, 1) void mha_block_fused(const void* x, const void* kc, const void* vc, const void* wq,
                                                          const void* wo, void* out, void* ws) {
  __shared__ __attribute__((aligned(16))) char smem[163840];
  const unsigned wid = __builtin_amdgcn_readfirstlane(threadIdx.x >> 6);
  const unsigned ldsbase = (unsigned)(size_t)(__attribute__((address_space(3))) char*)smem;

  asm volatile(
R"(.macro mz dk,d,ak,a,bk,b
v_mfma_f32_16x16x32_bf16 \dk[\d:\d+3],\ak[\a:\a+3],\bk[\b:\b+3],0
.endm
.macro ma dk,d,ak,a,bk,b
v_mfma_f32_16x16x32_bf16 \dk[\d:\d+3],\ak[\a:\a+3],\bk[\b:\b+3],\dk[\d:\d+3]
.endm
.macro mc dk,d,ak,a,bk,b,c
v_mfma_f32_16x16x32_bf16 \dk[\d:\d+3],\ak[\a:\a+3],\bk[\b:\b+3],\dk[\c:\c+3]
.endm
.macro tr d,v,o
ds_read_b64_tr_b16 a[\d:\d+1],v\v offset:\o
.endm
.macro rd dk,d,v,o
ds_read_b128 \dk[\d:\d+3],v\v offset:\o
.endm
.macro dl v,r,s,o
buffer_load_dwordx4 v\v,s[\r:\r+3],s\s offen offset:\o lds
.endm
.macro dn v,r,s,o
buffer_load_dwordx4 v\v,s[\r:\r+3],s\s offen offset:\o nt lds
.endm
.macro wl n
s_waitcnt lgkmcnt(\n)
.endm
.macro cv d,a,b
v_cvt_pk_bf16_f32 v\d,v\a,v\b
.endm
s_load_dwordx8 s[4:11],%0,0
s_load_dwordx4 s[12:15],%0,32
s_load_dwordx2 s[16:17],%0,48
s_cmp_eq_u32 %2,3
s_cbranch_scc1 LB_start_%=
v_mbcnt_lo_u32_b32 v16,-1,0
v_mbcnt_hi_u32_b32 v16,-1,v16
s_getpc_b64 s[60:61]
s_lshr_b32 s88,%1,3
s_lshl_b32 s89,%2,5
s_add_u32 s88,s88,s89
s_lshl_b32 s88,s88,13
v_and_b32 v23,63,v16
v_lshl_add_u32 v23,v23,7,s88
v_min_u32 v23,0x1b450,v23
global_load_dwordx2 v[40:41],v23,s[60:61] 
s_mov_b32 s64,%2
s_add_u32 s88,%1,0
s_and_b32 s88,s88,7
s_lshr_b32 s89,%1,3
s_and_b32 s90,s89,1
s_lshr_b32 s65,s89,1
s_lshl_b32 s66,s88,1
s_add_u32 s66,s66,s90
s_and_b32 s92,s64,1
v_lshrrev_b32 v17,4,v16
v_lshrrev_b32 v21,3,v16
v_and_b32 v22,7,v16
s_mul_i32 s88,s65,5
s_lshr_b32 s68,s88,3
s_lshr_b32 s88,s65,2
s_mul_i32 s88,s88,3
s_mul_i32 s89,s90,5
s_add_u32 s67,s88,s89
s_cmp_ge_u32 s67,10
s_cselect_b32 s89,10,0
s_sub_u32 s67,s67,s89
v_mul_u32_u24 v24,0x500,v21
s_xor_b32 s89,s92,0
s_lshl_b32 s89,s89,2
v_add_u32 v23,s89,v17
v_xor_b32 v23,v23,v22
v_lshl_add_u32 v235,v23,4,v24
s_xor_b32 s89,s92,1
s_lshl_b32 s89,s89,2
v_add_u32 v23,s89,v17
v_xor_b32 v23,v23,v22
v_lshl_add_u32 v236,v23,4,v24
v_add_u32 v237,0x4800,v235
v_add_u32 v236,0x2400,v236
v_add_u32 v238,0x4800,v236
s_mul_i32 s73,s64,0x18000
s_add_u32 s74,s73,0x780000
s_mul_i32 s88,s67,0xc0000
s_add_u32 s72,s73,s88
s_mul_i32 s75,s64,0xc00
s_mul_i32 s77,s64,0x7800
s_add_u32 s78,s77,0x500
s_lshl_b32 s89,s67,7
s_add_u32 s76,s77,s89
s_add_u32 s79,s75,0xf000
s_mov_b32 s69,64
s_mov_b32 s79,s75
v_and_b32 v23,2,v21
s_lshl_b32 s89,s92,2
v_or_b32 v23,s89,v23
v_xor_b32 v23,v23,v22
v_mul_u32_u24 v24,0x3000,v21
v_lshl_add_u32 v234,v23,4,v24
v_add_u32 v233,0x880,v234
v_add_u32 v232,0xc00,v234
v_add_u32 v252,0x500,v234
s_getreg_b32 s69,hwreg(20,0,4)
s_xor_b32 s69,s69,1
s_mov_b32 s95,0x60000
s_mul_i32 s88,s65,0x23000
s_mul_i32 s91,s66,0x300
s_mov_b32 s22,0x23000
s_mov_b32 s23,0x20000
s_mov_b32 s26,0x77df00
s_mov_b32 s27,0x20000
wl 0
s_add_u32 s20,s4,s88
s_addc_u32 s21,s5,0
s_and_b32 s21,s21,0xffff
s_add_u32 s24,s10,s91
s_addc_u32 s25,s11,0
s_sub_u32 s24,s24,0xc00
s_subb_u32 s25,s25,0
s_and_b32 s25,s25,0xffff
s_mov_b32 m0,s79
s_nop 0
dl 235,20,76,0
dl 236,20,76,1024
dl 237,20,76,2048
s_addk_i32 s76,128
s_cmp_ge_u32 s76,s78
s_cselect_b32 s76,s77,s76
s_add_u32 m0,s75,0x13000
s_nop 0
dl 232,24,72,0
dl 233,24,72,1024
dl 252,24,72,2048
s_add_u32 s90,s72,0x180
s_add_u32 m0,s75,0x16000
s_nop 0
dl 232,24,90,0
dl 233,24,90,1024
dl 252,24,90,2048
s_add_u32 s72,s72,s95
s_lshl_b32 s88,s65,7
s_add_u32 s54,s16,s88
s_addc_u32 s55,s17,0
v_mov_b32 v23,0
global_load_dwordx2 v[250:251],v23,s[54:55] sc1
s_lshl_b32 s88,s65,4
s_add_u32 s88,s88,s66
s_mul_i32 s88,s88,0x60000
s_add_u32 s28,s6,s88
s_addc_u32 s29,s7,0
s_and_b32 s29,s29,0xffff
s_mov_b32 s30,0x60000
s_mov_b32 s31,0x20000
s_add_u32 s36,s8,s88
s_addc_u32 s37,s9,0
s_and_b32 s37,s37,0xffff
s_mov_b32 s38,0x60000
s_mov_b32 s39,0x20000
s_mul_i32 s35,s64,0xc00
s_lshl_b32 s89,s92,3
v_add_u32 v23,0,v16
v_mul_u32_u24 v24,0xaab,v23
v_lshrrev_b32 v24,16,v24
v_and_b32 v25,6,v24
v_xor_b32 v25,v25,v16
v_lshl_add_u32 v231,v25,4,s35
v_add_u32 v24,s89,v24
v_lshrrev_b32 v24,1,v24
v_xor_b32 v24,v24,v16
v_lshl_add_u32 v228,v24,4,s35
v_add_u32 v23,64,v16
v_mul_u32_u24 v24,0xaab,v23
v_lshrrev_b32 v24,16,v24
v_and_b32 v25,6,v24
v_xor_b32 v25,v25,v16
v_lshl_add_u32 v246,v25,4,s35
v_add_u32 v24,s89,v24
v_lshrrev_b32 v24,1,v24
v_xor_b32 v24,v24,v16
v_lshl_add_u32 v229,v24,4,s35
v_add_u32 v23,0x80,v16
v_mul_u32_u24 v24,0xaab,v23
v_lshrrev_b32 v24,16,v24
v_and_b32 v25,6,v24
v_xor_b32 v25,v25,v16
v_lshl_add_u32 v247,v25,4,s35
v_add_u32 v24,s89,v24
v_lshrrev_b32 v24,1,v24
v_xor_b32 v24,v24,v16
v_lshl_add_u32 v230,v24,4,s35
s_mov_b32 s80,0
s_mov_b32 s83,0
s_mov_b32 s82,0x1f000
s_mov_b32 s84,0x13000
s_mov_b32 s96,0x3000
s_mov_b32 s97,0xc000
s_mov_b32 s98,0xffff7000
s_mov_b32 s99,0x28000
s_mov_b32 s34,0x1c000
s_lshl_b32 s88,s65,11
s_add_u32 s88,s88,0x1000
s_add_u32 s52,s16,s88
s_addc_u32 s53,s17,0
)"
R"(s_mul_i32 s88,s66,0x78000
s_add_u32 s40,s12,s88
s_addc_u32 s41,s13,0
s_and_b32 s41,s41,0xffff
s_mov_b32 s42,0x78000
s_mov_b32 s43,0x20000
s_mul_i32 s88,s65,0x230000
s_add_u32 s88,s88,0x10000
s_add_u32 s44,s16,s88
s_addc_u32 s45,s17,0
s_and_b32 s45,s45,0xffff
s_mov_b32 s46,0x230000
s_mov_b32 s47,0x20000
s_mul_i32 s88,s65,0x23000
s_add_u32 s48,s14,s88
s_addc_u32 s49,s15,0
s_and_b32 s49,s49,0xffff
s_mov_b32 s50,0x23000
s_mov_b32 s51,0x20000
v_and_b32 v18,15,v16
v_lshrrev_b32 v19,2,v18
v_and_b32 v20,3,v16
v_lshlrev_b32 v239,4,v16
v_and_b32 v23,1,v17
v_lshlrev_b32 v24,1,v23
v_lshrrev_b32 v25,1,v19
v_or_b32 v24,v24,v25
v_lshlrev_b32 v21,3,v20
v_lshl_add_u32 v21,v19,7,v21
v_mul_u32_u24 v22,0xc00,v17
v_add_u32 v22,v22,v21
v_xor_b32 v25,0,v24
v_lshl_add_u32 v222,v25,5,v22
v_xor_b32 v25,1,v24
v_lshl_add_u32 v223,v25,5,v22
v_xor_b32 v25,2,v24
v_lshl_add_u32 v224,v25,5,v22
v_xor_b32 v25,3,v24
v_lshl_add_u32 v225,v25,5,v22
v_lshl_add_u32 v22,v17,2,v19
v_mul_u32_u24 v22,0x180,v22
v_lshl_add_u32 v22,v20,3,v22
v_add_u32 v22,0x13000,v22
v_xor_b32 v25,0,v24
v_lshl_add_u32 v218,v25,5,v22
v_xor_b32 v25,1,v24
v_lshl_add_u32 v219,v25,5,v22
v_xor_b32 v25,2,v24
v_lshl_add_u32 v220,v25,5,v22
v_xor_b32 v25,3,v24
v_lshl_add_u32 v221,v25,5,v22
s_lshl_b32 s88,s64,12
s_add_u32 s88,s88,0xf000
v_lshlrev_b32 v21,7,v18
v_add_u32 v21,s88,v21
v_lshrrev_b32 v24,1,v18
v_mul_u32_u24 v23,0x180,v18
v_add_u32 v23,0x1f000,v23
v_add_u32 v25,0,v17
v_xor_b32 v25,v25,v24
v_lshl_add_u32 v226,v25,4,v21
v_lshl_add_u32 v216,v25,4,v23
v_add_u32 v32,0xe000,v226
v_add_u32 v25,4,v17
v_xor_b32 v25,v25,v24
v_lshl_add_u32 v227,v25,4,v21
v_lshl_add_u32 v217,v25,4,v23
v_add_u32 v33,0xe000,v227
v_mov_b32 v23,0x3f803f80
v_accvgpr_write_b32 a240,v23
v_accvgpr_write_b32 a241,v23
v_accvgpr_write_b32 a242,v23
v_accvgpr_write_b32 a243,v23
s_mov_b32 s85,0x3000
s_mov_b32 s86,0x3000
s_mov_b32 s58,0x3dd53b95
s_mov_b32 s59,0x3dd53b95
s_mov_b32 s87,0x41000000
s_add_u32 m0,s75,0x19000
s_nop 0
dl 232,24,72,0
dl 233,24,72,1024
dl 252,24,72,2048
s_add_u32 s90,s72,0x180
s_add_u32 m0,s75,0x1c000
s_nop 0
dl 232,24,90,0
dl 233,24,90,1024
dl 252,24,90,2048
s_add_u32 s72,s72,s95
s_cmp_ge_u32 s72,s74
s_cselect_b32 s72,s73,s72
s_add_u32 m0,s79,0x3800
s_nop 0
dl 235,20,76,0
dl 236,20,76,1024
dl 237,20,76,2048
s_addk_i32 s76,128
s_cmp_ge_u32 s76,s78
s_cselect_b32 s76,s77,s76
v_add_u32 v0,0x7000,v222
v_add_u32 v1,0x7000,v223
v_add_u32 v2,0x7000,v224
v_add_u32 v3,0x7000,v225
v_add_u32 v8,0x13000,v222
v_add_u32 v9,0x13000,v223
v_add_u32 v10,0x13000,v224
v_add_u32 v11,0x13000,v225
v_add_u32 v16,0xffff1000,v226
v_add_u32 v17,0xffff1000,v227
v_add_u32 v24,0x1f000,v16
v_add_u32 v25,0x1f000,v17
s_waitcnt vmcnt(13)
s_barrier
rd v,200,16,0
rd v,204,16,2048
tr 48,8,0
tr 50,8,512
tr 52,9,0
tr 54,9,512
tr 56,10,0
tr 58,10,512
tr 60,11,0
tr 62,11,512
tr 64,8,1024
tr 66,8,1536
tr 68,9,1024
tr 70,9,1536
tr 72,10,1024
tr 74,10,1536
tr 76,11,1024
tr 78,11,1536
wl 12
mz v,48,a,48,v,200
tr 80,8,2048
tr 82,8,2560
mz v,52,a,48,v,204
rd v,208,17,0
tr 84,9,2048
mz v,56,a,52,v,200
rd v,212,17,2048
tr 86,9,2560
mz v,60,a,52,v,204
wl 14
tr 88,10,2048
mz v,64,a,56,v,200
tr 90,10,2560
mz v,68,a,56,v,204
s_waitcnt vmcnt(10)
tr 92,11,2048
s_barrier
mz v,72,a,60,v,200
tr 94,11,2560
mz v,76,a,60,v,204
wl 14
tr 48,8,12288
mz v,80,a,64,v,200
tr 50,8,12800
mz v,84,a,64,v,204
tr 52,9,12288
mz v,88,a,68,v,200
tr 54,9,12800
mz v,92,a,68,v,204
wl 14
tr 56,10,12288
mz v,96,a,72,v,200
tr 58,10,12800
mz v,100,a,72,v,204
tr 60,11,12288
mz v,104,a,76,v,200
tr 62,11,12800
mz v,108,a,76,v,204
wl 12
tr 64,8,13312
mz v,112,a,80,v,200
tr 66,8,13824
mz v,116,a,80,v,204
tr 68,9,13312
mz v,120,a,84,v,200
tr 70,9,13824
mz v,124,a,84,v,204
wl 12
tr 72,10,13312
mz v,128,a,88,v,200
tr 74,10,13824
mz v,132,a,88,v,204
tr 76,11,13312
mz v,136,a,92,v,200
tr 78,11,13824
mz v,140,a,92,v,204
wl 12
tr 80,8,14336
mz a,96,a,48,v,200
tr 82,8,14848
mz a,100,a,48,v,204
tr 84,9,14336
mz a,104,a,52,v,200
tr 86,9,14848
mz a,108,a,52,v,204
wl 12
)"
R"(tr 88,10,14336
mz a,112,a,56,v,200
tr 90,10,14848
mz a,116,a,56,v,204
s_waitcnt vmcnt(3)
tr 92,11,14336
s_barrier
mz a,120,a,60,v,200
s_add_u32 m0,s75,0x7000
tr 94,11,14848
mz a,124,a,60,v,204
wl 12
dl 232,24,72,0
mz a,128,a,64,v,200
tr 48,8,24576
tr 50,8,25088
mz a,132,a,64,v,204
dl 233,24,72,1024
mz a,136,a,68,v,200
tr 52,9,24576
tr 54,9,25088
mz a,140,a,68,v,204
wl 12
dl 252,24,72,2048
mz a,144,a,72,v,200
s_add_u32 s90,s72,0x180
tr 56,10,24576
mz a,148,a,72,v,204
s_add_u32 m0,s75,0xa000
tr 58,10,25088
mz a,152,a,76,v,200
tr 60,11,24576
tr 62,11,25088
mz a,156,a,76,v,204
wl 12
dl 232,24,90,0
mz a,160,a,80,v,200
tr 64,8,25600
tr 66,8,26112
mz a,164,a,80,v,204
dl 233,24,90,1024
mz a,168,a,84,v,200
tr 68,9,25600
tr 70,9,26112
mz a,172,a,84,v,204
wl 12
dl 252,24,90,2048
mz a,176,a,88,v,200
s_add_u32 s72,s72,s95
tr 72,10,25600
tr 74,10,26112
mz a,180,a,88,v,204
s_add_u32 m0,s75,0xd000
tr 76,11,25600
mz a,184,a,92,v,200
wl 11
dl 232,24,72,0
mz a,188,a,92,v,204
tr 78,11,26112
dl 233,24,72,1024
dl 252,24,72,2048
s_add_u32 s90,s72,0x180
s_add_u32 m0,s75,0x10000
s_nop 0
dl 232,24,90,0
dl 233,24,90,1024
dl 252,24,90,2048
s_add_u32 s72,s72,s95
s_cmp_ge_u32 s72,s74
s_cselect_b32 s72,s73,s72
s_add_u32 m0,s79,0x1f000
s_nop 0
dl 235,20,76,0
dl 236,20,76,1024
dl 237,20,76,2048
s_addk_i32 s76,128
s_cmp_ge_u32 s76,s78
s_cselect_b32 s76,s77,s76
s_waitcnt vmcnt(9)
s_barrier
ma v,48,a,48,v,208
s_add_u32 m0,s75,0x13000
tr 80,8,26624
ma v,52,a,48,v,212
tr 82,8,27136
rd v,200,16,14336
ma v,56,a,52,v,208
tr 84,9,26624
rd v,204,16,16384
ma v,60,a,52,v,212
wl 13
tr 86,9,27136
tr 88,10,26624
ma v,64,a,56,v,208
dl 232,24,72,0
ma v,68,a,56,v,212
tr 90,10,27136
tr 92,11,26624
ma v,72,a,60,v,208
dl 233,24,72,1024
ma v,76,a,60,v,212
wl 13
tr 94,11,27136
tr 48,8,36864
ma v,80,a,64,v,208
tr 50,8,37376
ma v,84,a,64,v,212
tr 52,9,36864
ma v,88,a,68,v,208
tr 54,9,37376
ma v,92,a,68,v,212
wl 14
tr 56,10,36864
ma v,96,a,72,v,208
dl 252,24,72,2048
ma v,100,a,72,v,212
tr 58,10,37376
tr 60,11,36864
ma v,104,a,76,v,208
tr 62,11,37376
ma v,108,a,76,v,212
wl 12
tr 64,8,37888
ma v,112,a,80,v,208
s_add_u32 s90,s72,0x180
tr 66,8,38400
ma v,116,a,80,v,212
s_add_u32 m0,s75,0x16000
tr 68,9,37888
ma v,120,a,84,v,208
dl 232,24,90,0
ma v,124,a,84,v,212
wl 11
tr 70,9,38400
tr 72,10,37888
ma v,128,a,88,v,208
tr 74,10,38400
ma v,132,a,88,v,212
tr 76,11,37888
ma v,136,a,92,v,208
tr 78,11,38400
ma v,140,a,92,v,212
wl 12
tr 80,8,38912
ma a,96,a,48,v,208
dl 233,24,90,1024
ma a,100,a,48,v,212
tr 82,8,39424
tr 84,9,38912
ma a,104,a,52,v,208
tr 86,9,39424
ma a,108,a,52,v,212
wl 12
tr 88,10,38912
ma a,112,a,56,v,208
tr 90,10,39424
ma a,116,a,56,v,212
tr 92,11,38912
ma a,120,a,60,v,208
dl 252,24,90,2048
ma a,124,a,60,v,212
s_add_u32 s72,s72,s95
tr 94,11,39424
tr 48,0,0
wl 13
ma a,128,a,64,v,208
tr 50,0,512
ma a,132,a,64,v,212
tr 52,1,0
ma a,136,a,68,v,208
tr 54,1,512
ma a,140,a,68,v,212
wl 12
tr 56,2,0
ma a,144,a,72,v,208
tr 58,2,512
ma a,148,a,72,v,212
tr 60,3,0
ma a,152,a,76,v,208
tr 62,3,512
ma a,156,a,76,v,212
wl 12
tr 64,0,1024
ma a,160,a,80,v,208
tr 66,0,1536
ma a,164,a,80,v,212
tr 68,1,1024
ma a,168,a,84,v,208
tr 70,1,1536
ma a,172,a,84,v,212
wl 12
tr 72,2,1024
ma a,176,a,88,v,208
tr 74,2,1536
ma a,180,a,88,v,212
tr 76,3,1024
ma a,184,a,92,v,208
wl 11
tr 78,3,1536
ma a,188,a,92,v,212
)"
R"(s_waitcnt vmcnt(9)
s_barrier
ma v,48,a,48,v,200
s_add_u32 m0,s75,0x19000
tr 80,0,2048
ma v,52,a,48,v,204
tr 82,0,2560
rd v,208,17,14336
ma v,56,a,52,v,200
tr 84,1,2048
rd v,212,17,16384
ma v,60,a,52,v,204
wl 13
tr 86,1,2560
tr 88,2,2048
ma v,64,a,56,v,200
dl 232,24,72,0
ma v,68,a,56,v,204
tr 90,2,2560
tr 92,3,2048
ma v,72,a,60,v,200
dl 233,24,72,1024
ma v,76,a,60,v,204
wl 13
tr 94,3,2560
tr 48,0,12288
ma v,80,a,64,v,200
dl 252,24,72,2048
ma v,84,a,64,v,204
tr 50,0,12800
tr 52,1,12288
ma v,88,a,68,v,200
s_add_u32 s90,s72,0x180
tr 54,1,12800
ma v,92,a,68,v,204
s_add_u32 m0,s75,0x1c000
tr 56,2,12288
wl 15
ma v,96,a,72,v,200
dl 232,24,90,0
ma v,100,a,72,v,204
tr 58,2,12800
tr 60,3,12288
ma v,104,a,76,v,200
tr 62,3,12800
ma v,108,a,76,v,204
wl 12
tr 64,0,13312
ma v,112,a,80,v,200
dl 233,24,90,1024
ma v,116,a,80,v,204
tr 66,0,13824
tr 68,1,13312
ma v,120,a,84,v,200
tr 70,1,13824
ma v,124,a,84,v,204
wl 12
tr 72,2,13312
ma v,128,a,88,v,200
dl 252,24,90,2048
ma v,132,a,88,v,204
tr 74,2,13824
tr 76,3,13312
ma v,136,a,92,v,200
s_add_u32 s72,s72,s95
tr 78,3,13824
s_cmp_ge_u32 s72,s74
ma v,140,a,92,v,204
s_cselect_b32 s72,s73,s72
tr 80,0,14336
s_add_u32 m0,s79,0x22800
wl 13
ma a,96,a,48,v,200
dl 235,20,76,0
ma a,100,a,48,v,204
tr 82,0,14848
tr 84,1,14336
ma a,104,a,52,v,200
tr 86,1,14848
ma a,108,a,52,v,204
wl 12
tr 88,2,14336
ma a,112,a,56,v,200
dl 236,20,76,1024
ma a,116,a,56,v,204
tr 90,2,14848
tr 92,3,14336
ma a,120,a,60,v,200
tr 94,3,14848
ma a,124,a,60,v,204
wl 12
tr 48,0,24576
ma a,128,a,64,v,200
dl 237,20,76,2048
ma a,132,a,64,v,204
s_addk_i32 s76,128
tr 50,0,25088
tr 52,1,24576
ma a,136,a,68,v,200
s_cmp_ge_u32 s76,s78
tr 54,1,25088
s_cselect_b32 s76,s77,s76
ma a,140,a,68,v,204
wl 12
tr 56,2,24576
ma a,144,a,72,v,200
tr 58,2,25088
ma a,148,a,72,v,204
tr 60,3,24576
ma a,152,a,76,v,200
tr 62,3,25088
ma a,156,a,76,v,204
wl 12
tr 64,0,25600
ma a,160,a,80,v,200
tr 66,0,26112
ma a,164,a,80,v,204
tr 68,1,25600
ma a,168,a,84,v,200
tr 70,1,26112
ma a,172,a,84,v,204
wl 12
tr 72,2,25600
ma a,176,a,88,v,200
tr 74,2,26112
ma a,180,a,88,v,204
tr 76,3,25600
ma a,184,a,92,v,200
wl 11
tr 78,3,26112
ma a,188,a,92,v,204
s_waitcnt vmcnt(9)
s_barrier
ma v,48,a,48,v,208
s_add_u32 m0,s75,0x7000
tr 80,0,26624
ma v,52,a,48,v,212
tr 82,0,27136
rd v,200,24,0
ma v,56,a,52,v,208
tr 84,1,26624
rd v,204,24,2048
ma v,60,a,52,v,212
wl 13
tr 86,1,27136
tr 88,2,26624
ma v,64,a,56,v,208
dl 232,24,72,0
ma v,68,a,56,v,212
tr 90,2,27136
tr 92,3,26624
ma v,72,a,60,v,208
dl 233,24,72,1024
ma v,76,a,60,v,212
wl 13
tr 94,3,27136
tr 48,0,36864
ma v,80,a,64,v,208
tr 50,0,37376
ma v,84,a,64,v,212
tr 52,1,36864
ma v,88,a,68,v,208
tr 54,1,37376
ma v,92,a,68,v,212
wl 14
tr 56,2,36864
ma v,96,a,72,v,208
dl 252,24,72,2048
ma v,100,a,72,v,212
tr 58,2,37376
tr 60,3,36864
ma v,104,a,76,v,208
tr 62,3,37376
ma v,108,a,76,v,212
wl 12
tr 64,0,37888
ma v,112,a,80,v,208
s_add_u32 s90,s72,0x180
tr 66,0,38400
ma v,116,a,80,v,212
s_add_u32 m0,s75,0xa000
tr 68,1,37888
ma v,120,a,84,v,208
dl 232,24,90,0
ma v,124,a,84,v,212
wl 11
tr 70,1,38400
tr 72,2,37888
ma v,128,a,88,v,208
tr 74,2,38400
ma v,132,a,88,v,212
tr 76,3,37888
ma v,136,a,92,v,208
tr 78,3,38400
ma v,140,a,92,v,212
wl 12
tr 80,0,38912
ma a,96,a,48,v,208
dl 233,24,90,1024
ma a,100,a,48,v,212
)"
R"(tr 82,0,39424
tr 84,1,38912
ma a,104,a,52,v,208
tr 86,1,39424
ma a,108,a,52,v,212
wl 12
tr 88,2,38912
ma a,112,a,56,v,208
tr 90,2,39424
ma a,116,a,56,v,212
tr 92,3,38912
ma a,120,a,60,v,208
dl 252,24,90,2048
ma a,124,a,60,v,212
s_add_u32 s72,s72,s95
tr 94,3,39424
tr 48,8,0
wl 13
ma a,128,a,64,v,208
tr 50,8,512
ma a,132,a,64,v,212
tr 52,9,0
ma a,136,a,68,v,208
tr 54,9,512
ma a,140,a,68,v,212
wl 12
tr 56,10,0
ma a,144,a,72,v,208
tr 58,10,512
ma a,148,a,72,v,212
tr 60,11,0
ma a,152,a,76,v,208
tr 62,11,512
ma a,156,a,76,v,212
wl 12
tr 64,8,1024
ma a,160,a,80,v,208
tr 66,8,1536
ma a,164,a,80,v,212
tr 68,9,1024
ma a,168,a,84,v,208
tr 70,9,1536
ma a,172,a,84,v,212
wl 12
tr 72,10,1024
ma a,176,a,88,v,208
tr 74,10,1536
ma a,180,a,88,v,212
tr 76,11,1024
ma a,184,a,92,v,208
wl 11
tr 78,11,1536
ma a,188,a,92,v,212
s_waitcnt vmcnt(9)
s_barrier
ma v,48,a,48,v,200
s_add_u32 m0,s75,0xd000
tr 80,8,2048
ma v,52,a,48,v,204
tr 82,8,2560
rd v,208,25,0
ma v,56,a,52,v,200
tr 84,9,2048
rd v,212,25,2048
ma v,60,a,52,v,204
wl 13
tr 86,9,2560
tr 88,10,2048
ma v,64,a,56,v,200
dl 232,24,72,0
ma v,68,a,56,v,204
tr 90,10,2560
tr 92,11,2048
ma v,72,a,60,v,200
dl 233,24,72,1024
ma v,76,a,60,v,204
wl 13
tr 94,11,2560
tr 48,8,12288
ma v,80,a,64,v,200
dl 252,24,72,2048
ma v,84,a,64,v,204
tr 50,8,12800
tr 52,9,12288
ma v,88,a,68,v,200
s_add_u32 s90,s72,0x180
tr 54,9,12800
ma v,92,a,68,v,204
s_add_u32 m0,s75,0x10000
tr 56,10,12288
wl 15
ma v,96,a,72,v,200
dl 232,24,90,0
ma v,100,a,72,v,204
tr 58,10,12800
tr 60,11,12288
ma v,104,a,76,v,200
tr 62,11,12800
ma v,108,a,76,v,204
wl 12
tr 64,8,13312
ma v,112,a,80,v,200
dl 233,24,90,1024
ma v,116,a,80,v,204
tr 66,8,13824
tr 68,9,13312
ma v,120,a,84,v,200
tr 70,9,13824
ma v,124,a,84,v,204
wl 12
tr 72,10,13312
ma v,128,a,88,v,200
dl 252,24,90,2048
ma v,132,a,88,v,204
tr 74,10,13824
tr 76,11,13312
ma v,136,a,92,v,200
s_add_u32 s72,s72,s95
tr 78,11,13824
s_cmp_ge_u32 s72,s74
ma v,140,a,92,v,204
s_cselect_b32 s72,s73,s72
tr 80,8,14336
s_mov_b32 m0,s79
wl 13
ma a,96,a,48,v,200
dl 235,20,76,0
ma a,100,a,48,v,204
tr 82,8,14848
tr 84,9,14336
ma a,104,a,52,v,200
tr 86,9,14848
ma a,108,a,52,v,204
wl 12
tr 88,10,14336
ma a,112,a,56,v,200
dl 236,20,76,1024
ma a,116,a,56,v,204
tr 90,10,14848
tr 92,11,14336
ma a,120,a,60,v,200
tr 94,11,14848
ma a,124,a,60,v,204
wl 12
tr 48,8,24576
ma a,128,a,64,v,200
dl 237,20,76,2048
ma a,132,a,64,v,204
s_addk_i32 s76,128
tr 50,8,25088
tr 52,9,24576
ma a,136,a,68,v,200
s_cmp_ge_u32 s76,s78
tr 54,9,25088
s_cselect_b32 s76,s77,s76
ma a,140,a,68,v,204
wl 12
tr 56,10,24576
ma a,144,a,72,v,200
tr 58,10,25088
ma a,148,a,72,v,204
tr 60,11,24576
ma a,152,a,76,v,200
tr 62,11,25088
ma a,156,a,76,v,204
wl 12
tr 64,8,25600
ma a,160,a,80,v,200
tr 66,8,26112
ma a,164,a,80,v,204
tr 68,9,25600
ma a,168,a,84,v,200
tr 70,9,26112
ma a,172,a,84,v,204
wl 12
tr 72,10,25600
ma a,176,a,88,v,200
tr 74,10,26112
ma a,180,a,88,v,204
tr 76,11,25600
ma a,184,a,92,v,200
wl 11
tr 78,11,26112
ma a,188,a,92,v,204
s_waitcnt vmcnt(9)
s_barrier
ma v,48,a,48,v,208
s_add_u32 m0,s75,0x13000
tr 80,8,26624
ma v,52,a,48,v,212
tr 82,8,27136
rd v,200,24,14336
ma v,56,a,52,v,208
tr 84,9,26624
rd v,204,24,16384
ma v,60,a,52,v,212
wl 13
tr 86,9,27136
tr 88,10,26624
ma v,64,a,56,v,208
)"
R"(dl 232,24,72,0
ma v,68,a,56,v,212
tr 90,10,27136
tr 92,11,26624
ma v,72,a,60,v,208
dl 233,24,72,1024
ma v,76,a,60,v,212
wl 13
tr 94,11,27136
tr 48,8,36864
ma v,80,a,64,v,208
tr 50,8,37376
ma v,84,a,64,v,212
tr 52,9,36864
ma v,88,a,68,v,208
tr 54,9,37376
ma v,92,a,68,v,212
wl 14
tr 56,10,36864
ma v,96,a,72,v,208
dl 252,24,72,2048
ma v,100,a,72,v,212
tr 58,10,37376
tr 60,11,36864
ma v,104,a,76,v,208
tr 62,11,37376
ma v,108,a,76,v,212
wl 12
tr 64,8,37888
ma v,112,a,80,v,208
s_add_u32 s90,s72,0x180
tr 66,8,38400
ma v,116,a,80,v,212
s_add_u32 m0,s75,0x16000
tr 68,9,37888
ma v,120,a,84,v,208
dl 232,24,90,0
ma v,124,a,84,v,212
wl 11
tr 70,9,38400
tr 72,10,37888
ma v,128,a,88,v,208
tr 74,10,38400
ma v,132,a,88,v,212
tr 76,11,37888
ma v,136,a,92,v,208
tr 78,11,38400
ma v,140,a,92,v,212
wl 12
tr 80,8,38912
ma a,96,a,48,v,208
dl 233,24,90,1024
ma a,100,a,48,v,212
tr 82,8,39424
tr 84,9,38912
ma a,104,a,52,v,208
tr 86,9,39424
ma a,108,a,52,v,212
wl 12
tr 88,10,38912
ma a,112,a,56,v,208
tr 90,10,39424
ma a,116,a,56,v,212
tr 92,11,38912
ma a,120,a,60,v,208
dl 252,24,90,2048
ma a,124,a,60,v,212
s_add_u32 s72,s72,s95
tr 94,11,39424
tr 48,0,0
wl 13
ma a,128,a,64,v,208
tr 50,0,512
ma a,132,a,64,v,212
tr 52,1,0
ma a,136,a,68,v,208
tr 54,1,512
ma a,140,a,68,v,212
wl 12
tr 56,2,0
ma a,144,a,72,v,208
tr 58,2,512
ma a,148,a,72,v,212
tr 60,3,0
ma a,152,a,76,v,208
tr 62,3,512
ma a,156,a,76,v,212
wl 12
tr 64,0,1024
ma a,160,a,80,v,208
tr 66,0,1536
ma a,164,a,80,v,212
tr 68,1,1024
ma a,168,a,84,v,208
tr 70,1,1536
ma a,172,a,84,v,212
wl 12
tr 72,2,1024
ma a,176,a,88,v,208
tr 74,2,1536
ma a,180,a,88,v,212
tr 76,3,1024
ma a,184,a,92,v,208
wl 11
tr 78,3,1536
ma a,188,a,92,v,212
s_waitcnt vmcnt(9)
s_barrier
ma v,48,a,48,v,200
s_add_u32 m0,s75,0x19000
tr 80,0,2048
ma v,52,a,48,v,204
tr 82,0,2560
rd v,208,25,14336
ma v,56,a,52,v,200
tr 84,1,2048
rd v,212,25,16384
ma v,60,a,52,v,204
wl 13
tr 86,1,2560
tr 88,2,2048
ma v,64,a,56,v,200
dl 232,24,72,0
ma v,68,a,56,v,204
tr 90,2,2560
tr 92,3,2048
ma v,72,a,60,v,200
dl 233,24,72,1024
ma v,76,a,60,v,204
wl 13
tr 94,3,2560
tr 48,0,12288
ma v,80,a,64,v,200
dl 252,24,72,2048
ma v,84,a,64,v,204
tr 50,0,12800
tr 52,1,12288
ma v,88,a,68,v,200
s_add_u32 s90,s72,0x180
tr 54,1,12800
ma v,92,a,68,v,204
s_add_u32 m0,s75,0x1c000
tr 56,2,12288
wl 15
ma v,96,a,72,v,200
dl 232,24,90,0
ma v,100,a,72,v,204
tr 58,2,12800
tr 60,3,12288
ma v,104,a,76,v,200
tr 62,3,12800
ma v,108,a,76,v,204
wl 12
tr 64,0,13312
ma v,112,a,80,v,200
dl 233,24,90,1024
ma v,116,a,80,v,204
tr 66,0,13824
tr 68,1,13312
ma v,120,a,84,v,200
tr 70,1,13824
ma v,124,a,84,v,204
wl 12
tr 72,2,13312
ma v,128,a,88,v,200
dl 252,24,90,2048
ma v,132,a,88,v,204
tr 74,2,13824
tr 76,3,13312
ma v,136,a,92,v,200
s_add_u32 s72,s72,s95
tr 78,3,13824
s_cmp_ge_u32 s72,s74
ma v,140,a,92,v,204
s_cselect_b32 s72,s73,s72
tr 80,0,14336
s_add_u32 m0,s79,0x3800
wl 13
ma a,96,a,48,v,200
dl 235,20,76,0
ma a,100,a,48,v,204
tr 82,0,14848
tr 84,1,14336
ma a,104,a,52,v,200
tr 86,1,14848
ma a,108,a,52,v,204
wl 12
tr 88,2,14336
ma a,112,a,56,v,200
dl 236,20,76,1024
ma a,116,a,56,v,204
tr 90,2,14848
tr 92,3,14336
ma a,120,a,60,v,200
tr 94,3,14848
ma a,124,a,60,v,204
wl 12
tr 48,0,24576
ma a,128,a,64,v,200
dl 237,20,76,2048
)"
R"(ma a,132,a,64,v,204
s_addk_i32 s76,128
tr 50,0,25088
tr 52,1,24576
ma a,136,a,68,v,200
s_cmp_ge_u32 s76,s78
tr 54,1,25088
s_cselect_b32 s76,s77,s76
ma a,140,a,68,v,204
wl 12
tr 56,2,24576
ma a,144,a,72,v,200
tr 58,2,25088
ma a,148,a,72,v,204
tr 60,3,24576
ma a,152,a,76,v,200
tr 62,3,25088
ma a,156,a,76,v,204
wl 12
tr 64,0,25600
ma a,160,a,80,v,200
tr 66,0,26112
ma a,164,a,80,v,204
tr 68,1,25600
ma a,168,a,84,v,200
tr 70,1,26112
ma a,172,a,84,v,204
wl 12
tr 72,2,25600
ma a,176,a,88,v,200
tr 74,2,26112
ma a,180,a,88,v,204
tr 76,3,25600
ma a,184,a,92,v,200
wl 11
tr 78,3,26112
ma a,188,a,92,v,204
s_waitcnt vmcnt(9)
s_barrier
ma v,48,a,48,v,208
s_add_u32 m0,s75,0x7000
tr 80,0,26624
ma v,52,a,48,v,212
tr 82,0,27136
rd v,200,16,0
ma v,56,a,52,v,208
tr 84,1,26624
rd v,204,16,2048
ma v,60,a,52,v,212
wl 13
tr 86,1,27136
tr 88,2,26624
ma v,64,a,56,v,208
dl 232,24,72,0
ma v,68,a,56,v,212
tr 90,2,27136
tr 92,3,26624
ma v,72,a,60,v,208
dl 233,24,72,1024
ma v,76,a,60,v,212
wl 13
tr 94,3,27136
tr 48,0,36864
ma v,80,a,64,v,208
tr 50,0,37376
ma v,84,a,64,v,212
tr 52,1,36864
ma v,88,a,68,v,208
tr 54,1,37376
ma v,92,a,68,v,212
wl 14
tr 56,2,36864
ma v,96,a,72,v,208
dl 252,24,72,2048
ma v,100,a,72,v,212
tr 58,2,37376
tr 60,3,36864
ma v,104,a,76,v,208
tr 62,3,37376
ma v,108,a,76,v,212
wl 12
tr 64,0,37888
ma v,112,a,80,v,208
s_add_u32 s90,s72,0x180
tr 66,0,38400
ma v,116,a,80,v,212
s_add_u32 m0,s75,0xa000
tr 68,1,37888
ma v,120,a,84,v,208
dl 232,24,90,0
ma v,124,a,84,v,212
wl 11
tr 70,1,38400
tr 72,2,37888
ma v,128,a,88,v,208
tr 74,2,38400
ma v,132,a,88,v,212
tr 76,3,37888
ma v,136,a,92,v,208
tr 78,3,38400
ma v,140,a,92,v,212
wl 12
tr 80,0,38912
ma a,96,a,48,v,208
dl 233,24,90,1024
ma a,100,a,48,v,212
tr 82,0,39424
tr 84,1,38912
ma a,104,a,52,v,208
tr 86,1,39424
ma a,108,a,52,v,212
wl 12
tr 88,2,38912
ma a,112,a,56,v,208
tr 90,2,39424
ma a,116,a,56,v,212
tr 92,3,38912
ma a,120,a,60,v,208
dl 252,24,90,2048
ma a,124,a,60,v,212
s_add_u32 s72,s72,s95
tr 94,3,39424
tr 48,8,0
wl 13
ma a,128,a,64,v,208
tr 50,8,512
ma a,132,a,64,v,212
tr 52,9,0
ma a,136,a,68,v,208
tr 54,9,512
ma a,140,a,68,v,212
wl 12
tr 56,10,0
ma a,144,a,72,v,208
tr 58,10,512
ma a,148,a,72,v,212
tr 60,11,0
ma a,152,a,76,v,208
tr 62,11,512
ma a,156,a,76,v,212
wl 12
tr 64,8,1024
ma a,160,a,80,v,208
tr 66,8,1536
ma a,164,a,80,v,212
tr 68,9,1024
ma a,168,a,84,v,208
tr 70,9,1536
ma a,172,a,84,v,212
wl 12
tr 72,10,1024
ma a,176,a,88,v,208
tr 74,10,1536
ma a,180,a,88,v,212
tr 76,11,1024
ma a,184,a,92,v,208
wl 11
tr 78,11,1536
ma a,188,a,92,v,212
s_waitcnt vmcnt(9)
s_barrier
ma v,48,a,48,v,200
s_add_u32 m0,s75,0xd000
tr 80,8,2048
ma v,52,a,48,v,204
tr 82,8,2560
rd v,208,17,0
ma v,56,a,52,v,200
tr 84,9,2048
rd v,212,17,2048
ma v,60,a,52,v,204
wl 13
tr 86,9,2560
tr 88,10,2048
ma v,64,a,56,v,200
dl 232,24,72,0
ma v,68,a,56,v,204
tr 90,10,2560
tr 92,11,2048
ma v,72,a,60,v,200
dl 233,24,72,1024
ma v,76,a,60,v,204
wl 13
tr 94,11,2560
tr 48,8,12288
ma v,80,a,64,v,200
tr 50,8,12800
ma v,84,a,64,v,204
tr 52,9,12288
ma v,88,a,68,v,200
tr 54,9,12800
ma v,92,a,68,v,204
wl 14
tr 56,10,12288
ma v,96,a,72,v,200
dl 252,24,72,2048
ma v,100,a,72,v,204
tr 58,10,12800
tr 60,11,12288
ma v,104,a,76,v,200
)"
R"(tr 62,11,12800
ma v,108,a,76,v,204
wl 12
tr 64,8,13312
ma v,112,a,80,v,200
s_add_u32 s90,s72,0x180
tr 66,8,13824
ma v,116,a,80,v,204
s_add_u32 m0,s75,0x10000
tr 68,9,13312
ma v,120,a,84,v,200
dl 232,24,90,0
ma v,124,a,84,v,204
wl 11
tr 70,9,13824
tr 72,10,13312
ma v,128,a,88,v,200
tr 74,10,13824
ma v,132,a,88,v,204
tr 76,11,13312
ma v,136,a,92,v,200
tr 78,11,13824
ma v,140,a,92,v,204
wl 12
tr 80,8,14336
ma a,96,a,48,v,200
dl 233,24,90,1024
ma a,100,a,48,v,204
tr 82,8,14848
tr 84,9,14336
ma a,104,a,52,v,200
tr 86,9,14848
ma a,108,a,52,v,204
wl 12
tr 88,10,14336
ma a,112,a,56,v,200
tr 90,10,14848
ma a,116,a,56,v,204
tr 92,11,14336
ma a,120,a,60,v,200
dl 252,24,90,2048
ma a,124,a,60,v,204
s_add_u32 s72,s72,s95
tr 94,11,14848
tr 48,8,24576
wl 13
ma a,128,a,64,v,200
s_cmp_ge_u32 s72,s74
tr 50,8,25088
s_cselect_b32 s72,s73,s72
ma a,132,a,64,v,204
tr 52,9,24576
ma a,136,a,68,v,200
tr 54,9,25088
ma a,140,a,68,v,204
wl 12
tr 56,10,24576
ma a,144,a,72,v,200
tr 58,10,25088
ma a,148,a,72,v,204
tr 60,11,24576
ma a,152,a,76,v,200
tr 62,11,25088
ma a,156,a,76,v,204
wl 12
tr 64,8,25600
ma a,160,a,80,v,200
tr 66,8,26112
ma a,164,a,80,v,204
tr 68,9,25600
ma a,168,a,84,v,200
tr 70,9,26112
ma a,172,a,84,v,204
wl 12
tr 72,10,25600
ma a,176,a,88,v,200
tr 74,10,26112
ma a,180,a,88,v,204
tr 76,11,25600
ma a,184,a,92,v,200
wl 11
tr 78,11,26112
ma a,188,a,92,v,204
s_waitcnt vmcnt(6)
s_barrier
ma v,48,a,48,v,208
s_add_u32 m0,s75,0x13000
tr 80,8,26624
ma v,52,a,48,v,212
tr 82,8,27136
rd v,200,16,14336
ma v,56,a,52,v,208
tr 84,9,26624
rd v,204,16,16384
ma v,60,a,52,v,212
wl 13
tr 86,9,27136
tr 88,10,26624
ma v,64,a,56,v,208
dl 232,24,72,0
ma v,68,a,56,v,212
tr 90,10,27136
tr 92,11,26624
ma v,72,a,60,v,208
dl 233,24,72,1024
ma v,76,a,60,v,212
wl 13
tr 94,11,27136
tr 48,8,36864
ma v,80,a,64,v,208
dl 252,24,72,2048
ma v,84,a,64,v,212
tr 50,8,37376
tr 52,9,36864
ma v,88,a,68,v,208
s_add_u32 s90,s72,0x180
tr 54,9,37376
ma v,92,a,68,v,212
s_add_u32 m0,s75,0x16000
tr 56,10,36864
wl 15
ma v,96,a,72,v,208
dl 232,24,90,0
ma v,100,a,72,v,212
tr 58,10,37376
tr 60,11,36864
ma v,104,a,76,v,208
tr 62,11,37376
ma v,108,a,76,v,212
wl 12
tr 64,8,37888
ma v,112,a,80,v,208
dl 233,24,90,1024
ma v,116,a,80,v,212
tr 66,8,38400
tr 68,9,37888
ma v,120,a,84,v,208
tr 70,9,38400
ma v,124,a,84,v,212
wl 12
tr 72,10,37888
ma v,128,a,88,v,208
dl 252,24,90,2048
ma v,132,a,88,v,212
tr 74,10,38400
tr 76,11,37888
ma v,136,a,92,v,208
s_add_u32 s72,s72,s95
tr 78,11,38400
s_mov_b32 m0,s79
ma v,140,a,92,v,212
wl 12
tr 80,8,38912
ma a,96,a,48,v,208
dl 235,20,76,0
ma a,100,a,48,v,212
tr 82,8,39424
tr 84,9,38912
ma a,104,a,52,v,208
tr 86,9,39424
ma a,108,a,52,v,212
wl 12
tr 88,10,38912
ma a,112,a,56,v,208
dl 236,20,76,1024
ma a,116,a,56,v,212
tr 90,10,39424
tr 92,11,38912
ma a,120,a,60,v,208
tr 94,11,39424
ma a,124,a,60,v,212
wl 12
tr 48,0,0
ma a,128,a,64,v,208
dl 237,20,76,2048
ma a,132,a,64,v,212
s_addk_i32 s76,128
tr 50,0,512
tr 52,1,0
ma a,136,a,68,v,208
s_cmp_ge_u32 s76,s78
tr 54,1,512
s_cselect_b32 s76,s77,s76
ma a,140,a,68,v,212
wl 12
tr 56,2,0
ma a,144,a,72,v,208
tr 58,2,512
ma a,148,a,72,v,212
tr 60,3,0
ma a,152,a,76,v,208
tr 62,3,512
ma a,156,a,76,v,212
wl 12
tr 64,0,1024
ma a,160,a,80,v,208
tr 66,0,1536
ma a,164,a,80,v,212
tr 68,1,1024
ma a,168,a,84,v,208
)"
R"(tr 70,1,1536
ma a,172,a,84,v,212
wl 12
tr 72,2,1024
ma a,176,a,88,v,208
tr 74,2,1536
ma a,180,a,88,v,212
tr 76,3,1024
ma a,184,a,92,v,208
wl 11
tr 78,3,1536
ma a,188,a,92,v,212
s_waitcnt vmcnt(9)
s_barrier
ma v,48,a,48,v,200
s_add_u32 m0,s75,0x19000
tr 80,0,2048
ma v,52,a,48,v,204
tr 82,0,2560
rd v,208,17,14336
ma v,56,a,52,v,200
tr 84,1,2048
rd v,212,17,16384
ma v,60,a,52,v,204
wl 13
tr 86,1,2560
tr 88,2,2048
ma v,64,a,56,v,200
dl 232,24,72,0
ma v,68,a,56,v,204
tr 90,2,2560
tr 92,3,2048
ma v,72,a,60,v,200
dl 233,24,72,1024
ma v,76,a,60,v,204
wl 13
tr 94,3,2560
tr 48,0,12288
ma v,80,a,64,v,200
dl 252,24,72,2048
ma v,84,a,64,v,204
tr 50,0,12800
tr 52,1,12288
ma v,88,a,68,v,200
s_add_u32 s90,s72,0x180
tr 54,1,12800
ma v,92,a,68,v,204
s_add_u32 m0,s75,0x1c000
tr 56,2,12288
wl 15
ma v,96,a,72,v,200
dl 232,24,90,0
ma v,100,a,72,v,204
tr 58,2,12800
tr 60,3,12288
ma v,104,a,76,v,200
tr 62,3,12800
ma v,108,a,76,v,204
wl 12
tr 64,0,13312
ma v,112,a,80,v,200
dl 233,24,90,1024
ma v,116,a,80,v,204
tr 66,0,13824
tr 68,1,13312
ma v,120,a,84,v,200
tr 70,1,13824
ma v,124,a,84,v,204
wl 12
tr 72,2,13312
ma v,128,a,88,v,200
dl 252,24,90,2048
ma v,132,a,88,v,204
tr 74,2,13824
tr 76,3,13312
ma v,136,a,92,v,200
s_add_u32 s72,s72,s95
tr 78,3,13824
s_cmp_ge_u32 s72,s74
ma v,140,a,92,v,204
s_cselect_b32 s72,s73,s72
tr 80,0,14336
s_add_u32 m0,s82,s35
wl 13
ma a,96,a,48,v,200
dn 228,28,80,0
ma a,100,a,48,v,204
tr 82,0,14848
tr 84,1,14336
ma a,104,a,52,v,200
tr 86,1,14848
ma a,108,a,52,v,204
wl 12
tr 88,2,14336
ma a,112,a,56,v,200
dn 229,28,80,1024
ma a,116,a,56,v,204
tr 90,2,14848
tr 92,3,14336
ma a,120,a,60,v,200
tr 94,3,14848
ma a,124,a,60,v,204
wl 12
tr 48,0,24576
ma a,128,a,64,v,200
dn 230,28,80,2048
ma a,132,a,64,v,204
s_add_u32 s80,s80,s96
tr 50,0,25088
tr 52,1,24576
ma a,136,a,68,v,200
s_add_u32 s82,s82,s96
tr 54,1,25088
s_cmp_ge_u32 s82,s99
ma a,140,a,68,v,204
s_cselect_b32 s89,s97,0
tr 56,2,24576
s_sub_u32 s82,s82,s89
wl 13
ma a,144,a,72,v,200
tr 58,2,25088
ma a,148,a,72,v,204
tr 60,3,24576
ma a,152,a,76,v,200
tr 62,3,25088
ma a,156,a,76,v,204
wl 12
tr 64,0,25600
ma a,160,a,80,v,200
tr 66,0,26112
ma a,164,a,80,v,204
tr 68,1,25600
ma a,168,a,84,v,200
tr 70,1,26112
ma a,172,a,84,v,204
wl 12
tr 72,2,25600
ma a,176,a,88,v,200
tr 74,2,26112
ma a,180,a,88,v,204
tr 76,3,25600
ma a,184,a,92,v,200
wl 11
tr 78,3,26112
ma a,188,a,92,v,204
s_waitcnt vmcnt(9)
s_barrier
ma v,48,a,48,v,208
s_add_u32 m0,s75,0x7000
tr 80,0,26624
ma v,52,a,48,v,212
tr 82,0,27136
rd v,200,16,0
ma v,56,a,52,v,208
tr 84,1,26624
rd v,204,16,2048
ma v,60,a,52,v,212
wl 13
tr 86,1,27136
tr 88,2,26624
ma v,64,a,56,v,208
dl 232,24,72,0
ma v,68,a,56,v,212
tr 90,2,27136
tr 92,3,26624
ma v,72,a,60,v,208
dl 233,24,72,1024
ma v,76,a,60,v,212
wl 13
tr 94,3,27136
tr 48,0,36864
ma v,80,a,64,v,208
dl 252,24,72,2048
ma v,84,a,64,v,212
tr 50,0,37376
tr 52,1,36864
ma v,88,a,68,v,208
s_add_u32 s90,s72,0x180
tr 54,1,37376
ma v,92,a,68,v,212
s_add_u32 m0,s75,0xa000
tr 56,2,36864
wl 15
ma v,96,a,72,v,208
dl 232,24,90,0
ma v,100,a,72,v,212
tr 58,2,37376
tr 60,3,36864
ma v,104,a,76,v,208
tr 62,3,37376
ma v,108,a,76,v,212
wl 12
tr 64,0,37888
ma v,112,a,80,v,208
dl 233,24,90,1024
ma v,116,a,80,v,212
tr 66,0,38400
tr 68,1,37888
ma v,120,a,84,v,208
tr 70,1,38400
)"
R"(ma v,124,a,84,v,212
wl 12
tr 72,2,37888
ma v,128,a,88,v,208
dl 252,24,90,2048
ma v,132,a,88,v,212
tr 74,2,38400
tr 76,3,37888
ma v,136,a,92,v,208
s_add_u32 s72,s72,s95
tr 78,3,38400
s_add_u32 m0,s79,0x3800
ma v,140,a,92,v,212
wl 12
tr 80,0,38912
ma a,96,a,48,v,208
dl 235,20,76,0
ma a,100,a,48,v,212
tr 82,0,39424
tr 84,1,38912
ma a,104,a,52,v,208
tr 86,1,39424
ma a,108,a,52,v,212
wl 12
tr 88,2,38912
ma a,112,a,56,v,208
dl 236,20,76,1024
ma a,116,a,56,v,212
tr 90,2,39424
tr 92,3,38912
ma a,120,a,60,v,208
tr 94,3,39424
ma a,124,a,60,v,212
wl 12
tr 48,8,0
ma a,128,a,64,v,208
dl 237,20,76,2048
ma a,132,a,64,v,212
s_addk_i32 s76,128
tr 50,8,512
tr 52,9,0
ma a,136,a,68,v,208
s_cmp_ge_u32 s76,s78
tr 54,9,512
s_cselect_b32 s76,s77,s76
ma a,140,a,68,v,212
wl 12
tr 56,10,0
ma a,144,a,72,v,208
tr 58,10,512
ma a,148,a,72,v,212
tr 60,11,0
ma a,152,a,76,v,208
tr 62,11,512
ma a,156,a,76,v,212
wl 12
tr 64,8,1024
ma a,160,a,80,v,208
tr 66,8,1536
ma a,164,a,80,v,212
tr 68,9,1024
ma a,168,a,84,v,208
tr 70,9,1536
ma a,172,a,84,v,212
wl 12
tr 72,10,1024
ma a,176,a,88,v,208
tr 74,10,1536
ma a,180,a,88,v,212
tr 76,11,1024
ma a,184,a,92,v,208
wl 11
tr 78,11,1536
ma a,188,a,92,v,212
s_waitcnt vmcnt(12)
s_barrier
ma v,48,a,48,v,200
s_add_u32 m0,s75,0xd000
tr 80,8,2048
ma v,52,a,48,v,204
tr 82,8,2560
rd v,208,17,0
ma v,56,a,52,v,200
tr 84,9,2048
rd v,212,17,2048
ma v,60,a,52,v,204
wl 13
tr 86,9,2560
tr 88,10,2048
ma v,64,a,56,v,200
dl 232,24,72,0
ma v,68,a,56,v,204
tr 90,10,2560
tr 92,11,2048
ma v,72,a,60,v,200
dl 233,24,72,1024
ma v,76,a,60,v,204
wl 13
tr 94,11,2560
tr 48,8,12288
ma v,80,a,64,v,200
dl 252,24,72,2048
ma v,84,a,64,v,204
tr 50,8,12800
tr 52,9,12288
ma v,88,a,68,v,200
s_add_u32 s90,s72,0x180
tr 54,9,12800
ma v,92,a,68,v,204
s_add_u32 m0,s75,0x10000
tr 56,10,12288
wl 15
ma v,96,a,72,v,200
dl 232,24,90,0
ma v,100,a,72,v,204
tr 58,10,12800
tr 60,11,12288
ma v,104,a,76,v,200
tr 62,11,12800
ma v,108,a,76,v,204
wl 12
tr 64,8,13312
ma v,112,a,80,v,200
dl 233,24,90,1024
ma v,116,a,80,v,204
tr 66,8,13824
tr 68,9,13312
ma v,120,a,84,v,200
tr 70,9,13824
ma v,124,a,84,v,204
wl 12
tr 72,10,13312
ma v,128,a,88,v,200
dl 252,24,90,2048
ma v,132,a,88,v,204
tr 74,10,13824
tr 76,11,13312
ma v,136,a,92,v,200
s_add_u32 s72,s72,s95
tr 78,11,13824
s_cmp_ge_u32 s72,s74
ma v,140,a,92,v,204
s_cselect_b32 s72,s73,s72
tr 80,8,14336
s_add_u32 m0,s82,s35
wl 13
ma a,96,a,48,v,200
dn 228,28,80,0
ma a,100,a,48,v,204
tr 82,8,14848
tr 84,9,14336
ma a,104,a,52,v,200
tr 86,9,14848
ma a,108,a,52,v,204
wl 12
tr 88,10,14336
ma a,112,a,56,v,200
dn 229,28,80,1024
ma a,116,a,56,v,204
tr 90,10,14848
tr 92,11,14336
ma a,120,a,60,v,200
tr 94,11,14848
ma a,124,a,60,v,204
wl 12
tr 48,8,24576
ma a,128,a,64,v,200
dn 230,28,80,2048
ma a,132,a,64,v,204
s_add_u32 s80,s80,s96
tr 50,8,25088
tr 52,9,24576
ma a,136,a,68,v,200
s_add_u32 s82,s82,s96
tr 54,9,25088
s_cmp_ge_u32 s82,s99
ma a,140,a,68,v,204
s_cselect_b32 s89,s97,0
tr 56,10,24576
s_sub_u32 s82,s82,s89
wl 13
ma a,144,a,72,v,200
tr 58,10,25088
ma a,148,a,72,v,204
tr 60,11,24576
ma a,152,a,76,v,200
tr 62,11,25088
ma a,156,a,76,v,204
wl 12
tr 64,8,25600
ma a,160,a,80,v,200
tr 66,8,26112
ma a,164,a,80,v,204
tr 68,9,25600
ma a,168,a,84,v,200
tr 70,9,26112
ma a,172,a,84,v,204
wl 12
tr 72,10,25600
ma a,176,a,88,v,200
)"
R"(tr 74,10,26112
ma a,180,a,88,v,204
tr 76,11,25600
ma a,184,a,92,v,200
wl 11
tr 78,11,26112
ma a,188,a,92,v,204
s_waitcnt vmcnt(9)
s_barrier
ma v,48,a,48,v,208
s_add_u32 m0,s75,0x13000
tr 80,8,26624
ma v,52,a,48,v,212
tr 82,8,27136
rd v,200,16,14336
ma v,56,a,52,v,208
tr 84,9,26624
rd v,204,16,16384
ma v,60,a,52,v,212
wl 13
tr 86,9,27136
tr 88,10,26624
ma v,64,a,56,v,208
dl 232,24,72,0
ma v,68,a,56,v,212
tr 90,10,27136
tr 92,11,26624
ma v,72,a,60,v,208
dl 233,24,72,1024
ma v,76,a,60,v,212
wl 13
tr 94,11,27136
tr 48,8,36864
ma v,80,a,64,v,208
dl 252,24,72,2048
ma v,84,a,64,v,212
tr 50,8,37376
tr 52,9,36864
ma v,88,a,68,v,208
s_add_u32 s90,s72,0x180
tr 54,9,37376
ma v,92,a,68,v,212
s_add_u32 m0,s75,0x16000
tr 56,10,36864
wl 15
ma v,96,a,72,v,208
dl 232,24,90,0
ma v,100,a,72,v,212
tr 58,10,37376
tr 60,11,36864
ma v,104,a,76,v,208
tr 62,11,37376
ma v,108,a,76,v,212
wl 12
tr 64,8,37888
ma v,112,a,80,v,208
dl 233,24,90,1024
ma v,116,a,80,v,212
tr 66,8,38400
tr 68,9,37888
ma v,120,a,84,v,208
tr 70,9,38400
ma v,124,a,84,v,212
wl 12
tr 72,10,37888
ma v,128,a,88,v,208
dl 252,24,90,2048
ma v,132,a,88,v,212
tr 74,10,38400
tr 76,11,37888
ma v,136,a,92,v,208
s_add_u32 s72,s72,s95
tr 78,11,38400
s_mov_b32 m0,s79
ma v,140,a,92,v,212
wl 12
tr 80,8,38912
ma a,96,a,48,v,208
dl 235,20,76,0
ma a,100,a,48,v,212
tr 82,8,39424
tr 84,9,38912
ma a,104,a,52,v,208
tr 86,9,39424
ma a,108,a,52,v,212
wl 12
tr 88,10,38912
ma a,112,a,56,v,208
dl 236,20,76,1024
ma a,116,a,56,v,212
tr 90,10,39424
tr 92,11,38912
ma a,120,a,60,v,208
tr 94,11,39424
ma a,124,a,60,v,212
wl 12
tr 48,0,0
ma a,128,a,64,v,208
dl 237,20,76,2048
ma a,132,a,64,v,212
s_addk_i32 s76,128
tr 50,0,512
tr 52,1,0
ma a,136,a,68,v,208
s_cmp_ge_u32 s76,s78
tr 54,1,512
s_cselect_b32 s76,s77,s76
ma a,140,a,68,v,212
wl 12
tr 56,2,0
ma a,144,a,72,v,208
tr 58,2,512
ma a,148,a,72,v,212
tr 60,3,0
ma a,152,a,76,v,208
tr 62,3,512
ma a,156,a,76,v,212
wl 12
tr 64,0,1024
ma a,160,a,80,v,208
tr 66,0,1536
ma a,164,a,80,v,212
tr 68,1,1024
ma a,168,a,84,v,208
tr 70,1,1536
ma a,172,a,84,v,212
wl 12
tr 72,2,1024
ma a,176,a,88,v,208
tr 74,2,1536
ma a,180,a,88,v,212
tr 76,3,1024
ma a,184,a,92,v,208
wl 11
tr 78,3,1536
ma a,188,a,92,v,212
s_waitcnt vmcnt(12)
s_barrier
ma v,48,a,48,v,200
s_add_u32 m0,s75,0x19000
tr 80,0,2048
ma v,52,a,48,v,204
tr 82,0,2560
rd v,208,17,14336
ma v,56,a,52,v,200
tr 84,1,2048
rd v,212,17,16384
ma v,60,a,52,v,204
wl 13
tr 86,1,2560
tr 88,2,2048
ma v,64,a,56,v,200
dl 232,24,72,0
ma v,68,a,56,v,204
tr 90,2,2560
tr 92,3,2048
ma v,72,a,60,v,200
dl 233,24,72,1024
ma v,76,a,60,v,204
wl 13
tr 94,3,2560
tr 48,0,12288
ma v,80,a,64,v,200
dl 252,24,72,2048
ma v,84,a,64,v,204
tr 50,0,12800
tr 52,1,12288
ma v,88,a,68,v,200
s_add_u32 s90,s72,0x180
tr 54,1,12800
ma v,92,a,68,v,204
s_add_u32 m0,s75,0x1c000
tr 56,2,12288
wl 15
ma v,96,a,72,v,200
dl 232,24,90,0
ma v,100,a,72,v,204
tr 58,2,12800
tr 60,3,12288
ma v,104,a,76,v,200
tr 62,3,12800
ma v,108,a,76,v,204
wl 12
tr 64,0,13312
ma v,112,a,80,v,200
dl 233,24,90,1024
ma v,116,a,80,v,204
tr 66,0,13824
tr 68,1,13312
ma v,120,a,84,v,200
tr 70,1,13824
ma v,124,a,84,v,204
wl 12
tr 72,2,13312
ma v,128,a,88,v,200
dl 252,24,90,2048
ma v,132,a,88,v,204
tr 74,2,13824
tr 76,3,13312
ma v,136,a,92,v,200
)"
R"(s_add_u32 s72,s72,s95
tr 78,3,13824
s_cmp_ge_u32 s72,s74
ma v,140,a,92,v,204
s_cselect_b32 s72,s73,s72
tr 80,0,14336
s_add_u32 m0,s82,s35
wl 13
ma a,96,a,48,v,200
dn 228,28,80,0
ma a,100,a,48,v,204
tr 82,0,14848
tr 84,1,14336
ma a,104,a,52,v,200
tr 86,1,14848
ma a,108,a,52,v,204
wl 12
tr 88,2,14336
ma a,112,a,56,v,200
dn 229,28,80,1024
ma a,116,a,56,v,204
tr 90,2,14848
tr 92,3,14336
ma a,120,a,60,v,200
tr 94,3,14848
ma a,124,a,60,v,204
wl 12
tr 48,0,24576
ma a,128,a,64,v,200
dn 230,28,80,2048
ma a,132,a,64,v,204
s_add_u32 s80,s80,s96
tr 50,0,25088
tr 52,1,24576
ma a,136,a,68,v,200
s_add_u32 s82,s82,s96
tr 54,1,25088
s_cmp_ge_u32 s82,s99
ma a,140,a,68,v,204
s_cselect_b32 s89,s97,0
tr 56,2,24576
s_sub_u32 s82,s82,s89
wl 13
ma a,144,a,72,v,200
tr 58,2,25088
ma a,148,a,72,v,204
tr 60,3,24576
ma a,152,a,76,v,200
tr 62,3,25088
ma a,156,a,76,v,204
wl 12
tr 64,0,25600
ma a,160,a,80,v,200
tr 66,0,26112
ma a,164,a,80,v,204
tr 68,1,25600
ma a,168,a,84,v,200
tr 70,1,26112
ma a,172,a,84,v,204
wl 12
tr 72,2,25600
ma a,176,a,88,v,200
tr 74,2,26112
ma a,180,a,88,v,204
tr 76,3,25600
ma a,184,a,92,v,200
wl 11
tr 78,3,26112
ma a,188,a,92,v,204
s_waitcnt vmcnt(9)
s_barrier
ma v,48,a,48,v,208
s_add_u32 m0,s75,0x7000
tr 80,0,26624
ma v,52,a,48,v,212
tr 82,0,27136
rd v,200,16,0
ma v,56,a,52,v,208
tr 84,1,26624
rd v,204,16,2048
ma v,60,a,52,v,212
wl 13
tr 86,1,27136
tr 88,2,26624
ma v,64,a,56,v,208
dl 232,24,72,0
ma v,68,a,56,v,212
tr 90,2,27136
tr 92,3,26624
ma v,72,a,60,v,208
dl 233,24,72,1024
ma v,76,a,60,v,212
wl 13
tr 94,3,27136
tr 48,0,36864
ma v,80,a,64,v,208
dl 252,24,72,2048
ma v,84,a,64,v,212
tr 50,0,37376
tr 52,1,36864
ma v,88,a,68,v,208
s_add_u32 s90,s72,0x180
tr 54,1,37376
ma v,92,a,68,v,212
s_add_u32 m0,s75,0xa000
tr 56,2,36864
wl 15
ma v,96,a,72,v,208
dl 232,24,90,0
ma v,100,a,72,v,212
tr 58,2,37376
tr 60,3,36864
ma v,104,a,76,v,208
tr 62,3,37376
ma v,108,a,76,v,212
wl 12
tr 64,0,37888
ma v,112,a,80,v,208
dl 233,24,90,1024
ma v,116,a,80,v,212
tr 66,0,38400
tr 68,1,37888
ma v,120,a,84,v,208
tr 70,1,38400
ma v,124,a,84,v,212
wl 12
tr 72,2,37888
ma v,128,a,88,v,208
dl 252,24,90,2048
ma v,132,a,88,v,212
tr 74,2,38400
tr 76,3,37888
ma v,136,a,92,v,208
s_add_u32 s72,s72,s95
tr 78,3,38400
s_add_u32 m0,s79,0x3800
ma v,140,a,92,v,212
wl 12
tr 80,0,38912
ma a,96,a,48,v,208
dl 235,20,76,0
ma a,100,a,48,v,212
tr 82,0,39424
tr 84,1,38912
ma a,104,a,52,v,208
tr 86,1,39424
ma a,108,a,52,v,212
wl 12
tr 88,2,38912
ma a,112,a,56,v,208
dl 236,20,76,1024
ma a,116,a,56,v,212
tr 90,2,39424
tr 92,3,38912
ma a,120,a,60,v,208
tr 94,3,39424
ma a,124,a,60,v,212
wl 12
tr 48,8,0
ma a,128,a,64,v,208
dl 237,20,76,2048
ma a,132,a,64,v,212
s_addk_i32 s76,128
tr 50,8,512
tr 52,9,0
ma a,136,a,68,v,208
s_cmp_ge_u32 s76,s78
tr 54,9,512
s_cselect_b32 s76,s77,s76
ma a,140,a,68,v,212
wl 12
tr 56,10,0
ma a,144,a,72,v,208
tr 58,10,512
ma a,148,a,72,v,212
tr 60,11,0
ma a,152,a,76,v,208
tr 62,11,512
ma a,156,a,76,v,212
wl 12
tr 64,8,1024
ma a,160,a,80,v,208
tr 66,8,1536
ma a,164,a,80,v,212
tr 68,9,1024
ma a,168,a,84,v,208
tr 70,9,1536
ma a,172,a,84,v,212
wl 12
tr 72,10,1024
ma a,176,a,88,v,208
tr 74,10,1536
ma a,180,a,88,v,212
tr 76,11,1024
ma a,184,a,92,v,208
wl 11
tr 78,11,1536
ma a,188,a,92,v,212
s_waitcnt vmcnt(12)
s_barrier
)"
R"(ma v,48,a,48,v,200
s_add_u32 m0,s75,0xd000
tr 80,8,2048
ma v,52,a,48,v,204
tr 82,8,2560
rd v,208,17,0
ma v,56,a,52,v,200
tr 84,9,2048
rd v,212,17,2048
ma v,60,a,52,v,204
wl 13
tr 86,9,2560
tr 88,10,2048
ma v,64,a,56,v,200
dl 232,24,72,0
ma v,68,a,56,v,204
tr 90,10,2560
tr 92,11,2048
ma v,72,a,60,v,200
dl 233,24,72,1024
ma v,76,a,60,v,204
wl 13
tr 94,11,2560
tr 48,8,12288
ma v,80,a,64,v,200
tr 50,8,12800
ma v,84,a,64,v,204
tr 52,9,12288
ma v,88,a,68,v,200
tr 54,9,12800
ma v,92,a,68,v,204
wl 14
tr 56,10,12288
ma v,96,a,72,v,200
dl 252,24,72,2048
ma v,100,a,72,v,204
tr 58,10,12800
tr 60,11,12288
ma v,104,a,76,v,200
tr 62,11,12800
ma v,108,a,76,v,204
wl 12
tr 64,8,13312
ma v,112,a,80,v,200
s_add_u32 s90,s72,0x180
tr 66,8,13824
ma v,116,a,80,v,204
s_add_u32 m0,s75,0x10000
tr 68,9,13312
ma v,120,a,84,v,200
dl 232,24,90,0
ma v,124,a,84,v,204
wl 11
tr 70,9,13824
tr 72,10,13312
ma v,128,a,88,v,200
tr 74,10,13824
ma v,132,a,88,v,204
tr 76,11,13312
ma v,136,a,92,v,200
tr 78,11,13824
ma v,140,a,92,v,204
wl 12
tr 80,8,14336
ma a,96,a,48,v,200
dl 233,24,90,1024
ma a,100,a,48,v,204
tr 82,8,14848
tr 84,9,14336
ma a,104,a,52,v,200
tr 86,9,14848
ma a,108,a,52,v,204
wl 12
tr 88,10,14336
ma a,112,a,56,v,200
tr 90,10,14848
ma a,116,a,56,v,204
tr 92,11,14336
ma a,120,a,60,v,200
dl 252,24,90,2048
ma a,124,a,60,v,204
s_add_u32 s72,s72,s95
tr 94,11,14848
tr 48,8,24576
wl 13
ma a,128,a,64,v,200
s_cmp_ge_u32 s72,s74
tr 50,8,25088
s_cselect_b32 s72,s73,s72
ma a,132,a,64,v,204
tr 52,9,24576
ma a,136,a,68,v,200
tr 54,9,25088
ma a,140,a,68,v,204
wl 12
tr 56,10,24576
ma a,144,a,72,v,200
tr 58,10,25088
ma a,148,a,72,v,204
tr 60,11,24576
ma a,152,a,76,v,200
tr 62,11,25088
ma a,156,a,76,v,204
wl 12
tr 64,8,25600
ma a,160,a,80,v,200
tr 66,8,26112
ma a,164,a,80,v,204
tr 68,9,25600
ma a,168,a,84,v,200
tr 70,9,26112
ma a,172,a,84,v,204
wl 12
tr 72,10,25600
ma a,176,a,88,v,200
tr 74,10,26112
ma a,180,a,88,v,204
tr 76,11,25600
ma a,184,a,92,v,200
wl 11
tr 78,11,26112
ma a,188,a,92,v,204
s_waitcnt vmcnt(6)
s_barrier
ma v,48,a,48,v,208
s_add_u32 m0,s84,s35
tr 80,8,26624
tr 82,8,27136
ma v,52,a,48,v,212
rd v,200,16,14336
tr 84,9,26624
ma v,56,a,52,v,208
rd v,204,16,16384
tr 86,9,27136
ma v,60,a,52,v,212
wl 14
dn 231,36,83,0
ma v,64,a,56,v,208
tr 88,10,26624
tr 90,10,27136
ma v,68,a,56,v,212
tr 92,11,26624
ma v,72,a,60,v,208
dn 246,36,83,1024
ma v,76,a,60,v,212
wl 13
tr 94,11,27136
tr 48,8,36864
ma v,80,a,64,v,208
tr 50,8,37376
ma v,84,a,64,v,212
tr 52,9,36864
ma v,88,a,68,v,208
tr 54,9,37376
ma v,92,a,68,v,212
wl 14
tr 56,10,36864
ma v,96,a,72,v,208
dn 247,36,83,2048
ma v,100,a,72,v,212
tr 58,10,37376
tr 60,11,36864
ma v,104,a,76,v,208
tr 62,11,37376
ma v,108,a,76,v,212
wl 12
tr 64,8,37888
ma v,112,a,80,v,208
s_add_u32 s83,s83,s96
tr 66,8,38400
s_add_u32 s84,s84,s96
ma v,116,a,80,v,212
s_cmp_ge_u32 s84,s34
tr 68,9,37888
s_cselect_b32 s89,s97,0
ma v,120,a,84,v,208
s_sub_u32 s84,s84,s89
tr 70,9,38400
s_add_u32 m0,s84,s35
ma v,124,a,84,v,212
wl 12
dn 231,36,83,0
ma v,128,a,88,v,208
tr 72,10,37888
tr 74,10,38400
ma v,132,a,88,v,212
tr 76,11,37888
ma v,136,a,92,v,208
tr 78,11,38400
ma v,140,a,92,v,212
wl 12
tr 80,8,38912
ma a,96,a,48,v,208
dn 246,36,83,1024
ma a,100,a,48,v,212
tr 82,8,39424
tr 84,9,38912
ma a,104,a,52,v,208
tr 86,9,39424
ma a,108,a,52,v,212
)"
R"(wl 12
tr 88,10,38912
ma a,112,a,56,v,208
tr 90,10,39424
ma a,116,a,56,v,212
tr 92,11,38912
ma a,120,a,60,v,208
dn 247,36,83,2048
ma a,124,a,60,v,212
s_add_u32 s83,s83,s96
tr 94,11,39424
tr 48,0,0
wl 13
ma a,128,a,64,v,208
s_add_u32 s84,s84,s96
tr 50,0,512
s_cmp_ge_u32 s84,s34
ma a,132,a,64,v,212
s_cselect_b32 s89,s97,0
tr 52,1,0
s_sub_u32 s84,s84,s89
ma a,136,a,68,v,208
tr 54,1,512
ma a,140,a,68,v,212
wl 12
tr 56,2,0
ma a,144,a,72,v,208
tr 58,2,512
ma a,148,a,72,v,212
tr 60,3,0
ma a,152,a,76,v,208
tr 62,3,512
ma a,156,a,76,v,212
wl 12
tr 64,0,1024
ma a,160,a,80,v,208
tr 66,0,1536
ma a,164,a,80,v,212
tr 68,1,1024
ma a,168,a,84,v,208
tr 70,1,1536
ma a,172,a,84,v,212
wl 12
tr 72,2,1024
ma a,176,a,88,v,208
tr 74,2,1536
ma a,180,a,88,v,212
tr 76,3,1024
ma a,184,a,92,v,208
wl 11
tr 78,3,1536
ma a,188,a,92,v,212
s_waitcnt vmcnt(6)
s_barrier
ma v,48,a,48,v,200
s_add_u32 m0,s82,s35
tr 80,0,2048
tr 82,0,2560
ma v,52,a,48,v,204
rd v,208,17,14336
tr 84,1,2048
ma v,56,a,52,v,200
rd v,212,17,16384
tr 86,1,2560
ma v,60,a,52,v,204
wl 14
dn 228,28,80,0
ma v,64,a,56,v,200
tr 88,2,2048
tr 90,2,2560
ma v,68,a,56,v,204
tr 92,3,2048
ma v,72,a,60,v,200
tr 94,3,2560
ma v,76,a,60,v,204
wl 14
tr 48,0,12288
ma v,80,a,64,v,200
tr 50,0,12800
ma v,84,a,64,v,204
tr 52,1,12288
ma v,88,a,68,v,200
tr 54,1,12800
ma v,92,a,68,v,204
wl 14
tr 56,2,12288
ma v,96,a,72,v,200
dn 229,28,80,1024
ma v,100,a,72,v,204
tr 58,2,12800
tr 60,3,12288
ma v,104,a,76,v,200
tr 62,3,12800
ma v,108,a,76,v,204
wl 12
tr 64,0,13312
ma v,112,a,80,v,200
tr 66,0,13824
ma v,116,a,80,v,204
tr 68,1,13312
ma v,120,a,84,v,200
tr 70,1,13824
ma v,124,a,84,v,204
wl 12
tr 72,2,13312
ma v,128,a,88,v,200
tr 74,2,13824
ma v,132,a,88,v,204
tr 76,3,13312
ma v,136,a,92,v,200
tr 78,3,13824
ma v,140,a,92,v,204
wl 12
tr 80,0,14336
ma a,96,a,48,v,200
dn 230,28,80,2048
ma a,100,a,48,v,204
s_add_u32 s80,s80,s96
tr 82,0,14848
tr 84,1,14336
ma a,104,a,52,v,200
s_add_u32 s82,s82,s96
tr 86,1,14848
s_cmp_ge_u32 s82,s99
ma a,108,a,52,v,204
s_cselect_b32 s89,s97,0
tr 88,2,14336
s_sub_u32 s82,s82,s89
wl 13
ma a,112,a,56,v,200
tr 90,2,14848
ma a,116,a,56,v,204
tr 92,3,14336
ma a,120,a,60,v,200
tr 94,3,14848
ma a,124,a,60,v,204
wl 12
tr 48,0,24576
ma a,128,a,64,v,200
tr 50,0,25088
ma a,132,a,64,v,204
tr 52,1,24576
ma a,136,a,68,v,200
tr 54,1,25088
ma a,140,a,68,v,204
wl 12
tr 56,2,24576
ma a,144,a,72,v,200
tr 58,2,25088
ma a,148,a,72,v,204
tr 60,3,24576
ma a,152,a,76,v,200
tr 62,3,25088
ma a,156,a,76,v,204
wl 12
tr 64,0,25600
ma a,160,a,80,v,200
tr 66,0,26112
ma a,164,a,80,v,204
tr 68,1,25600
ma a,168,a,84,v,200
tr 70,1,26112
ma a,172,a,84,v,204
wl 12
tr 72,2,25600
ma a,176,a,88,v,200
tr 74,2,26112
ma a,180,a,88,v,204
tr 76,3,25600
ma a,184,a,92,v,200
wl 11
tr 78,3,26112
ma a,188,a,92,v,204
ma v,48,a,48,v,208
s_add_u32 m0,s84,s35
tr 80,0,26624
tr 82,0,27136
ma v,52,a,48,v,212
dn 231,36,83,0
ma v,56,a,52,v,208
tr 84,1,26624
tr 86,1,27136
ma v,60,a,52,v,212
wl 12
tr 88,2,26624
ma v,64,a,56,v,208
tr 90,2,27136
ma v,68,a,56,v,212
tr 92,3,26624
ma v,72,a,60,v,208
tr 94,3,27136
ma v,76,a,60,v,212
wl 12
tr 48,0,36864
ma v,80,a,64,v,208
tr 50,0,37376
ma v,84,a,64,v,212
tr 52,1,36864
ma v,88,a,68,v,208
tr 54,1,37376
)"
R"(ma v,92,a,68,v,212
wl 12
tr 56,2,36864
ma v,96,a,72,v,208
dn 246,36,83,1024
ma v,100,a,72,v,212
tr 58,2,37376
tr 60,3,36864
ma v,104,a,76,v,208
tr 62,3,37376
ma v,108,a,76,v,212
wl 12
tr 64,0,37888
ma v,112,a,80,v,208
tr 66,0,38400
ma v,116,a,80,v,212
tr 68,1,37888
ma v,120,a,84,v,208
tr 70,1,38400
ma v,124,a,84,v,212
wl 12
tr 72,2,37888
ma v,128,a,88,v,208
tr 74,2,38400
ma v,132,a,88,v,212
tr 76,3,37888
ma v,136,a,92,v,208
tr 78,3,38400
ma v,140,a,92,v,212
wl 12
tr 80,0,38912
ma a,96,a,48,v,208
dn 247,36,83,2048
ma a,100,a,48,v,212
s_add_u32 s83,s83,s96
tr 82,0,39424
tr 84,1,38912
ma a,104,a,52,v,208
s_add_u32 s84,s84,s96
tr 86,1,39424
s_cmp_ge_u32 s84,s34
ma a,108,a,52,v,212
s_cselect_b32 s89,s97,0
tr 88,2,38912
s_sub_u32 s84,s84,s89
wl 13
ma a,112,a,56,v,208
tr 90,2,39424
ma a,116,a,56,v,212
tr 92,3,38912
ma a,120,a,60,v,208
tr 94,3,39424
ma a,124,a,60,v,212
wl 12
ma a,128,a,64,v,208
ma a,132,a,64,v,212
ma a,136,a,68,v,208
ma a,140,a,68,v,212
wl 8
ma a,144,a,72,v,208
ma a,148,a,72,v,212
ma a,152,a,76,v,208
ma a,156,a,76,v,212
wl 4
ma a,160,a,80,v,208
ma a,164,a,80,v,212
ma a,168,a,84,v,208
ma a,172,a,84,v,212
wl 0
ma a,176,a,88,v,208
ma a,180,a,88,v,212
ma a,184,a,92,v,208
ma a,188,a,92,v,212
s_waitcnt vmcnt(45)
s_barrier
rd a,0,216,0
rd a,4,216,6144
rd a,8,217,0
rd a,12,217,6144
rd a,16,216,128
rd a,20,216,6272
rd a,24,217,128
rd a,28,217,6272
rd a,32,216,256
rd a,36,216,6400
rd a,40,217,256
rd a,44,217,6400
s_nop 7
v_pk_mul_f32 v[48:49],v[48:49],s[58:59] op_sel_hi:[1,0]
v_pk_mul_f32 v[50:51],v[50:51],s[58:59] op_sel_hi:[1,0]
v_pk_mul_f32 v[56:57],v[56:57],s[58:59] op_sel_hi:[1,0]
v_pk_mul_f32 v[58:59],v[58:59],s[58:59] op_sel_hi:[1,0]
cv 152,48,49
cv 153,50,51
cv 154,56,57
cv 155,58,59
v_pk_mul_f32 v[52:53],v[52:53],s[58:59] op_sel_hi:[1,0]
v_pk_mul_f32 v[54:55],v[54:55],s[58:59] op_sel_hi:[1,0]
v_pk_mul_f32 v[60:61],v[60:61],s[58:59] op_sel_hi:[1,0]
v_pk_mul_f32 v[62:63],v[62:63],s[58:59] op_sel_hi:[1,0]
cv 156,52,53
cv 157,54,55
cv 158,60,61
cv 159,62,63
v_pk_mul_f32 v[64:65],v[64:65],s[58:59] op_sel_hi:[1,0]
v_pk_mul_f32 v[66:67],v[66:67],s[58:59] op_sel_hi:[1,0]
v_pk_mul_f32 v[72:73],v[72:73],s[58:59] op_sel_hi:[1,0]
v_pk_mul_f32 v[74:75],v[74:75],s[58:59] op_sel_hi:[1,0]
cv 160,64,65
cv 161,66,67
cv 162,72,73
cv 163,74,75
v_pk_mul_f32 v[68:69],v[68:69],s[58:59] op_sel_hi:[1,0]
v_pk_mul_f32 v[70:71],v[70:71],s[58:59] op_sel_hi:[1,0]
v_pk_mul_f32 v[76:77],v[76:77],s[58:59] op_sel_hi:[1,0]
v_pk_mul_f32 v[78:79],v[78:79],s[58:59] op_sel_hi:[1,0]
cv 164,68,69
cv 165,70,71
cv 166,76,77
cv 167,78,79
wl 0
s_barrier
v_pk_mul_f32 v[80:81],v[80:81],s[58:59] op_sel_hi:[1,0]
v_pk_mul_f32 v[82:83],v[82:83],s[58:59] op_sel_hi:[1,0]
v_pk_mul_f32 v[88:89],v[88:89],s[58:59] op_sel_hi:[1,0]
v_pk_mul_f32 v[90:91],v[90:91],s[58:59] op_sel_hi:[1,0]
cv 168,80,81
cv 169,82,83
cv 170,88,89
cv 171,90,91
v_pk_mul_f32 v[84:85],v[84:85],s[58:59] op_sel_hi:[1,0]
v_pk_mul_f32 v[86:87],v[86:87],s[58:59] op_sel_hi:[1,0]
v_pk_mul_f32 v[92:93],v[92:93],s[58:59] op_sel_hi:[1,0]
v_pk_mul_f32 v[94:95],v[94:95],s[58:59] op_sel_hi:[1,0]
cv 172,84,85
cv 173,86,87
cv 174,92,93
cv 175,94,95
v_pk_mul_f32 v[96:97],v[96:97],s[58:59] op_sel_hi:[1,0]
v_pk_mul_f32 v[98:99],v[98:99],s[58:59] op_sel_hi:[1,0]
v_pk_mul_f32 v[104:105],v[104:105],s[58:59] op_sel_hi:[1,0]
v_pk_mul_f32 v[106:107],v[106:107],s[58:59] op_sel_hi:[1,0]
cv 176,96,97
cv 177,98,99
cv 178,104,105
cv 179,106,107
v_pk_mul_f32 v[100:101],v[100:101],s[58:59] op_sel_hi:[1,0]
v_pk_mul_f32 v[102:103],v[102:103],s[58:59] op_sel_hi:[1,0]
v_pk_mul_f32 v[108:109],v[108:109],s[58:59] op_sel_hi:[1,0]
v_pk_mul_f32 v[110:111],v[110:111],s[58:59] op_sel_hi:[1,0]
cv 180,100,101
cv 181,102,103
cv 182,108,109
cv 183,110,111
v_pk_mul_f32 v[112:113],v[112:113],s[58:59] op_sel_hi:[1,0]
v_pk_mul_f32 v[114:115],v[114:115],s[58:59] op_sel_hi:[1,0]
v_pk_mul_f32 v[120:121],v[120:121],s[58:59] op_sel_hi:[1,0]
v_pk_mul_f32 v[122:123],v[122:123],s[58:59] op_sel_hi:[1,0]
cv 184,112,113
cv 185,114,115
cv 186,120,121
cv 187,122,123
v_pk_mul_f32 v[116:117],v[116:117],s[58:59] op_sel_hi:[1,0]
v_pk_mul_f32 v[118:119],v[118:119],s[58:59] op_sel_hi:[1,0]
v_pk_mul_f32 v[124:125],v[124:125],s[58:59] op_sel_hi:[1,0]
v_pk_mul_f32 v[126:127],v[126:127],s[58:59] op_sel_hi:[1,0]
cv 188,116,117
cv 189,118,119
cv 190,124,125
cv 191,126,127
v_pk_mul_f32 v[128:129],v[128:129],s[58:59] op_sel_hi:[1,0]
v_pk_mul_f32 v[130:131],v[130:131],s[58:59] op_sel_hi:[1,0]
v_pk_mul_f32 v[136:137],v[136:137],s[58:59] op_sel_hi:[1,0]
v_pk_mul_f32 v[138:139],v[138:139],s[58:59] op_sel_hi:[1,0]
cv 192,128,129
cv 193,130,131
cv 194,136,137
cv 195,138,139
v_pk_mul_f32 v[132:133],v[132:133],s[58:59] op_sel_hi:[1,0]
v_pk_mul_f32 v[134:135],v[134:135],s[58:59] op_sel_hi:[1,0]
v_pk_mul_f32 v[140:141],v[140:141],s[58:59] op_sel_hi:[1,0]
v_pk_mul_f32 v[142:143],v[142:143],s[58:59] op_sel_hi:[1,0]
cv 196,132,133
cv 197,134,135
cv 198,140,141
cv 199,142,143
v_permlane32_swap_b32 v152,v154
v_permlane32_swap_b32 v153,v155
v_permlane32_swap_b32 v156,v158
v_permlane32_swap_b32 v157,v159
v_permlane16_swap_b32 v152,v154
v_permlane16_swap_b32 v153,v155
v_permlane16_swap_b32 v156,v158
v_permlane16_swap_b32 v157,v159
mz v,0,a,0,v,152
v_permlane32_swap_b32 v160,v162
v_permlane32_swap_b32 v161,v163
mz v,4,a,0,v,156
v_permlane32_swap_b32 v164,v166
v_permlane32_swap_b32 v165,v167
)"
R"(mz v,8,a,4,v,152
v_permlane16_swap_b32 v160,v162
v_permlane16_swap_b32 v161,v163
mz v,12,a,4,v,156
v_permlane16_swap_b32 v164,v166
v_permlane16_swap_b32 v165,v167
ma v,0,a,8,v,160
v_permlane32_swap_b32 v168,v170
v_permlane32_swap_b32 v169,v171
ma v,4,a,8,v,164
v_permlane32_swap_b32 v172,v174
v_permlane32_swap_b32 v173,v175
ma v,8,a,12,v,160
v_permlane16_swap_b32 v168,v170
v_permlane16_swap_b32 v169,v171
ma v,12,a,12,v,164
v_permlane16_swap_b32 v172,v174
v_permlane16_swap_b32 v173,v175
ma v,0,a,16,v,168
v_permlane32_swap_b32 v176,v178
v_permlane32_swap_b32 v177,v179
ma v,4,a,16,v,172
v_permlane32_swap_b32 v180,v182
v_permlane32_swap_b32 v181,v183
ma v,8,a,20,v,168
v_permlane16_swap_b32 v176,v178
v_permlane16_swap_b32 v177,v179
ma v,12,a,20,v,172
v_permlane16_swap_b32 v180,v182
v_permlane16_swap_b32 v181,v183
ma v,0,a,24,v,176
v_permlane32_swap_b32 v184,v186
v_permlane32_swap_b32 v185,v187
ma v,4,a,24,v,180
v_permlane32_swap_b32 v188,v190
v_permlane32_swap_b32 v189,v191
ma v,8,a,28,v,176
v_permlane16_swap_b32 v184,v186
v_permlane16_swap_b32 v185,v187
ma v,12,a,28,v,180
v_permlane16_swap_b32 v188,v190
v_permlane16_swap_b32 v189,v191
ma v,0,a,32,v,184
v_permlane32_swap_b32 v192,v194
v_permlane32_swap_b32 v193,v195
ma v,4,a,32,v,188
v_permlane32_swap_b32 v196,v198
v_permlane32_swap_b32 v197,v199
ma v,8,a,36,v,184
v_permlane16_swap_b32 v192,v194
v_permlane16_swap_b32 v193,v195
ma v,12,a,36,v,188
v_permlane16_swap_b32 v196,v198
v_permlane16_swap_b32 v197,v199
ma v,0,a,40,v,192
s_nop 0
ma v,4,a,40,v,196
ma v,8,a,44,v,192
ma v,12,a,44,v,196
v_readfirstlane_b32 s56,v250
v_readfirstlane_b32 s57,v251
s_add_u32 s56,s56,1
s_addc_u32 s57,s57,0
s_add_u32 s85,s85,s96
s_cmp_ge_u32 s85,s97
s_cselect_b32 s93,s98,s96
s_cselect_b32 s85,0,s85
v_add_u32 v216,s93,v216
v_add_u32 v217,s93,v217
rd a,0,216,0
rd a,4,216,6144
rd a,8,217,0
rd a,12,217,6144
rd a,16,216,128
rd a,20,216,6272
rd a,24,217,128
rd a,28,217,6272
rd a,32,216,256
rd a,36,216,6400
rd a,40,217,256
rd a,44,217,6400
v_max3_f32 v240,v0,v1,v2
v_max3_f32 v241,v4,v5,v6
v_max3_f32 v240,v240,v3,v8
v_max3_f32 v241,v241,v7,v12
v_max3_f32 v240,v240,v9,v10
v_max3_f32 v241,v241,v13,v14
v_max_f32 v240,v240,v11
v_max_f32 v241,v241,v15
v_mov_b32 v242,v240
v_mov_b32 v244,v241
s_nop 0
v_permlane32_swap_b32 v240,v242
v_permlane32_swap_b32 v241,v244
v_max_f32 v240,v240,v242
v_max_f32 v241,v241,v244
v_mov_b32 v242,v240
v_mov_b32 v244,v241
s_nop 0
v_permlane16_swap_b32 v240,v242
v_permlane16_swap_b32 v241,v244
v_max_f32 v240,v240,v242
v_max_f32 v241,v241,v244
v_sub_f32 v32,0,v240
v_sub_f32 v33,0,v240
v_sub_f32 v34,0,v240
v_sub_f32 v35,0,v240
v_sub_f32 v0,v0,v240
v_sub_f32 v1,v1,v240
v_sub_f32 v2,v2,v240
v_sub_f32 v3,v3,v240
v_sub_f32 v8,v8,v240
v_sub_f32 v9,v9,v240
v_sub_f32 v10,v10,v240
v_sub_f32 v11,v11,v240
v_sub_f32 v36,0,v241
v_sub_f32 v37,0,v241
v_sub_f32 v38,0,v241
v_sub_f32 v39,0,v241
v_sub_f32 v4,v4,v241
v_sub_f32 v5,v5,v241
v_sub_f32 v6,v6,v241
v_sub_f32 v7,v7,v241
v_sub_f32 v12,v12,v241
v_sub_f32 v13,v13,v241
v_sub_f32 v14,v14,v241
v_sub_f32 v15,v15,v241
s_waitcnt vmcnt(9) lgkmcnt(0)
s_barrier
mc v,16,a,0,v,152,32
s_add_u32 s85,s85,s96
v_exp_f32 v0,v0
mc v,20,a,0,v,156,36
s_cmp_ge_u32 s85,s97
tr 48,218,0
tr 50,218,6144
mc v,24,a,4,v,152,32
s_cselect_b32 s93,s98,s96
tr 52,219,0
tr 54,219,6144
mc v,28,a,4,v,156,36
s_cselect_b32 s85,0,s85
v_exp_f32 v1,v1
ma v,16,a,8,v,160
tr 56,220,0
cv 40,0,1
ma v,20,a,8,v,164
tr 58,220,6144
v_add_u32 v216,s93,v216
ma v,24,a,12,v,160
s_add_u32 s86,s86,s96
v_exp_f32 v2,v2
ma v,28,a,12,v,164
s_cmp_ge_u32 s86,s97
tr 60,221,0
tr 62,221,6144
ma v,16,a,16,v,168
s_cselect_b32 s94,s98,s96
tr 64,218,128
tr 66,218,6272
ma v,20,a,16,v,172
s_cselect_b32 s86,0,s86
v_exp_f32 v3,v3
ma v,24,a,20,v,168
tr 68,219,128
cv 41,2,3
ma v,28,a,20,v,172
tr 70,219,6272
v_add_u32 v217,s93,v217
ma v,16,a,24,v,176
v_exp_f32 v8,v8
ma v,20,a,24,v,180
tr 72,220,128
tr 74,220,6272
ma v,24,a,28,v,176
tr 76,221,128
tr 78,221,6272
ma v,28,a,28,v,180
v_exp_f32 v9,v9
ma v,16,a,32,v,184
tr 80,218,256
cv 42,8,9
ma v,20,a,32,v,188
tr 82,218,6400
ma v,24,a,36,v,184
v_exp_f32 v10,v10
ma v,28,a,36,v,188
tr 84,219,256
tr 86,219,6400
ma v,16,a,40,v,192
tr 88,220,256
tr 90,220,6400
ma v,20,a,40,v,196
v_exp_f32 v11,v11
ma v,24,a,44,v,192
tr 92,221,256
cv 43,10,11
ma v,28,a,44,v,196
tr 94,221,6400
rd a,0,216,0
)"
R"(mz v,144,a,240,v,40
wl 15
v_exp_f32 v4,v4
mz v,48,a,48,v,40
v_exp_f32 v5,v5
mz v,56,a,52,v,40
rd a,4,216,6144
cv 44,4,5
mz v,64,a,56,v,40
wl 14
v_exp_f32 v6,v6
mz v,72,a,60,v,40
v_exp_f32 v7,v7
mz v,80,a,64,v,40
rd a,8,217,0
cv 45,6,7
mz v,88,a,68,v,40
wl 9
v_exp_f32 v12,v12
mz v,96,a,72,v,40
v_exp_f32 v13,v13
mz v,104,a,76,v,40
rd a,12,217,6144
cv 46,12,13
mz v,112,a,80,v,40
wl 4
v_exp_f32 v14,v14
mz v,120,a,84,v,40
v_exp_f32 v15,v15
mz v,128,a,88,v,40
rd a,16,216,128
cv 47,14,15
mz v,136,a,92,v,40
rd a,20,216,6272
v_max3_f32 v240,v16,v17,v18
mz v,148,a,240,v,44
rd a,24,217,128
v_max3_f32 v241,v20,v21,v22
mz v,52,a,48,v,44
rd a,28,217,6272
v_max3_f32 v240,v240,v19,v24
mz v,60,a,52,v,44
rd a,32,216,256
v_max3_f32 v241,v241,v23,v28
mz v,68,a,56,v,44
rd a,36,216,6400
v_max3_f32 v240,v240,v25,v26
mz v,76,a,60,v,44
rd a,40,217,256
v_max3_f32 v241,v241,v29,v30
mz v,84,a,64,v,44
rd a,44,217,6400
v_max_f32 v240,v240,v27
mz v,92,a,68,v,44
v_max_f32 v241,v241,v31
v_max_f32 v248,v240,v241
mz v,100,a,72,v,44
v_add_u32 v218,s94,v218
v_cmp_lt_f32 vcc,s87,v248
mz v,108,a,76,v,44
v_add_u32 v219,s94,v219
v_add_u32 v220,s94,v220
mz v,116,a,80,v,44
v_add_u32 v221,s94,v221
mz v,124,a,84,v,44
mz v,132,a,88,v,44
s_waitcnt vmcnt(0) lgkmcnt(0)
mz v,140,a,92,v,44
s_cbranch_vccnz LA_a0i0_%=
LA_a0i0_ret_%=:
s_barrier
s_mov_b32 s70,0
LA_a0top_%=:
s_cmp_lg_u32 s70,0
s_cbranch_scc1 LA_de0_%=
v_accvgpr_read_b32 v200,a96
v_accvgpr_read_b32 v201,a97
v_accvgpr_read_b32 v202,a98
v_accvgpr_read_b32 v203,a99
v_accvgpr_read_b32 v204,a104
v_accvgpr_read_b32 v205,a105
v_accvgpr_read_b32 v206,a106
v_accvgpr_read_b32 v207,a107
v_pk_mul_f32 v[200:201],v[200:201],s[58:59] op_sel_hi:[1,0]
v_pk_mul_f32 v[202:203],v[202:203],s[58:59] op_sel_hi:[1,0]
v_pk_mul_f32 v[204:205],v[204:205],s[58:59] op_sel_hi:[1,0]
v_pk_mul_f32 v[206:207],v[206:207],s[58:59] op_sel_hi:[1,0]
cv 40,200,201
cv 41,202,203
cv 42,204,205
cv 43,206,207
v_accvgpr_read_b32 v208,a100
v_accvgpr_read_b32 v209,a101
v_accvgpr_read_b32 v210,a102
v_accvgpr_read_b32 v211,a103
v_accvgpr_read_b32 v212,a108
v_accvgpr_read_b32 v213,a109
v_accvgpr_read_b32 v214,a110
v_accvgpr_read_b32 v215,a111
v_pk_mul_f32 v[208:209],v[208:209],s[58:59] op_sel_hi:[1,0]
v_pk_mul_f32 v[210:211],v[210:211],s[58:59] op_sel_hi:[1,0]
v_pk_mul_f32 v[212:213],v[212:213],s[58:59] op_sel_hi:[1,0]
v_pk_mul_f32 v[214:215],v[214:215],s[58:59] op_sel_hi:[1,0]
cv 44,208,209
cv 45,210,211
cv 46,212,213
cv 47,214,215
v_permlane32_swap_b32 v40,v42
v_permlane32_swap_b32 v41,v43
v_permlane32_swap_b32 v44,v46
v_permlane32_swap_b32 v45,v47
v_permlane16_swap_b32 v40,v42
v_permlane16_swap_b32 v41,v43
v_permlane16_swap_b32 v44,v46
v_permlane16_swap_b32 v45,v47
v_accvgpr_write_b32 a192,v40
v_accvgpr_write_b32 a193,v41
v_accvgpr_write_b32 a194,v42
v_accvgpr_write_b32 a195,v43
v_accvgpr_write_b32 a196,v44
v_accvgpr_write_b32 a197,v45
v_accvgpr_write_b32 a198,v46
v_accvgpr_write_b32 a199,v47
s_branch LA_de6_%=
LA_de0_%=:
s_cmp_lg_u32 s70,1
s_cbranch_scc1 LA_de1_%=
v_accvgpr_read_b32 v200,a112
v_accvgpr_read_b32 v201,a113
v_accvgpr_read_b32 v202,a114
v_accvgpr_read_b32 v203,a115
v_accvgpr_read_b32 v204,a120
v_accvgpr_read_b32 v205,a121
v_accvgpr_read_b32 v206,a122
v_accvgpr_read_b32 v207,a123
v_pk_mul_f32 v[200:201],v[200:201],s[58:59] op_sel_hi:[1,0]
v_pk_mul_f32 v[202:203],v[202:203],s[58:59] op_sel_hi:[1,0]
v_pk_mul_f32 v[204:205],v[204:205],s[58:59] op_sel_hi:[1,0]
v_pk_mul_f32 v[206:207],v[206:207],s[58:59] op_sel_hi:[1,0]
cv 40,200,201
cv 41,202,203
cv 42,204,205
cv 43,206,207
v_accvgpr_read_b32 v208,a116
v_accvgpr_read_b32 v209,a117
v_accvgpr_read_b32 v210,a118
v_accvgpr_read_b32 v211,a119
v_accvgpr_read_b32 v212,a124
v_accvgpr_read_b32 v213,a125
v_accvgpr_read_b32 v214,a126
v_accvgpr_read_b32 v215,a127
v_pk_mul_f32 v[208:209],v[208:209],s[58:59] op_sel_hi:[1,0]
v_pk_mul_f32 v[210:211],v[210:211],s[58:59] op_sel_hi:[1,0]
v_pk_mul_f32 v[212:213],v[212:213],s[58:59] op_sel_hi:[1,0]
v_pk_mul_f32 v[214:215],v[214:215],s[58:59] op_sel_hi:[1,0]
cv 44,208,209
cv 45,210,211
cv 46,212,213
cv 47,214,215
v_permlane32_swap_b32 v40,v42
v_permlane32_swap_b32 v41,v43
v_permlane32_swap_b32 v44,v46
v_permlane32_swap_b32 v45,v47
v_permlane16_swap_b32 v40,v42
v_permlane16_swap_b32 v41,v43
v_permlane16_swap_b32 v44,v46
v_permlane16_swap_b32 v45,v47
v_accvgpr_write_b32 a200,v40
v_accvgpr_write_b32 a201,v41
v_accvgpr_write_b32 a202,v42
v_accvgpr_write_b32 a203,v43
v_accvgpr_write_b32 a204,v44
v_accvgpr_write_b32 a205,v45
v_accvgpr_write_b32 a206,v46
v_accvgpr_write_b32 a207,v47
s_branch LA_de6_%=
LA_de1_%=:
s_cmp_lg_u32 s70,2
s_cbranch_scc1 LA_de2_%=
v_accvgpr_read_b32 v200,a128
v_accvgpr_read_b32 v201,a129
v_accvgpr_read_b32 v202,a130
v_accvgpr_read_b32 v203,a131
v_accvgpr_read_b32 v204,a136
v_accvgpr_read_b32 v205,a137
v_accvgpr_read_b32 v206,a138
v_accvgpr_read_b32 v207,a139
v_pk_mul_f32 v[200:201],v[200:201],s[58:59] op_sel_hi:[1,0]
v_pk_mul_f32 v[202:203],v[202:203],s[58:59] op_sel_hi:[1,0]
v_pk_mul_f32 v[204:205],v[204:205],s[58:59] op_sel_hi:[1,0]
v_pk_mul_f32 v[206:207],v[206:207],s[58:59] op_sel_hi:[1,0]
cv 40,200,201
cv 41,202,203
cv 42,204,205
cv 43,206,207
v_accvgpr_read_b32 v208,a132
v_accvgpr_read_b32 v209,a133
v_accvgpr_read_b32 v210,a134
v_accvgpr_read_b32 v211,a135
v_accvgpr_read_b32 v212,a140
)"
R"(v_accvgpr_read_b32 v213,a141
v_accvgpr_read_b32 v214,a142
v_accvgpr_read_b32 v215,a143
v_pk_mul_f32 v[208:209],v[208:209],s[58:59] op_sel_hi:[1,0]
v_pk_mul_f32 v[210:211],v[210:211],s[58:59] op_sel_hi:[1,0]
v_pk_mul_f32 v[212:213],v[212:213],s[58:59] op_sel_hi:[1,0]
v_pk_mul_f32 v[214:215],v[214:215],s[58:59] op_sel_hi:[1,0]
cv 44,208,209
cv 45,210,211
cv 46,212,213
cv 47,214,215
v_permlane32_swap_b32 v40,v42
v_permlane32_swap_b32 v41,v43
v_permlane32_swap_b32 v44,v46
v_permlane32_swap_b32 v45,v47
v_permlane16_swap_b32 v40,v42
v_permlane16_swap_b32 v41,v43
v_permlane16_swap_b32 v44,v46
v_permlane16_swap_b32 v45,v47
v_accvgpr_write_b32 a208,v40
v_accvgpr_write_b32 a209,v41
v_accvgpr_write_b32 a210,v42
v_accvgpr_write_b32 a211,v43
v_accvgpr_write_b32 a212,v44
v_accvgpr_write_b32 a213,v45
v_accvgpr_write_b32 a214,v46
v_accvgpr_write_b32 a215,v47
s_branch LA_de6_%=
LA_de2_%=:
s_cmp_lg_u32 s70,3
s_cbranch_scc1 LA_de3_%=
v_accvgpr_read_b32 v200,a144
v_accvgpr_read_b32 v201,a145
v_accvgpr_read_b32 v202,a146
v_accvgpr_read_b32 v203,a147
v_accvgpr_read_b32 v204,a152
v_accvgpr_read_b32 v205,a153
v_accvgpr_read_b32 v206,a154
v_accvgpr_read_b32 v207,a155
v_pk_mul_f32 v[200:201],v[200:201],s[58:59] op_sel_hi:[1,0]
v_pk_mul_f32 v[202:203],v[202:203],s[58:59] op_sel_hi:[1,0]
v_pk_mul_f32 v[204:205],v[204:205],s[58:59] op_sel_hi:[1,0]
v_pk_mul_f32 v[206:207],v[206:207],s[58:59] op_sel_hi:[1,0]
cv 40,200,201
cv 41,202,203
cv 42,204,205
cv 43,206,207
v_accvgpr_read_b32 v208,a148
v_accvgpr_read_b32 v209,a149
v_accvgpr_read_b32 v210,a150
v_accvgpr_read_b32 v211,a151
v_accvgpr_read_b32 v212,a156
v_accvgpr_read_b32 v213,a157
v_accvgpr_read_b32 v214,a158
v_accvgpr_read_b32 v215,a159
v_pk_mul_f32 v[208:209],v[208:209],s[58:59] op_sel_hi:[1,0]
v_pk_mul_f32 v[210:211],v[210:211],s[58:59] op_sel_hi:[1,0]
v_pk_mul_f32 v[212:213],v[212:213],s[58:59] op_sel_hi:[1,0]
v_pk_mul_f32 v[214:215],v[214:215],s[58:59] op_sel_hi:[1,0]
cv 44,208,209
cv 45,210,211
cv 46,212,213
cv 47,214,215
v_permlane32_swap_b32 v40,v42
v_permlane32_swap_b32 v41,v43
v_permlane32_swap_b32 v44,v46
v_permlane32_swap_b32 v45,v47
v_permlane16_swap_b32 v40,v42
v_permlane16_swap_b32 v41,v43
v_permlane16_swap_b32 v44,v46
v_permlane16_swap_b32 v45,v47
v_accvgpr_write_b32 a216,v40
v_accvgpr_write_b32 a217,v41
v_accvgpr_write_b32 a218,v42
v_accvgpr_write_b32 a219,v43
v_accvgpr_write_b32 a220,v44
v_accvgpr_write_b32 a221,v45
v_accvgpr_write_b32 a222,v46
v_accvgpr_write_b32 a223,v47
s_branch LA_de6_%=
LA_de3_%=:
s_cmp_lg_u32 s70,4
s_cbranch_scc1 LA_de4_%=
v_accvgpr_read_b32 v200,a160
v_accvgpr_read_b32 v201,a161
v_accvgpr_read_b32 v202,a162
v_accvgpr_read_b32 v203,a163
v_accvgpr_read_b32 v204,a168
v_accvgpr_read_b32 v205,a169
v_accvgpr_read_b32 v206,a170
v_accvgpr_read_b32 v207,a171
v_pk_mul_f32 v[200:201],v[200:201],s[58:59] op_sel_hi:[1,0]
v_pk_mul_f32 v[202:203],v[202:203],s[58:59] op_sel_hi:[1,0]
v_pk_mul_f32 v[204:205],v[204:205],s[58:59] op_sel_hi:[1,0]
v_pk_mul_f32 v[206:207],v[206:207],s[58:59] op_sel_hi:[1,0]
cv 40,200,201
cv 41,202,203
cv 42,204,205
cv 43,206,207
v_accvgpr_read_b32 v208,a164
v_accvgpr_read_b32 v209,a165
v_accvgpr_read_b32 v210,a166
v_accvgpr_read_b32 v211,a167
v_accvgpr_read_b32 v212,a172
v_accvgpr_read_b32 v213,a173
v_accvgpr_read_b32 v214,a174
v_accvgpr_read_b32 v215,a175
v_pk_mul_f32 v[208:209],v[208:209],s[58:59] op_sel_hi:[1,0]
v_pk_mul_f32 v[210:211],v[210:211],s[58:59] op_sel_hi:[1,0]
v_pk_mul_f32 v[212:213],v[212:213],s[58:59] op_sel_hi:[1,0]
v_pk_mul_f32 v[214:215],v[214:215],s[58:59] op_sel_hi:[1,0]
cv 44,208,209
cv 45,210,211
cv 46,212,213
cv 47,214,215
v_permlane32_swap_b32 v40,v42
v_permlane32_swap_b32 v41,v43
v_permlane32_swap_b32 v44,v46
v_permlane32_swap_b32 v45,v47
v_permlane16_swap_b32 v40,v42
v_permlane16_swap_b32 v41,v43
v_permlane16_swap_b32 v44,v46
v_permlane16_swap_b32 v45,v47
v_accvgpr_write_b32 a224,v40
v_accvgpr_write_b32 a225,v41
v_accvgpr_write_b32 a226,v42
v_accvgpr_write_b32 a227,v43
v_accvgpr_write_b32 a228,v44
v_accvgpr_write_b32 a229,v45
v_accvgpr_write_b32 a230,v46
v_accvgpr_write_b32 a231,v47
s_branch LA_de6_%=
LA_de4_%=:
s_cmp_lg_u32 s70,5
s_cbranch_scc1 LA_de5_%=
v_accvgpr_read_b32 v200,a176
v_accvgpr_read_b32 v201,a177
v_accvgpr_read_b32 v202,a178
v_accvgpr_read_b32 v203,a179
v_accvgpr_read_b32 v204,a184
v_accvgpr_read_b32 v205,a185
v_accvgpr_read_b32 v206,a186
v_accvgpr_read_b32 v207,a187
v_pk_mul_f32 v[200:201],v[200:201],s[58:59] op_sel_hi:[1,0]
v_pk_mul_f32 v[202:203],v[202:203],s[58:59] op_sel_hi:[1,0]
v_pk_mul_f32 v[204:205],v[204:205],s[58:59] op_sel_hi:[1,0]
v_pk_mul_f32 v[206:207],v[206:207],s[58:59] op_sel_hi:[1,0]
cv 40,200,201
cv 41,202,203
cv 42,204,205
cv 43,206,207
v_accvgpr_read_b32 v208,a180
v_accvgpr_read_b32 v209,a181
v_accvgpr_read_b32 v210,a182
v_accvgpr_read_b32 v211,a183
v_accvgpr_read_b32 v212,a188
v_accvgpr_read_b32 v213,a189
v_accvgpr_read_b32 v214,a190
v_accvgpr_read_b32 v215,a191
v_pk_mul_f32 v[208:209],v[208:209],s[58:59] op_sel_hi:[1,0]
v_pk_mul_f32 v[210:211],v[210:211],s[58:59] op_sel_hi:[1,0]
v_pk_mul_f32 v[212:213],v[212:213],s[58:59] op_sel_hi:[1,0]
v_pk_mul_f32 v[214:215],v[214:215],s[58:59] op_sel_hi:[1,0]
cv 44,208,209
cv 45,210,211
cv 46,212,213
cv 47,214,215
v_permlane32_swap_b32 v40,v42
v_permlane32_swap_b32 v41,v43
v_permlane32_swap_b32 v44,v46
v_permlane32_swap_b32 v45,v47
v_permlane16_swap_b32 v40,v42
v_permlane16_swap_b32 v41,v43
v_permlane16_swap_b32 v44,v46
v_permlane16_swap_b32 v45,v47
v_accvgpr_write_b32 a232,v40
v_accvgpr_write_b32 a233,v41
v_accvgpr_write_b32 a234,v42
v_accvgpr_write_b32 a235,v43
v_accvgpr_write_b32 a236,v44
v_accvgpr_write_b32 a237,v45
v_accvgpr_write_b32 a238,v46
v_accvgpr_write_b32 a239,v47
s_branch LA_de6_%=
LA_de5_%=:
v_mbcnt_lo_u32_b32 v200,-1,0
v_mbcnt_hi_u32_b32 v200,-1,v200
v_lshrrev_b32 v201,3,v200
v_and_b32 v202,7,v200
v_and_b32 v203,6,v201
v_xor_b32 v203,v203,v202
v_mul_u32_u24 v204,0x500,v201
v_lshl_add_u32 v235,v203,4,v204
v_add_u32 v236,0x2400,v235
v_add_u32 v237,0x4800,v235
v_add_u32 v238,0x6c00,v235
v_mbcnt_lo_u32_b32 v200,-1,0
v_mbcnt_hi_u32_b32 v200,-1,v200
v_lshrrev_b32 v201,4,v200
v_and_b32 v202,15,v200
)"
R"(v_lshrrev_b32 v202,2,v202
v_and_b32 v203,3,v200
v_and_b32 v204,1,v201
v_lshlrev_b32 v205,1,v204
v_lshrrev_b32 v206,1,v202
v_add_u32 v205,v205,v206
v_lshlrev_b32 v204,9,v204
v_lshrrev_b32 v206,1,v201
v_lshl_add_u32 v204,v206,10,v204
v_lshl_add_u32 v204,v202,7,v204
v_lshl_add_u32 v204,v203,3,v204
v_xor_b32 v206,0,v205
v_lshl_add_u32 v222,v206,5,v204
v_xor_b32 v206,1,v205
v_lshl_add_u32 v223,v206,5,v204
v_xor_b32 v206,2,v205
v_lshl_add_u32 v224,v206,5,v204
v_xor_b32 v206,3,v205
v_lshl_add_u32 v225,v206,5,v204
s_mov_b32 s7,0x3c000
s_mov_b32 s8,0x3bf80
s_mov_b32 s9,0x50000
s_mov_b32 s10,0x8000
s_add_u32 s88,s68,1
s_cmp_eq_u32 s88,10
s_cselect_b32 s88,0,s88
s_lshl_b32 s91,s88,7
s_lshl_b32 s88,s68,7
s_mul_i32 s89,s64,0x5000
s_add_u32 s11,s88,s89
s_lshl_b32 s12,s64,11
s_add_u32 s14,s12,0xc000
s_add_u32 s13,s91,s89
s_mov_b32 s15,0xf000
s_mul_i32 s89,s64,3
s_mul_i32 s73,s89,0x2800
s_add_u32 s74,s73,0x500
s_add_u32 s72,s73,s91
s_add_u32 s72,s72,0
s_lshl_b32 s75,s89,10
s_mul_i32 s89,s64,0x140
s_add_u32 s89,s89,s66
s_lshl_b32 s78,s89,11
s_add_u32 s79,s78,s9
s_lshl_b32 s88,s68,15
s_add_u32 s77,s78,s88
v_mbcnt_lo_u32_b32 v200,-1,0
v_mbcnt_hi_u32_b32 v200,-1,v200
v_lshrrev_b32 v201,4,v200
v_and_b32 v202,15,v200
v_and_b32 v205,1,v202
v_lshlrev_b32 v205,4,v205
v_mov_b32 v204,0x3f80
v_lshlrev_b32 v204,v205,v204
v_lshrrev_b32 v203,3,v202
v_lshlrev_b32 v203,1,v203
v_lshrrev_b32 v205,1,v202
v_and_b32 v205,1,v205
v_add_u32 v203,v203,v205
v_lshrrev_b32 v202,2,v202
v_lshlrev_b32 v202,1,v202
v_add_u32 v205,0,v202
v_and_b32 v205,3,v205
v_cmp_eq_u32 vcc,v205,v201
s_nop 1
v_cndmask_b32 v206,0,v204,vcc
v_cmp_eq_u32 vcc,0,v203
s_nop 1
v_cndmask_b32 v205,0,v206,vcc
v_accvgpr_write_b32 a244,v205
v_cmp_eq_u32 vcc,1,v203
s_nop 1
v_cndmask_b32 v205,0,v206,vcc
v_accvgpr_write_b32 a245,v205
v_cmp_eq_u32 vcc,2,v203
s_nop 1
v_cndmask_b32 v205,0,v206,vcc
v_accvgpr_write_b32 a246,v205
v_cmp_eq_u32 vcc,3,v203
s_nop 1
v_cndmask_b32 v205,0,v206,vcc
v_accvgpr_write_b32 a247,v205
v_add_u32 v205,1,v202
v_and_b32 v205,3,v205
v_cmp_eq_u32 vcc,v205,v201
s_nop 1
v_cndmask_b32 v206,0,v204,vcc
v_cmp_eq_u32 vcc,0,v203
s_nop 1
v_cndmask_b32 v205,0,v206,vcc
v_accvgpr_write_b32 a248,v205
v_cmp_eq_u32 vcc,1,v203
s_nop 1
v_cndmask_b32 v205,0,v206,vcc
v_accvgpr_write_b32 a249,v205
v_cmp_eq_u32 vcc,2,v203
s_nop 1
v_cndmask_b32 v205,0,v206,vcc
v_accvgpr_write_b32 a250,v205
v_cmp_eq_u32 vcc,3,v203
s_nop 1
v_cndmask_b32 v205,0,v206,vcc
v_accvgpr_write_b32 a251,v205
LA_de6_%=:
mc v,0,a,0,v,152,32
s_add_u32 s85,s85,s96
v_exp_f32 v16,v16
mc v,4,a,0,v,156,36
s_cmp_ge_u32 s85,s97
tr 48,218,0
tr 50,218,6144
mc v,8,a,4,v,152,32
s_cselect_b32 s93,s98,s96
tr 52,219,0
tr 54,219,6144
mc v,12,a,4,v,156,36
s_cselect_b32 s85,0,s85
v_exp_f32 v17,v17
ma v,0,a,8,v,160
tr 56,220,0
cv 40,16,17
ma v,4,a,8,v,164
tr 58,220,6144
v_add_u32 v216,s93,v216
ma v,8,a,12,v,160
s_add_u32 s86,s86,s96
v_exp_f32 v18,v18
ma v,12,a,12,v,164
s_cmp_ge_u32 s86,s97
tr 60,221,0
tr 62,221,6144
ma v,0,a,16,v,168
s_cselect_b32 s94,s98,s96
tr 64,218,128
tr 66,218,6272
ma v,4,a,16,v,172
s_cselect_b32 s86,0,s86
v_exp_f32 v19,v19
ma v,8,a,20,v,168
tr 68,219,128
cv 41,18,19
ma v,12,a,20,v,172
tr 70,219,6272
v_add_u32 v217,s93,v217
ma v,0,a,24,v,176
v_exp_f32 v24,v24
ma v,4,a,24,v,180
tr 72,220,128
tr 74,220,6272
ma v,8,a,28,v,176
tr 76,221,128
tr 78,221,6272
ma v,12,a,28,v,180
v_exp_f32 v25,v25
ma v,0,a,32,v,184
tr 80,218,256
cv 42,24,25
ma v,4,a,32,v,188
tr 82,218,6400
ma v,8,a,36,v,184
v_exp_f32 v26,v26
ma v,12,a,36,v,188
tr 84,219,256
tr 86,219,6400
ma v,0,a,40,v,192
tr 88,220,256
tr 90,220,6400
ma v,4,a,40,v,196
v_exp_f32 v27,v27
ma v,8,a,44,v,192
tr 92,221,256
cv 43,26,27
ma v,12,a,44,v,196
tr 94,221,6400
rd a,0,216,0
ma v,144,a,240,v,40
wl 15
v_exp_f32 v20,v20
ma v,48,a,48,v,40
v_exp_f32 v21,v21
ma v,56,a,52,v,40
rd a,4,216,6144
cv 44,20,21
ma v,64,a,56,v,40
wl 14
v_exp_f32 v22,v22
ma v,72,a,60,v,40
v_exp_f32 v23,v23
ma v,80,a,64,v,40
rd a,8,217,0
cv 45,22,23
ma v,88,a,68,v,40
wl 9
v_exp_f32 v28,v28
ma v,96,a,72,v,40
v_exp_f32 v29,v29
ma v,104,a,76,v,40
rd a,12,217,6144
cv 46,28,29
ma v,112,a,80,v,40
)"
R"(wl 4
v_exp_f32 v30,v30
ma v,120,a,84,v,40
v_exp_f32 v31,v31
ma v,128,a,88,v,40
rd a,16,216,128
cv 47,30,31
ma v,136,a,92,v,40
rd a,20,216,6272
v_max3_f32 v240,v0,v1,v2
ma v,148,a,240,v,44
rd a,24,217,128
v_max3_f32 v241,v4,v5,v6
ma v,52,a,48,v,44
rd a,28,217,6272
v_max3_f32 v240,v240,v3,v8
ma v,60,a,52,v,44
rd a,32,216,256
v_max3_f32 v241,v241,v7,v12
ma v,68,a,56,v,44
rd a,36,216,6400
v_max3_f32 v240,v240,v9,v10
ma v,76,a,60,v,44
rd a,40,217,256
v_max3_f32 v241,v241,v13,v14
ma v,84,a,64,v,44
rd a,44,217,6400
v_max_f32 v240,v240,v11
ma v,92,a,68,v,44
v_max_f32 v241,v241,v15
v_max_f32 v248,v240,v241
ma v,100,a,72,v,44
v_add_u32 v218,s94,v218
v_cmp_lt_f32 vcc,s87,v248
ma v,108,a,76,v,44
v_add_u32 v219,s94,v219
v_add_u32 v220,s94,v220
ma v,116,a,80,v,44
v_add_u32 v221,s94,v221
ma v,124,a,84,v,44
ma v,132,a,88,v,44
wl 0
ma v,140,a,92,v,44
s_cbranch_vccnz LA_a0i1_%=
LA_a0i1_ret_%=:
s_barrier
mc v,16,a,0,v,152,32
s_add_u32 s85,s85,s96
v_exp_f32 v0,v0
mc v,20,a,0,v,156,36
s_cmp_ge_u32 s85,s97
tr 48,218,0
tr 50,218,6144
mc v,24,a,4,v,152,32
s_cselect_b32 s93,s98,s96
tr 52,219,0
tr 54,219,6144
mc v,28,a,4,v,156,36
s_cselect_b32 s85,0,s85
v_exp_f32 v1,v1
ma v,16,a,8,v,160
tr 56,220,0
cv 40,0,1
ma v,20,a,8,v,164
tr 58,220,6144
v_add_u32 v216,s93,v216
ma v,24,a,12,v,160
s_add_u32 s86,s86,s96
v_exp_f32 v2,v2
ma v,28,a,12,v,164
s_cmp_ge_u32 s86,s97
tr 60,221,0
tr 62,221,6144
ma v,16,a,16,v,168
s_cselect_b32 s94,s98,s96
tr 64,218,128
tr 66,218,6272
ma v,20,a,16,v,172
s_cselect_b32 s86,0,s86
v_exp_f32 v3,v3
ma v,24,a,20,v,168
tr 68,219,128
cv 41,2,3
ma v,28,a,20,v,172
tr 70,219,6272
v_add_u32 v217,s93,v217
ma v,16,a,24,v,176
s_add_u32 s70,s70,1
v_exp_f32 v8,v8
ma v,20,a,24,v,180
s_cmp_lt_u32 s70,7
tr 72,220,128
tr 74,220,6272
ma v,24,a,28,v,176
tr 76,221,128
tr 78,221,6272
ma v,28,a,28,v,180
v_exp_f32 v9,v9
ma v,16,a,32,v,184
tr 80,218,256
cv 42,8,9
ma v,20,a,32,v,188
tr 82,218,6400
ma v,24,a,36,v,184
v_exp_f32 v10,v10
ma v,28,a,36,v,188
tr 84,219,256
tr 86,219,6400
ma v,16,a,40,v,192
tr 88,220,256
tr 90,220,6400
ma v,20,a,40,v,196
v_exp_f32 v11,v11
ma v,24,a,44,v,192
tr 92,221,256
cv 43,10,11
ma v,28,a,44,v,196
tr 94,221,6400
rd a,0,216,0
ma v,144,a,240,v,40
wl 15
v_exp_f32 v4,v4
ma v,48,a,48,v,40
v_exp_f32 v5,v5
ma v,56,a,52,v,40
rd a,4,216,6144
cv 44,4,5
ma v,64,a,56,v,40
wl 14
v_exp_f32 v6,v6
ma v,72,a,60,v,40
v_exp_f32 v7,v7
ma v,80,a,64,v,40
rd a,8,217,0
cv 45,6,7
ma v,88,a,68,v,40
wl 9
v_exp_f32 v12,v12
ma v,96,a,72,v,40
v_exp_f32 v13,v13
ma v,104,a,76,v,40
rd a,12,217,6144
cv 46,12,13
ma v,112,a,80,v,40
wl 4
v_exp_f32 v14,v14
ma v,120,a,84,v,40
v_exp_f32 v15,v15
ma v,128,a,88,v,40
rd a,16,216,128
cv 47,14,15
ma v,136,a,92,v,40
rd a,20,216,6272
v_max3_f32 v240,v16,v17,v18
ma v,148,a,240,v,44
rd a,24,217,128
v_max3_f32 v241,v20,v21,v22
ma v,52,a,48,v,44
rd a,28,217,6272
v_max3_f32 v240,v240,v19,v24
ma v,60,a,52,v,44
rd a,32,216,256
v_max3_f32 v241,v241,v23,v28
ma v,68,a,56,v,44
rd a,36,216,6400
v_max3_f32 v240,v240,v25,v26
ma v,76,a,60,v,44
rd a,40,217,256
v_max3_f32 v241,v241,v29,v30
ma v,84,a,64,v,44
rd a,44,217,6400
v_max_f32 v240,v240,v27
ma v,92,a,68,v,44
v_max_f32 v241,v241,v31
v_max_f32 v248,v240,v241
ma v,100,a,72,v,44
v_add_u32 v218,s94,v218
v_cmp_lt_f32 vcc,s87,v248
ma v,108,a,76,v,44
v_add_u32 v219,s94,v219
v_add_u32 v220,s94,v220
ma v,116,a,80,v,44
v_add_u32 v221,s94,v221
ma v,124,a,84,v,44
ma v,132,a,88,v,44
wl 0
ma v,140,a,92,v,44
s_cbranch_vccnz LA_a0i2_%=
LA_a0i2_ret_%=:
s_barrier
s_cbranch_scc1 LA_a0top_%=
tr 48,218,0
tr 50,218,6144
tr 52,219,0
tr 54,219,6144
tr 56,220,0
tr 58,220,6144
tr 60,221,0
tr 62,221,6144
v_exp_f32 v16,v16
)"
R"(v_exp_f32 v17,v17
v_exp_f32 v18,v18
v_exp_f32 v19,v19
v_exp_f32 v24,v24
v_exp_f32 v25,v25
v_exp_f32 v26,v26
v_exp_f32 v27,v27
v_exp_f32 v20,v20
v_exp_f32 v21,v21
v_exp_f32 v22,v22
v_exp_f32 v23,v23
v_exp_f32 v28,v28
v_exp_f32 v29,v29
v_exp_f32 v30,v30
v_exp_f32 v31,v31
cv 40,16,17
cv 41,18,19
cv 42,24,25
cv 43,26,27
cv 44,20,21
cv 45,22,23
cv 46,28,29
cv 47,30,31
tr 64,218,128
tr 66,218,6272
tr 68,219,128
tr 70,219,6272
tr 72,220,128
tr 74,220,6272
tr 76,221,128
tr 78,221,6272
tr 80,218,256
tr 82,218,6400
tr 84,219,256
tr 86,219,6400
tr 88,220,256
tr 90,220,6400
tr 92,221,256
tr 94,221,6400
ma v,144,a,240,v,40
s_add_u32 s86,s86,s96
ma v,148,a,240,v,44
wl 15
ma v,48,a,48,v,40
ma v,52,a,48,v,44
s_cmp_ge_u32 s86,s97
ma v,56,a,52,v,40
ma v,60,a,52,v,44
ma v,64,a,56,v,40
s_cselect_b32 s94,s98,s96
ma v,68,a,56,v,44
ma v,72,a,60,v,40
ma v,76,a,60,v,44
s_cselect_b32 s86,0,s86
wl 14
ma v,80,a,64,v,40
ma v,84,a,64,v,44
wl 12
ma v,88,a,68,v,40
ma v,92,a,68,v,44
v_add_u32 v218,s94,v218
wl 10
ma v,96,a,72,v,40
ma v,100,a,72,v,44
wl 8
ma v,104,a,76,v,40
v_add_u32 v219,s94,v219
ma v,108,a,76,v,44
wl 6
ma v,112,a,80,v,40
ma v,116,a,80,v,44
v_add_u32 v220,s94,v220
wl 4
ma v,120,a,84,v,40
ma v,124,a,84,v,44
wl 2
ma v,128,a,88,v,40
v_add_u32 v221,s94,v221
ma v,132,a,88,v,44
wl 0
ma v,136,a,92,v,40
ma v,140,a,92,v,44
s_barrier
s_waitcnt vmcnt(48)
s_nop 7
v_rcp_f32 v242,v144
v_rcp_f32 v244,v148
v_pk_mul_f32 v[48:49],v[48:49],v[242:243] op_sel_hi:[1,0]
v_pk_mul_f32 v[50:51],v[50:51],v[242:243] op_sel_hi:[1,0]
v_pk_mul_f32 v[56:57],v[56:57],v[242:243] op_sel_hi:[1,0]
v_pk_mul_f32 v[58:59],v[58:59],v[242:243] op_sel_hi:[1,0]
cv 152,48,49
cv 153,50,51
cv 154,56,57
cv 155,58,59
v_pk_mul_f32 v[52:53],v[52:53],v[244:245] op_sel_hi:[1,0]
v_pk_mul_f32 v[54:55],v[54:55],v[244:245] op_sel_hi:[1,0]
v_pk_mul_f32 v[60:61],v[60:61],v[244:245] op_sel_hi:[1,0]
v_pk_mul_f32 v[62:63],v[62:63],v[244:245] op_sel_hi:[1,0]
cv 156,52,53
cv 157,54,55
cv 158,60,61
cv 159,62,63
v_pk_mul_f32 v[64:65],v[64:65],v[242:243] op_sel_hi:[1,0]
v_pk_mul_f32 v[66:67],v[66:67],v[242:243] op_sel_hi:[1,0]
v_pk_mul_f32 v[72:73],v[72:73],v[242:243] op_sel_hi:[1,0]
v_pk_mul_f32 v[74:75],v[74:75],v[242:243] op_sel_hi:[1,0]
cv 160,64,65
cv 161,66,67
cv 162,72,73
cv 163,74,75
v_pk_mul_f32 v[68:69],v[68:69],v[244:245] op_sel_hi:[1,0]
v_pk_mul_f32 v[70:71],v[70:71],v[244:245] op_sel_hi:[1,0]
v_pk_mul_f32 v[76:77],v[76:77],v[244:245] op_sel_hi:[1,0]
v_pk_mul_f32 v[78:79],v[78:79],v[244:245] op_sel_hi:[1,0]
cv 164,68,69
cv 165,70,71
cv 166,76,77
cv 167,78,79
v_pk_mul_f32 v[80:81],v[80:81],v[242:243] op_sel_hi:[1,0]
v_pk_mul_f32 v[82:83],v[82:83],v[242:243] op_sel_hi:[1,0]
v_pk_mul_f32 v[88:89],v[88:89],v[242:243] op_sel_hi:[1,0]
v_pk_mul_f32 v[90:91],v[90:91],v[242:243] op_sel_hi:[1,0]
cv 168,80,81
cv 169,82,83
cv 170,88,89
cv 171,90,91
v_pk_mul_f32 v[84:85],v[84:85],v[244:245] op_sel_hi:[1,0]
v_pk_mul_f32 v[86:87],v[86:87],v[244:245] op_sel_hi:[1,0]
v_pk_mul_f32 v[92:93],v[92:93],v[244:245] op_sel_hi:[1,0]
v_pk_mul_f32 v[94:95],v[94:95],v[244:245] op_sel_hi:[1,0]
cv 172,84,85
cv 173,86,87
cv 174,92,93
cv 175,94,95
v_pk_mul_f32 v[96:97],v[96:97],v[242:243] op_sel_hi:[1,0]
v_pk_mul_f32 v[98:99],v[98:99],v[242:243] op_sel_hi:[1,0]
v_pk_mul_f32 v[104:105],v[104:105],v[242:243] op_sel_hi:[1,0]
v_pk_mul_f32 v[106:107],v[106:107],v[242:243] op_sel_hi:[1,0]
cv 176,96,97
cv 177,98,99
cv 178,104,105
cv 179,106,107
v_pk_mul_f32 v[100:101],v[100:101],v[244:245] op_sel_hi:[1,0]
v_pk_mul_f32 v[102:103],v[102:103],v[244:245] op_sel_hi:[1,0]
v_pk_mul_f32 v[108:109],v[108:109],v[244:245] op_sel_hi:[1,0]
v_pk_mul_f32 v[110:111],v[110:111],v[244:245] op_sel_hi:[1,0]
cv 180,100,101
cv 181,102,103
cv 182,108,109
cv 183,110,111
v_pk_mul_f32 v[112:113],v[112:113],v[242:243] op_sel_hi:[1,0]
v_pk_mul_f32 v[114:115],v[114:115],v[242:243] op_sel_hi:[1,0]
v_pk_mul_f32 v[120:121],v[120:121],v[242:243] op_sel_hi:[1,0]
v_pk_mul_f32 v[122:123],v[122:123],v[242:243] op_sel_hi:[1,0]
cv 184,112,113
cv 185,114,115
cv 186,120,121
cv 187,122,123
v_pk_mul_f32 v[116:117],v[116:117],v[244:245] op_sel_hi:[1,0]
v_pk_mul_f32 v[118:119],v[118:119],v[244:245] op_sel_hi:[1,0]
v_pk_mul_f32 v[124:125],v[124:125],v[244:245] op_sel_hi:[1,0]
v_pk_mul_f32 v[126:127],v[126:127],v[244:245] op_sel_hi:[1,0]
cv 188,116,117
cv 189,118,119
cv 190,124,125
cv 191,126,127
v_pk_mul_f32 v[128:129],v[128:129],v[242:243] op_sel_hi:[1,0]
v_pk_mul_f32 v[130:131],v[130:131],v[242:243] op_sel_hi:[1,0]
v_pk_mul_f32 v[136:137],v[136:137],v[242:243] op_sel_hi:[1,0]
v_pk_mul_f32 v[138:139],v[138:139],v[242:243] op_sel_hi:[1,0]
cv 192,128,129
cv 193,130,131
cv 194,136,137
cv 195,138,139
v_pk_mul_f32 v[132:133],v[132:133],v[244:245] op_sel_hi:[1,0]
v_pk_mul_f32 v[134:135],v[134:135],v[244:245] op_sel_hi:[1,0]
v_pk_mul_f32 v[140:141],v[140:141],v[244:245] op_sel_hi:[1,0]
v_pk_mul_f32 v[142:143],v[142:143],v[244:245] op_sel_hi:[1,0]
cv 196,132,133
cv 197,134,135
cv 198,140,141
cv 199,142,143
mz v,0,a,0,a,192
s_add_u32 s85,s85,s96
mz v,4,a,0,a,196
s_cmp_ge_u32 s85,s97
mz v,8,a,4,a,192
s_cselect_b32 s93,s98,s96
mz v,12,a,4,a,196
s_cselect_b32 s85,0,s85
ma v,0,a,8,a,200
v_add_u32 v216,s93,v216
ma v,4,a,8,a,204
v_add_u32 v217,s93,v217
ma v,8,a,12,a,200
ma v,12,a,12,a,204
ma v,0,a,16,a,208
ma v,4,a,16,a,212
ma v,8,a,20,a,208
)"
R"(ma v,12,a,20,a,212
ma v,0,a,24,a,216
ma v,4,a,24,a,220
ma v,8,a,28,a,216
ma v,12,a,28,a,220
ma v,0,a,32,a,224
ma v,4,a,32,a,228
ma v,8,a,36,a,224
ma v,12,a,36,a,228
ma v,0,a,40,a,232
ma v,4,a,40,a,236
ma v,8,a,44,a,232
ma v,12,a,44,a,236
rd a,0,216,0
rd a,4,216,6144
rd a,8,217,0
rd a,12,217,6144
rd a,16,216,128
rd a,20,216,6272
rd a,24,217,128
rd a,28,217,6272
rd a,32,216,256
rd a,36,216,6400
rd a,40,217,256
rd a,44,217,6400
v_max3_f32 v240,v0,v1,v2
v_max3_f32 v241,v4,v5,v6
v_max3_f32 v240,v240,v3,v8
v_max3_f32 v241,v241,v7,v12
v_max3_f32 v240,v240,v9,v10
v_max3_f32 v241,v241,v13,v14
v_max_f32 v240,v240,v11
v_max_f32 v241,v241,v15
v_mov_b32 v242,v240
v_mov_b32 v244,v241
s_nop 0
v_permlane32_swap_b32 v240,v242
v_permlane32_swap_b32 v241,v244
v_max_f32 v240,v240,v242
v_max_f32 v241,v241,v244
v_mov_b32 v242,v240
v_mov_b32 v244,v241
s_nop 0
v_permlane16_swap_b32 v240,v242
v_permlane16_swap_b32 v241,v244
v_max_f32 v240,v240,v242
v_max_f32 v241,v241,v244
v_sub_f32 v32,0,v240
v_sub_f32 v33,0,v240
v_sub_f32 v34,0,v240
v_sub_f32 v35,0,v240
v_sub_f32 v0,v0,v240
v_sub_f32 v1,v1,v240
v_sub_f32 v2,v2,v240
v_sub_f32 v3,v3,v240
v_sub_f32 v8,v8,v240
v_sub_f32 v9,v9,v240
v_sub_f32 v10,v10,v240
v_sub_f32 v11,v11,v240
v_sub_f32 v36,0,v241
v_sub_f32 v37,0,v241
v_sub_f32 v38,0,v241
v_sub_f32 v39,0,v241
v_sub_f32 v4,v4,v241
v_sub_f32 v5,v5,v241
v_sub_f32 v6,v6,v241
v_sub_f32 v7,v7,v241
v_sub_f32 v12,v12,v241
v_sub_f32 v13,v13,v241
v_sub_f32 v14,v14,v241
v_sub_f32 v15,v15,v241
wl 0
s_barrier
mc v,16,a,0,a,192,32
s_add_u32 s85,s85,s96
v_exp_f32 v0,v0
mc v,20,a,0,a,196,36
s_cmp_ge_u32 s85,s97
tr 48,218,0
tr 50,218,6144
mc v,24,a,4,a,192,32
s_cselect_b32 s93,s98,s96
tr 52,219,0
tr 54,219,6144
mc v,28,a,4,a,196,36
s_cselect_b32 s85,0,s85
v_exp_f32 v1,v1
ma v,16,a,8,a,200
tr 56,220,0
cv 40,0,1
ma v,20,a,8,a,204
tr 58,220,6144
v_add_u32 v216,s93,v216
ma v,24,a,12,a,200
s_add_u32 s86,s86,s96
v_exp_f32 v2,v2
ma v,28,a,12,a,204
s_cmp_ge_u32 s86,s97
tr 60,221,0
tr 62,221,6144
ma v,16,a,16,a,208
s_cselect_b32 s94,s98,s96
tr 64,218,128
tr 66,218,6272
ma v,20,a,16,a,212
s_cselect_b32 s86,0,s86
v_exp_f32 v3,v3
ma v,24,a,20,a,208
tr 68,219,128
cv 41,2,3
ma v,28,a,20,a,212
tr 70,219,6272
v_add_u32 v217,s93,v217
ma v,16,a,24,a,216
v_exp_f32 v8,v8
ma v,20,a,24,a,220
tr 72,220,128
tr 74,220,6272
ma v,24,a,28,a,216
tr 76,221,128
tr 78,221,6272
ma v,28,a,28,a,220
v_exp_f32 v9,v9
ma v,16,a,32,a,224
tr 80,218,256
cv 42,8,9
ma v,20,a,32,a,228
tr 82,218,6400
ma v,24,a,36,a,224
v_exp_f32 v10,v10
ma v,28,a,36,a,228
tr 84,219,256
tr 86,219,6400
ma v,16,a,40,a,232
tr 88,220,256
tr 90,220,6400
ma v,20,a,40,a,236
v_exp_f32 v11,v11
ma v,24,a,44,a,232
tr 92,221,256
cv 43,10,11
ma v,28,a,44,a,236
tr 94,221,6400
rd a,0,216,0
mz v,144,a,240,v,40
wl 15
v_exp_f32 v4,v4
mz v,48,a,48,v,40
v_exp_f32 v5,v5
mz v,56,a,52,v,40
rd a,4,216,6144
cv 44,4,5
mz v,64,a,56,v,40
wl 14
v_exp_f32 v6,v6
mz v,72,a,60,v,40
v_exp_f32 v7,v7
mz v,80,a,64,v,40
rd a,8,217,0
cv 45,6,7
mz v,88,a,68,v,40
wl 9
v_exp_f32 v12,v12
mz v,96,a,72,v,40
v_exp_f32 v13,v13
mz v,104,a,76,v,40
rd a,12,217,6144
cv 46,12,13
mz v,112,a,80,v,40
wl 4
v_exp_f32 v14,v14
mz v,120,a,84,v,40
v_exp_f32 v15,v15
mz v,128,a,88,v,40
rd a,16,216,128
cv 47,14,15
mz v,136,a,92,v,40
rd a,20,216,6272
v_max3_f32 v240,v16,v17,v18
mz v,148,a,240,v,44
rd a,24,217,128
v_max3_f32 v241,v20,v21,v22
mz v,52,a,48,v,44
rd a,28,217,6272
v_max3_f32 v240,v240,v19,v24
mz v,60,a,52,v,44
rd a,32,216,256
v_max3_f32 v241,v241,v23,v28
mz v,68,a,56,v,44
rd a,36,216,6400
v_max3_f32 v240,v240,v25,v26
mz v,76,a,60,v,44
rd a,40,217,256
v_max3_f32 v241,v241,v29,v30
mz v,84,a,64,v,44
rd a,44,217,6400
v_max_f32 v240,v240,v27
mz v,92,a,68,v,44
v_max_f32 v241,v241,v31
v_max_f32 v248,v240,v241
)"
R"(mz v,100,a,72,v,44
v_add_u32 v218,s94,v218
v_cmp_lt_f32 vcc,s87,v248
mz v,108,a,76,v,44
v_add_u32 v219,s94,v219
v_add_u32 v220,s94,v220
mz v,116,a,80,v,44
v_add_u32 v221,s94,v221
mz v,124,a,84,v,44
mz v,132,a,88,v,44
wl 0
mz v,140,a,92,v,44
s_cbranch_vccnz LA_a1i0_%=
LA_a1i0_ret_%=:
s_barrier
s_mov_b32 s70,0
LA_a1top_%=:
s_cmp_lg_u32 s70,3
s_cbranch_scc1 LA_nrw_%=
s_getpc_b64 s[58:59]
s_lshl_b32 s88,s65,1
s_and_b32 s89,s66,1
s_add_u32 s88,s88,s89
s_lshl_b32 s89,s64,5
s_add_u32 s88,s88,s89
s_lshl_b32 s88,s88,11
v_lshrrev_b32 v200,1,v239
v_and_b32 v200,0x78,v200
v_lshl_add_u32 v200,v200,4,s88
v_min_u32 v200,0x13be0,v200
global_load_dwordx2 v[202:203],v200,s[58:59] 
LA_nrw_%=:
mc v,0,a,0,a,192,32
s_add_u32 s85,s85,s96
v_exp_f32 v16,v16
mc v,4,a,0,a,196,36
s_cmp_ge_u32 s85,s97
tr 48,218,0
tr 50,218,6144
mc v,8,a,4,a,192,32
s_cselect_b32 s93,s98,s96
tr 52,219,0
tr 54,219,6144
mc v,12,a,4,a,196,36
s_cselect_b32 s85,0,s85
v_exp_f32 v17,v17
ma v,0,a,8,a,200
tr 56,220,0
cv 40,16,17
ma v,4,a,8,a,204
tr 58,220,6144
v_add_u32 v216,s93,v216
ma v,8,a,12,a,200
s_add_u32 s86,s86,s96
v_exp_f32 v18,v18
ma v,12,a,12,a,204
s_cmp_ge_u32 s86,s97
tr 60,221,0
tr 62,221,6144
ma v,0,a,16,a,208
s_cselect_b32 s94,s98,s96
tr 64,218,128
tr 66,218,6272
ma v,4,a,16,a,212
s_cselect_b32 s86,0,s86
v_exp_f32 v19,v19
ma v,8,a,20,a,208
tr 68,219,128
cv 41,18,19
ma v,12,a,20,a,212
tr 70,219,6272
v_add_u32 v217,s93,v217
ma v,0,a,24,a,216
v_exp_f32 v24,v24
ma v,4,a,24,a,220
tr 72,220,128
tr 74,220,6272
ma v,8,a,28,a,216
tr 76,221,128
tr 78,221,6272
ma v,12,a,28,a,220
v_exp_f32 v25,v25
ma v,0,a,32,a,224
tr 80,218,256
cv 42,24,25
ma v,4,a,32,a,228
tr 82,218,6400
ma v,8,a,36,a,224
v_exp_f32 v26,v26
ma v,12,a,36,a,228
tr 84,219,256
tr 86,219,6400
ma v,0,a,40,a,232
tr 88,220,256
tr 90,220,6400
ma v,4,a,40,a,236
v_exp_f32 v27,v27
ma v,8,a,44,a,232
tr 92,221,256
cv 43,26,27
ma v,12,a,44,a,236
tr 94,221,6400
rd a,0,216,0
ma v,144,a,240,v,40
wl 15
v_exp_f32 v20,v20
ma v,48,a,48,v,40
v_exp_f32 v21,v21
ma v,56,a,52,v,40
rd a,4,216,6144
cv 44,20,21
ma v,64,a,56,v,40
wl 14
v_exp_f32 v22,v22
ma v,72,a,60,v,40
v_exp_f32 v23,v23
ma v,80,a,64,v,40
rd a,8,217,0
cv 45,22,23
ma v,88,a,68,v,40
wl 9
v_exp_f32 v28,v28
ma v,96,a,72,v,40
v_exp_f32 v29,v29
ma v,104,a,76,v,40
rd a,12,217,6144
cv 46,28,29
ma v,112,a,80,v,40
wl 4
v_exp_f32 v30,v30
ma v,120,a,84,v,40
v_exp_f32 v31,v31
ma v,128,a,88,v,40
rd a,16,216,128
cv 47,30,31
ma v,136,a,92,v,40
rd a,20,216,6272
v_max3_f32 v240,v0,v1,v2
ma v,148,a,240,v,44
rd a,24,217,128
v_max3_f32 v241,v4,v5,v6
ma v,52,a,48,v,44
rd a,28,217,6272
v_max3_f32 v240,v240,v3,v8
ma v,60,a,52,v,44
rd a,32,216,256
v_max3_f32 v241,v241,v7,v12
ma v,68,a,56,v,44
rd a,36,216,6400
v_max3_f32 v240,v240,v9,v10
ma v,76,a,60,v,44
s_mov_b32 m0,s12
rd a,40,217,256
v_max3_f32 v241,v241,v13,v14
ma v,84,a,64,v,44
rd a,44,217,6400
v_max_f32 v240,v240,v11
ma v,92,a,68,v,44
dl 235,40,11,0
ma v,100,a,72,v,44
v_max_f32 v241,v241,v15
v_max_f32 v248,v240,v241
ma v,108,a,76,v,44
v_add_u32 v218,s94,v218
v_cmp_lt_f32 vcc,s87,v248
ma v,116,a,80,v,44
dl 236,40,11,1024
ma v,124,a,84,v,44
s_addk_i32 s12,6144
v_add_u32 v219,s94,v219
v_add_u32 v220,s94,v220
ma v,132,a,88,v,44
s_add_u32 s11,s11,s15
v_add_u32 v221,s94,v221
s_cmp_ge_u32 s70,4
wl 0
ma v,140,a,92,v,44
s_cselect_b32 s42,0,s42
s_cselect_b32 s12,s14,s12
s_cbranch_vccnz LA_a1i1_%=
LA_a1i1_ret_%=:
s_barrier
mc v,16,a,0,a,192,32
s_add_u32 s85,s85,s96
v_exp_f32 v0,v0
mc v,20,a,0,a,196,36
s_cmp_ge_u32 s85,s97
tr 48,218,0
tr 50,218,6144
mc v,24,a,4,a,192,32
s_cselect_b32 s93,s98,s96
tr 52,219,0
tr 54,219,6144
mc v,28,a,4,a,196,36
s_cselect_b32 s85,0,s85
v_exp_f32 v1,v1
ma v,16,a,8,a,200
tr 56,220,0
cv 40,0,1
ma v,20,a,8,a,204
)"
R"(tr 58,220,6144
v_add_u32 v216,s93,v216
ma v,24,a,12,a,200
s_add_u32 s86,s86,s96
v_exp_f32 v2,v2
ma v,28,a,12,a,204
s_cmp_ge_u32 s86,s97
tr 60,221,0
tr 62,221,6144
ma v,16,a,16,a,208
s_cselect_b32 s94,s98,s96
tr 64,218,128
tr 66,218,6272
ma v,20,a,16,a,212
s_cselect_b32 s86,0,s86
v_exp_f32 v3,v3
ma v,24,a,20,a,208
tr 68,219,128
cv 41,2,3
ma v,28,a,20,a,212
tr 70,219,6272
v_add_u32 v217,s93,v217
ma v,16,a,24,a,216
v_exp_f32 v8,v8
ma v,20,a,24,a,220
tr 72,220,128
tr 74,220,6272
ma v,24,a,28,a,216
tr 76,221,128
tr 78,221,6272
ma v,28,a,28,a,220
v_exp_f32 v9,v9
ma v,16,a,32,a,224
tr 80,218,256
cv 42,8,9
ma v,20,a,32,a,228
tr 82,218,6400
ma v,24,a,36,a,224
v_exp_f32 v10,v10
ma v,28,a,36,a,228
tr 84,219,256
tr 86,219,6400
ma v,16,a,40,a,232
tr 88,220,256
tr 90,220,6400
ma v,20,a,40,a,236
v_exp_f32 v11,v11
ma v,24,a,44,a,232
tr 92,221,256
cv 43,10,11
ma v,28,a,44,a,236
tr 94,221,6400
rd a,0,216,0
ma v,144,a,240,v,40
wl 15
v_exp_f32 v4,v4
ma v,48,a,48,v,40
v_exp_f32 v5,v5
ma v,56,a,52,v,40
rd a,4,216,6144
cv 44,4,5
ma v,64,a,56,v,40
wl 14
v_exp_f32 v6,v6
ma v,72,a,60,v,40
v_exp_f32 v7,v7
ma v,80,a,64,v,40
rd a,8,217,0
cv 45,6,7
ma v,88,a,68,v,40
wl 9
v_exp_f32 v12,v12
ma v,96,a,72,v,40
v_exp_f32 v13,v13
ma v,104,a,76,v,40
rd a,12,217,6144
cv 46,12,13
ma v,112,a,80,v,40
wl 4
v_exp_f32 v14,v14
ma v,120,a,84,v,40
v_exp_f32 v15,v15
ma v,128,a,88,v,40
rd a,16,216,128
cv 47,14,15
ma v,136,a,92,v,40
rd a,20,216,6272
v_max3_f32 v240,v16,v17,v18
ma v,148,a,240,v,44
rd a,24,217,128
v_max3_f32 v241,v20,v21,v22
ma v,52,a,48,v,44
rd a,28,217,6272
v_max3_f32 v240,v240,v19,v24
ma v,60,a,52,v,44
rd a,32,216,256
v_max3_f32 v241,v241,v23,v28
ma v,68,a,56,v,44
rd a,36,216,6400
v_max3_f32 v240,v240,v25,v26
ma v,76,a,60,v,44
s_mov_b32 m0,s12
rd a,40,217,256
v_max3_f32 v241,v241,v29,v30
ma v,84,a,64,v,44
rd a,44,217,6400
v_max_f32 v240,v240,v27
ma v,92,a,68,v,44
dl 235,40,11,0
ma v,100,a,72,v,44
v_max_f32 v241,v241,v31
v_max_f32 v248,v240,v241
ma v,108,a,76,v,44
v_add_u32 v218,s94,v218
v_cmp_lt_f32 vcc,s87,v248
ma v,116,a,80,v,44
dl 236,40,11,1024
ma v,124,a,84,v,44
s_addk_i32 s12,6144
v_add_u32 v219,s94,v219
v_add_u32 v220,s94,v220
ma v,132,a,88,v,44
s_add_u32 s11,s11,s15
v_add_u32 v221,s94,v221
s_cmp_ge_u32 s70,3
wl 0
ma v,140,a,92,v,44
s_cselect_b32 s42,0,s42
s_cselect_b32 s12,s14,s12
s_add_u32 s70,s70,1
s_cmp_lt_u32 s70,7
s_cbranch_vccnz LA_a1i2_%=
LA_a1i2_ret_%=:
s_barrier
s_cbranch_scc1 LA_a1top_%=
s_waitcnt vmcnt(0)
s_barrier
tr 48,218,0
tr 50,218,6144
tr 52,219,0
tr 54,219,6144
tr 56,220,0
tr 58,220,6144
tr 60,221,0
tr 62,221,6144
tr 64,218,128
tr 66,218,6272
tr 68,219,128
tr 70,219,6272
v_exp_f32 v16,v16
v_exp_f32 v17,v17
v_exp_f32 v18,v18
v_exp_f32 v19,v19
v_exp_f32 v24,v24
v_exp_f32 v25,v25
v_exp_f32 v26,v26
v_exp_f32 v27,v27
v_exp_f32 v20,v20
v_exp_f32 v21,v21
v_exp_f32 v22,v22
v_exp_f32 v23,v23
v_exp_f32 v28,v28
v_exp_f32 v29,v29
v_exp_f32 v30,v30
v_exp_f32 v31,v31
cv 40,16,17
cv 41,18,19
cv 42,24,25
cv 43,26,27
cv 44,20,21
cv 45,22,23
cv 46,28,29
cv 47,30,31
tr 72,220,128
tr 74,220,6272
tr 76,221,128
tr 78,221,6272
tr 80,218,256
tr 82,218,6400
tr 84,219,256
tr 86,219,6400
tr 88,220,256
tr 90,220,6400
tr 92,221,256
tr 94,221,6400
ma v,144,a,240,v,40
s_mov_b32 s42,0x78000
s_add_u32 m0,s75,0x18000
ma v,148,a,240,v,44
dl 235,40,72,0
wl 15
ma v,48,a,48,v,40
ma v,52,a,48,v,44
ma v,56,a,52,v,40
ma v,60,a,52,v,44
dl 236,40,72,1024
ma v,64,a,56,v,40
ma v,68,a,56,v,44
ma v,72,a,60,v,40
ma v,76,a,60,v,44
)"
R"(dl 237,40,72,2048
wl 14
ma v,80,a,64,v,40
ma v,84,a,64,v,44
s_add_u32 s72,s72,s7
s_add_u32 m0,s75,0x1e000
wl 12
ma v,88,a,68,v,40
ma v,92,a,68,v,44
dl 235,40,72,0
wl 10
ma v,96,a,72,v,40
ma v,100,a,72,v,44
wl 8
ma v,104,a,76,v,40
ma v,108,a,76,v,44
dl 236,40,72,1024
wl 6
ma v,112,a,80,v,40
ma v,116,a,80,v,44
wl 4
ma v,120,a,84,v,40
ma v,124,a,84,v,44
dl 237,40,72,2048
wl 2
ma v,128,a,88,v,40
s_sub_u32 s72,s72,s8
s_cmp_ge_u32 s72,s74
s_cselect_b32 s72,s73,s72
ma v,132,a,88,v,44
wl 0
ma v,136,a,92,v,40
ma v,140,a,92,v,44
tr 48,222,0
tr 50,222,2048
tr 52,223,0
tr 54,223,2048
tr 56,224,0
tr 58,224,2048
tr 60,225,0
tr 62,225,2048
tr 64,222,4096
tr 66,222,6144
tr 68,223,4096
tr 70,223,6144
tr 72,224,4096
tr 74,224,6144
tr 76,225,4096
tr 78,225,6144
s_barrier
wl 12
mz v,0,a,48,v,152
s_add_u32 m0,s75,0xc000
tr 80,222,8192
mz v,4,a,48,v,156
tr 82,222,10240
tr 84,223,8192
mz v,8,a,52,v,152
dl 235,40,72,0
mz v,12,a,52,v,156
wl 11
v_rcp_f32 v242,v144
mz v,16,a,56,v,152
v_rcp_f32 v244,v148
mz v,20,a,56,v,156
tr 86,223,10240
tr 88,224,8192
v_mul_f32 v48,v48,v242
v_mul_f32 v49,v49,v242
mz v,24,a,60,v,152
tr 90,224,10240
tr 92,225,8192
v_mul_f32 v50,v50,v242
v_mul_f32 v51,v51,v242
mz v,28,a,60,v,156
wl 11
tr 94,225,10240
tr 48,222,12288
cv 48,48,49
ma v,0,a,64,v,160
dl 236,40,72,1024
cv 49,50,51
v_mul_f32 v56,v56,v242
ma v,4,a,64,v,164
tr 50,222,14336
tr 52,223,12288
v_mul_f32 v57,v57,v242
v_mul_f32 v58,v58,v242
ma v,8,a,68,v,160
tr 54,223,14336
v_mul_f32 v59,v59,v242
cv 50,56,57
ma v,12,a,68,v,164
wl 12
tr 56,224,12288
cv 51,58,59
ma v,16,a,72,v,160
tr 58,224,14336
v_mul_f32 v52,v52,v244
v_mul_f32 v53,v53,v244
ma v,20,a,72,v,164
tr 60,225,12288
v_mul_f32 v54,v54,v244
v_mul_f32 v55,v55,v244
ma v,24,a,76,v,160
tr 62,225,14336
cv 52,52,53
ma v,28,a,76,v,164
wl 12
dl 237,40,72,2048
cv 53,54,55
v_mul_f32 v60,v60,v244
ma v,0,a,80,v,168
tr 64,222,16384
tr 66,222,18432
v_mul_f32 v61,v61,v244
v_mul_f32 v62,v62,v244
ma v,4,a,80,v,172
tr 68,223,16384
v_mul_f32 v63,v63,v244
cv 54,60,61
ma v,8,a,84,v,168
tr 70,223,18432
cv 55,62,63
ma v,12,a,84,v,172
wl 12
tr 72,224,16384
v_mul_f32 v64,v64,v242
v_mul_f32 v65,v65,v242
ma v,16,a,88,v,168
s_add_u32 s72,s72,s7
tr 74,224,18432
v_mul_f32 v66,v66,v242
v_mul_f32 v67,v67,v242
ma v,20,a,88,v,172
s_add_u32 m0,s75,0x12000
tr 76,225,16384
cv 64,64,65
ma v,24,a,92,v,168
dl 235,40,72,0
cv 65,66,67
v_mul_f32 v72,v72,v242
ma v,28,a,92,v,172
wl 11
tr 78,225,18432
tr 80,222,20480
v_mul_f32 v73,v73,v242
v_mul_f32 v74,v74,v242
ma v,0,a,48,v,176
tr 82,222,22528
v_mul_f32 v75,v75,v242
cv 66,72,73
ma v,4,a,48,v,180
tr 84,223,20480
cv 67,74,75
ma v,8,a,52,v,176
tr 86,223,22528
v_mul_f32 v68,v68,v244
v_mul_f32 v69,v69,v244
ma v,12,a,52,v,180
wl 12
tr 88,224,20480
v_mul_f32 v70,v70,v244
v_mul_f32 v71,v71,v244
ma v,16,a,56,v,176
tr 90,224,22528
cv 68,68,69
cv 69,70,71
ma v,20,a,56,v,180
dl 236,40,72,1024
v_mul_f32 v76,v76,v244
ma v,24,a,60,v,176
tr 92,225,20480
tr 94,225,22528
v_mul_f32 v77,v77,v244
v_mul_f32 v78,v78,v244
ma v,28,a,60,v,180
wl 12
tr 48,222,24576
v_mul_f32 v79,v79,v244
cv 70,76,77
ma v,0,a,64,v,184
tr 50,222,26624
cv 71,78,79
ma v,4,a,64,v,188
tr 52,223,24576
v_mul_f32 v80,v80,v242
v_mul_f32 v81,v81,v242
ma v,8,a,68,v,184
tr 54,223,26624
v_mul_f32 v82,v82,v242
v_mul_f32 v83,v83,v242
ma v,12,a,68,v,188
wl 12
tr 56,224,24576
cv 80,80,81
cv 81,82,83
ma v,16,a,72,v,184
dl 237,40,72,2048
v_mul_f32 v88,v88,v242
)"
R"(ma v,20,a,72,v,188
s_sub_u32 s72,s72,s8
tr 58,224,26624
tr 60,225,24576
v_mul_f32 v89,v89,v242
v_mul_f32 v90,v90,v242
ma v,24,a,76,v,184
s_cmp_ge_u32 s72,s74
tr 62,225,26624
v_mul_f32 v91,v91,v242
cv 82,88,89
ma v,28,a,76,v,188
s_cselect_b32 s72,s73,s72
tr 64,222,28672
cv 83,90,91
wl 13
ma v,0,a,80,v,192
tr 66,222,30720
v_mul_f32 v84,v84,v244
v_mul_f32 v85,v85,v244
ma v,4,a,80,v,196
tr 68,223,28672
v_mul_f32 v86,v86,v244
v_mul_f32 v87,v87,v244
ma v,8,a,84,v,192
tr 70,223,30720
cv 84,84,85
cv 85,86,87
ma v,12,a,84,v,196
wl 12
tr 72,224,28672
v_mul_f32 v92,v92,v244
ma v,16,a,88,v,192
tr 74,224,30720
v_mul_f32 v93,v93,v244
v_mul_f32 v94,v94,v244
ma v,20,a,88,v,196
tr 76,225,28672
v_mul_f32 v95,v95,v244
cv 86,92,93
ma v,24,a,92,v,192
wl 11
tr 78,225,30720
cv 87,94,95
ma v,28,a,92,v,196
s_waitcnt vmcnt(9)
s_barrier
ma v,0,a,48,v,48
s_mov_b32 m0,s75
tr 80,222,32768
tr 82,222,34816
v_mul_f32 v96,v96,v242
ma v,4,a,48,v,52
dl 235,40,72,0
v_mul_f32 v97,v97,v242
ma v,8,a,52,v,48
tr 84,223,32768
tr 86,223,34816
v_mul_f32 v98,v98,v242
v_mul_f32 v99,v99,v242
ma v,12,a,52,v,52
wl 12
tr 88,224,32768
cv 96,96,97
ma v,16,a,56,v,48
tr 90,224,34816
cv 97,98,99
v_mul_f32 v104,v104,v242
ma v,20,a,56,v,52
tr 92,225,32768
v_mul_f32 v105,v105,v242
v_mul_f32 v106,v106,v242
ma v,24,a,60,v,48
tr 94,225,34816
v_mul_f32 v107,v107,v242
ma v,28,a,60,v,52
wl 12
tr 48,222,36864
cv 98,104,105
cv 99,106,107
ma v,0,a,64,v,64
tr 50,222,38912
v_mul_f32 v100,v100,v244
ma v,4,a,64,v,68
tr 52,223,36864
v_mul_f32 v101,v101,v244
v_mul_f32 v102,v102,v244
ma v,8,a,68,v,64
tr 54,223,38912
v_mul_f32 v103,v103,v244
cv 100,100,101
ma v,12,a,68,v,68
wl 12
tr 56,224,36864
cv 101,102,103
ma v,16,a,72,v,64
tr 58,224,38912
v_mul_f32 v108,v108,v244
v_mul_f32 v109,v109,v244
ma v,20,a,72,v,68
tr 60,225,36864
v_mul_f32 v110,v110,v244
ma v,24,a,76,v,64
tr 62,225,38912
v_mul_f32 v111,v111,v244
cv 102,108,109
ma v,28,a,76,v,68
wl 12
dl 236,40,72,1024
cv 103,110,111
v_mul_f32 v112,v112,v242
ma v,0,a,80,v,80
tr 64,222,40960
tr 66,222,43008
v_mul_f32 v113,v113,v242
ma v,4,a,80,v,84
tr 68,223,40960
v_mul_f32 v114,v114,v242
v_mul_f32 v115,v115,v242
ma v,8,a,84,v,80
tr 70,223,43008
cv 112,112,113
cv 113,114,115
ma v,12,a,84,v,84
wl 12
tr 72,224,40960
v_mul_f32 v120,v120,v242
ma v,16,a,88,v,80
tr 74,224,43008
v_mul_f32 v121,v121,v242
v_mul_f32 v122,v122,v242
ma v,20,a,88,v,84
tr 76,225,40960
v_mul_f32 v123,v123,v242
ma v,24,a,92,v,80
tr 78,225,43008
cv 114,120,121
cv 115,122,123
ma v,28,a,92,v,84
wl 12
tr 80,222,45056
v_mul_f32 v116,v116,v244
v_mul_f32 v117,v117,v244
ma v,0,a,48,v,96
tr 82,222,47104
v_mul_f32 v118,v118,v244
ma v,4,a,48,v,100
tr 84,223,45056
v_mul_f32 v119,v119,v244
cv 116,116,117
ma v,8,a,52,v,96
tr 86,223,47104
cv 117,118,119
ma v,12,a,52,v,100
wl 12
tr 88,224,45056
v_mul_f32 v124,v124,v244
v_mul_f32 v125,v125,v244
ma v,16,a,56,v,96
tr 90,224,47104
v_mul_f32 v126,v126,v244
v_mul_f32 v127,v127,v244
ma v,20,a,56,v,100
dl 237,40,72,2048
cv 118,124,125
cv 119,126,127
ma v,24,a,60,v,96
s_add_u32 s72,s72,s7
tr 92,225,45056
tr 94,225,47104
v_mul_f32 v128,v128,v242
ma v,28,a,60,v,100
wl 12
v_add_u32 v222,0x18000,v222
v_add_u32 v223,0x18000,v223
v_mul_f32 v129,v129,v242
v_mul_f32 v130,v130,v242
ma v,0,a,64,v,112
v_add_u32 v224,0x18000,v224
v_add_u32 v225,0x18000,v225
v_mul_f32 v131,v131,v242
ma v,4,a,64,v,116
tr 48,222,0
tr 50,222,2048
cv 128,128,129
cv 129,130,131
ma v,8,a,68,v,112
tr 52,223,0
tr 54,223,2048
v_mul_f32 v136,v136,v242
ma v,12,a,68,v,116
wl 12
tr 56,224,0
v_mul_f32 v137,v137,v242
v_mul_f32 v138,v138,v242
ma v,16,a,72,v,112
tr 58,224,2048
v_mul_f32 v139,v139,v242
cv 130,136,137
ma v,20,a,72,v,116
)"
R"(tr 60,225,0
cv 131,138,139
v_mul_f32 v132,v132,v244
v_mul_f32 v133,v133,v244
v_mul_f32 v134,v134,v244
v_mul_f32 v135,v135,v244
cv 132,132,133
cv 133,134,135
v_mul_f32 v140,v140,v244
v_mul_f32 v141,v141,v244
v_mul_f32 v142,v142,v244
v_mul_f32 v143,v143,v244
cv 134,140,141
cv 135,142,143
ma v,24,a,76,v,112
tr 62,225,2048
ma v,28,a,76,v,116
wl 12
tr 64,222,4096
ma v,0,a,80,v,128
tr 66,222,6144
ma v,4,a,80,v,132
tr 68,223,4096
ma v,8,a,84,v,128
tr 70,223,6144
ma v,12,a,84,v,132
wl 12
tr 72,224,4096
ma v,16,a,88,v,128
tr 74,224,6144
ma v,20,a,88,v,132
tr 76,225,4096
ma v,24,a,92,v,128
wl 11
tr 78,225,6144
ma v,28,a,92,v,132
s_waitcnt vmcnt(9)
s_barrier
mz v,56,a,48,v,152
s_lshl_b32 s88,s66,7
tr 80,222,8192
tr 82,222,10240
mz v,60,a,48,v,156
tr 84,223,8192
v_mov_b32 v32,s88
mz v,72,a,52,v,152
tr 86,223,10240
v_mov_b32 v36,s56
mz v,76,a,52,v,156
wl 12
tr 88,224,8192
v_mov_b32 v37,s57
mz v,88,a,56,v,152
tr 90,224,10240
v_lshrrev_b32 v246,4,v239
cv 200,0,1
mz v,92,a,56,v,156
tr 92,225,8192
v_and_b32 v33,15,v246
cv 201,2,3
mz v,104,a,60,v,152
tr 94,225,10240
v_mul_u32_u24 v34,0x500,v33
cv 202,8,9
mz v,108,a,60,v,156
wl 12
tr 48,222,12288
v_lshlrev_b32 v33,7,v33
cv 203,10,11
ma v,56,a,64,v,160
tr 50,222,14336
v_lshrrev_b32 v246,4,v246
cv 204,16,17
ma v,60,a,64,v,164
tr 52,223,12288
v_lshl_add_u32 v34,v246,4,v34
cv 205,18,19
ma v,72,a,68,v,160
s_add_u32 m0,s75,0x6000
tr 54,223,14336
cv 206,24,25
ma v,76,a,68,v,164
wl 12
dl 235,40,72,0
cv 207,26,27
ma v,88,a,72,v,160
tr 56,224,12288
tr 58,224,14336
cv 208,4,5
ma v,92,a,72,v,164
dl 236,40,72,1024
cv 209,6,7
ma v,104,a,76,v,160
tr 60,225,12288
tr 62,225,14336
cv 210,12,13
ma v,108,a,76,v,164
wl 12
tr 64,222,16384
cv 211,14,15
ma v,56,a,80,v,168
tr 66,222,18432
cv 212,20,21
ma v,60,a,80,v,172
tr 68,223,16384
cv 213,22,23
ma v,72,a,84,v,168
dl 237,40,72,2048
cv 214,28,29
ma v,76,a,84,v,172
s_sub_u32 s72,s72,s8
tr 70,223,18432
tr 72,224,16384
wl 13
cv 215,30,31
ma v,88,a,88,v,168
s_cmp_ge_u32 s72,s74
tr 74,224,18432
s_cselect_b32 s72,s73,s72
ma v,92,a,88,v,172
tr 76,225,16384
ma v,104,a,92,v,168
tr 78,225,18432
ma v,108,a,92,v,172
wl 12
tr 80,222,20480
ma v,56,a,48,v,176
tr 82,222,22528
ma v,60,a,48,v,180
buffer_store_dwordx4 v[200:203],v239,s[44:47],s77 offen sc1
ma v,72,a,52,v,176
tr 84,223,20480
tr 86,223,22528
ma v,76,a,52,v,180
wl 12
tr 88,224,20480
ma v,88,a,56,v,176
tr 90,224,22528
ma v,92,a,56,v,180
tr 92,225,20480
ma v,104,a,60,v,176
buffer_store_dwordx4 v[204:207],v239,s[44:47],s77 offen offset:1024 sc1
ma v,108,a,60,v,180
wl 11
tr 94,225,22528
tr 48,222,24576
ma v,56,a,64,v,184
tr 50,222,26624
ma v,60,a,64,v,188
s_add_u32 s89,s77,s9
tr 52,223,24576
ma v,72,a,68,v,184
tr 54,223,26624
ma v,76,a,68,v,188
wl 12
buffer_store_dwordx4 v[208:211],v239,s[44:47],s89 offen sc1
ma v,88,a,72,v,184
tr 56,224,24576
tr 58,224,26624
ma v,92,a,72,v,188
tr 60,225,24576
ma v,104,a,76,v,184
tr 62,225,26624
ma v,108,a,76,v,188
wl 12
tr 64,222,28672
ma v,56,a,80,v,192
buffer_store_dwordx4 v[212:215],v239,s[44:47],s89 offen offset:1024 sc1
ma v,60,a,80,v,196
s_add_u32 s77,s77,s10
tr 66,222,30720
tr 68,223,28672
ma v,72,a,84,v,192
s_cmp_ge_u32 s77,s79
tr 70,223,30720
s_cselect_b32 s77,s78,s77
ma v,76,a,84,v,196
wl 12
tr 72,224,28672
ma v,88,a,88,v,192
tr 74,224,30720
ma v,92,a,88,v,196
tr 76,225,28672
ma v,104,a,92,v,192
wl 11
tr 78,225,30720
ma v,108,a,92,v,196
s_waitcnt vmcnt(13)
s_barrier
ma v,56,a,48,v,48
s_add_u32 s88,s64,1
tr 80,222,32768
tr 82,222,34816
ma v,60,a,48,v,52
s_and_b32 s88,s88,3
tr 84,223,32768
s_lshl_b32 s88,s88,4
ma v,72,a,52,v,48
s_add_u32 s88,s88,s66
tr 86,223,34816
)"
R"(s_lshr_b32 s89,s88,3
ma v,76,a,52,v,52
wl 12
tr 88,224,32768
s_and_b32 s91,s88,1
ma v,88,a,56,v,48
s_lshr_b32 s88,s88,1
tr 90,224,34816
s_and_b32 s88,s88,3
ma v,92,a,56,v,52
s_add_u32 s88,s88,s68
tr 92,225,32768
ma v,104,a,60,v,48
s_add_u32 s88,s88,0
tr 94,225,34816
ma v,108,a,60,v,52
wl 12
tr 48,222,36864
s_cmp_ge_u32 s88,10
ma v,56,a,64,v,64
s_cselect_b32 s80,10,0
tr 50,222,38912
s_sub_u32 s88,s88,s80
ma v,60,a,64,v,68
s_mul_i32 s80,s89,10
tr 52,223,36864
s_add_u32 s80,s80,s88
ma v,72,a,68,v,64
s_lshl_b32 s80,s80,5
tr 54,223,38912
ma v,76,a,68,v,68
wl 12
tr 56,224,36864
s_add_u32 s80,s80,s91
ma v,88,a,72,v,64
s_lshl_b32 s80,s80,10
tr 58,224,38912
s_mul_i32 s83,s89,0x5000
ma v,92,a,72,v,68
s_lshl_b32 s88,s88,1
tr 60,225,36864
ma v,104,a,76,v,64
s_add_u32 s88,s88,s91
tr 62,225,38912
s_lshl_b32 s88,s88,6
ma v,108,a,76,v,68
wl 12
tr 64,222,40960
s_add_u32 s83,s83,s88
ma v,56,a,80,v,80
s_add_u32 m0,s75,0x18000
tr 66,222,43008
ma v,60,a,80,v,84
dl 235,40,72,0
ma v,72,a,84,v,80
tr 68,223,40960
tr 70,223,43008
ma v,76,a,84,v,84
wl 12
tr 72,224,40960
ma v,88,a,88,v,80
tr 74,224,43008
ma v,92,a,88,v,84
tr 76,225,40960
ma v,104,a,92,v,80
tr 78,225,43008
ma v,108,a,92,v,84
wl 12
tr 80,222,45056
ma v,56,a,48,v,96
tr 82,222,47104
ma v,60,a,48,v,100
tr 84,223,45056
ma v,72,a,52,v,96
dl 236,40,72,1024
ma v,76,a,52,v,100
wl 11
tr 86,223,47104
tr 88,224,45056
ma v,88,a,56,v,96
tr 90,224,47104
ma v,92,a,56,v,100
tr 92,225,45056
ma v,104,a,60,v,96
tr 94,225,47104
v_add_u32 v222,0xffff4000,v222
ma v,108,a,60,v,100
wl 12
v_add_u32 v223,0xffff4000,v223
v_add_u32 v224,0xffff4000,v224
ma v,56,a,64,v,112
v_add_u32 v225,0xffff4000,v225
ma v,60,a,64,v,116
tr 48,222,0
tr 50,222,2048
ma v,72,a,68,v,112
tr 52,223,0
tr 54,223,2048
ma v,76,a,68,v,116
wl 12
dl 237,40,72,2048
ma v,88,a,72,v,112
s_add_u32 s72,s72,s7
tr 56,224,0
tr 58,224,2048
ma v,92,a,72,v,116
tr 60,225,0
ma v,104,a,76,v,112
tr 62,225,2048
ma v,108,a,76,v,116
wl 12
tr 64,222,4096
ma v,56,a,80,v,128
tr 66,222,6144
ma v,60,a,80,v,132
tr 68,223,4096
ma v,72,a,84,v,128
tr 70,223,6144
ma v,76,a,84,v,132
wl 12
tr 72,224,4096
ma v,88,a,88,v,128
tr 74,224,6144
ma v,92,a,88,v,132
tr 76,225,4096
ma v,104,a,92,v,128
wl 11
tr 78,225,6144
ma v,108,a,92,v,132
s_waitcnt vmcnt(13)
s_barrier
mz v,0,a,48,v,152
s_add_u32 m0,s75,0x1e000
tr 80,222,8192
mz v,4,a,48,v,156
tr 82,222,10240
tr 84,223,8192
mz v,8,a,52,v,152
dl 235,40,72,0
mz v,12,a,52,v,156
wl 11
tr 86,223,10240
tr 88,224,8192
mz v,16,a,56,v,152
tr 90,224,10240
cv 200,56,57
mz v,20,a,56,v,156
tr 92,225,8192
cv 201,58,59
mz v,24,a,60,v,152
tr 94,225,10240
cv 202,72,73
mz v,28,a,60,v,156
wl 12
tr 48,222,12288
cv 203,74,75
ma v,0,a,64,v,160
dl 236,40,72,1024
cv 204,88,89
ma v,4,a,64,v,164
tr 50,222,14336
tr 52,223,12288
cv 205,90,91
ma v,8,a,68,v,160
tr 54,223,14336
cv 206,104,105
ma v,12,a,68,v,164
wl 12
tr 56,224,12288
cv 207,106,107
ma v,16,a,72,v,160
tr 58,224,14336
cv 208,60,61
ma v,20,a,72,v,164
tr 60,225,12288
cv 209,62,63
ma v,24,a,76,v,160
tr 62,225,14336
cv 210,76,77
ma v,28,a,76,v,164
wl 12
tr 64,222,16384
cv 211,78,79
ma v,0,a,80,v,168
dl 237,40,72,2048
cv 212,92,93
ma v,4,a,80,v,172
s_sub_u32 s72,s72,s8
tr 66,222,18432
tr 68,223,16384
cv 213,94,95
ma v,8,a,84,v,168
s_cmp_ge_u32 s72,s74
tr 70,223,18432
cv 214,108,109
ma v,12,a,84,v,172
s_cselect_b32 s72,s73,s72
tr 72,224,16384
cv 215,110,111
wl 13
)"
R"(ma v,16,a,88,v,168
tr 74,224,18432
ma v,20,a,88,v,172
tr 76,225,16384
ma v,24,a,92,v,168
tr 78,225,18432
ma v,28,a,92,v,172
wl 12
tr 80,222,20480
ma v,0,a,48,v,176
tr 82,222,22528
ma v,4,a,48,v,180
buffer_store_dwordx4 v[200:203],v239,s[44:47],s77 offen sc1
ma v,8,a,52,v,176
tr 84,223,20480
tr 86,223,22528
ma v,12,a,52,v,180
wl 12
tr 88,224,20480
ma v,16,a,56,v,176
tr 90,224,22528
ma v,20,a,56,v,180
tr 92,225,20480
ma v,24,a,60,v,176
buffer_store_dwordx4 v[204:207],v239,s[44:47],s77 offen offset:1024 sc1
ma v,28,a,60,v,180
wl 11
tr 94,225,22528
tr 48,222,24576
ma v,0,a,64,v,184
tr 50,222,26624
ma v,4,a,64,v,188
s_add_u32 s89,s77,s9
tr 52,223,24576
ma v,8,a,68,v,184
tr 54,223,26624
ma v,12,a,68,v,188
wl 12
buffer_store_dwordx4 v[208:211],v239,s[44:47],s89 offen sc1
ma v,16,a,72,v,184
tr 56,224,24576
tr 58,224,26624
ma v,20,a,72,v,188
tr 60,225,24576
ma v,24,a,76,v,184
tr 62,225,26624
ma v,28,a,76,v,188
wl 12
tr 64,222,28672
ma v,0,a,80,v,192
buffer_store_dwordx4 v[212:215],v239,s[44:47],s89 offen offset:1024 sc1
ma v,4,a,80,v,196
s_add_u32 s77,s77,s10
tr 66,222,30720
tr 68,223,28672
ma v,8,a,84,v,192
s_cmp_ge_u32 s77,s79
tr 70,223,30720
s_cselect_b32 s77,s78,s77
ma v,12,a,84,v,196
wl 12
tr 72,224,28672
ma v,16,a,88,v,192
tr 74,224,30720
ma v,20,a,88,v,196
tr 76,225,28672
ma v,24,a,92,v,192
wl 11
tr 78,225,30720
ma v,28,a,92,v,196
s_waitcnt vmcnt(17)
s_barrier
ma v,0,a,48,v,48
s_add_u32 s88,s64,1
tr 80,222,32768
tr 82,222,34816
ma v,4,a,48,v,52
s_and_b32 s88,s88,3
tr 84,223,32768
s_lshl_b32 s88,s88,4
ma v,8,a,52,v,48
s_add_u32 s88,s88,s66
tr 86,223,34816
s_lshr_b32 s89,s88,2
ma v,12,a,52,v,52
wl 12
tr 88,224,32768
s_and_b32 s91,s88,1
ma v,16,a,56,v,48
s_lshr_b32 s88,s88,1
tr 90,224,34816
s_and_b32 s88,s88,1
ma v,20,a,56,v,52
s_add_u32 s88,s88,s68
tr 92,225,32768
ma v,24,a,60,v,48
s_add_u32 s88,s88,4
tr 94,225,34816
ma v,28,a,60,v,52
wl 12
tr 48,222,36864
s_cmp_ge_u32 s88,10
ma v,0,a,64,v,64
s_cselect_b32 s81,10,0
tr 50,222,38912
s_sub_u32 s88,s88,s81
ma v,4,a,64,v,68
s_mul_i32 s81,s89,10
tr 52,223,36864
s_add_u32 s81,s81,s88
ma v,8,a,68,v,64
s_lshl_b32 s81,s81,5
tr 54,223,38912
ma v,12,a,68,v,68
wl 12
tr 56,224,36864
s_add_u32 s81,s81,s91
ma v,16,a,72,v,64
s_lshl_b32 s81,s81,10
tr 58,224,38912
s_mul_i32 s84,s89,0x5000
ma v,20,a,72,v,68
s_lshl_b32 s88,s88,1
tr 60,225,36864
ma v,24,a,76,v,64
s_add_u32 s88,s88,s91
tr 62,225,38912
s_lshl_b32 s88,s88,6
ma v,28,a,76,v,68
wl 12
tr 64,222,40960
s_add_u32 s84,s84,s88
ma v,0,a,80,v,80
s_add_u32 m0,s75,0xc000
tr 66,222,43008
ma v,4,a,80,v,84
dl 235,40,72,0
ma v,8,a,84,v,80
tr 68,223,40960
tr 70,223,43008
ma v,12,a,84,v,84
wl 12
tr 72,224,40960
ma v,16,a,88,v,80
tr 74,224,43008
ma v,20,a,88,v,84
tr 76,225,40960
ma v,24,a,92,v,80
tr 78,225,43008
ma v,28,a,92,v,84
wl 12
tr 80,222,45056
ma v,0,a,48,v,96
tr 82,222,47104
ma v,4,a,48,v,100
tr 84,223,45056
ma v,8,a,52,v,96
dl 236,40,72,1024
ma v,12,a,52,v,100
wl 11
tr 86,223,47104
tr 88,224,45056
ma v,16,a,56,v,96
tr 90,224,47104
ma v,20,a,56,v,100
tr 92,225,45056
ma v,24,a,60,v,96
tr 94,225,47104
v_add_u32 v222,0xffff4000,v222
ma v,28,a,60,v,100
wl 12
v_add_u32 v223,0xffff4000,v223
v_add_u32 v224,0xffff4000,v224
ma v,0,a,64,v,112
v_add_u32 v225,0xffff4000,v225
ma v,4,a,64,v,116
tr 48,222,0
tr 50,222,2048
ma v,8,a,68,v,112
tr 52,223,0
tr 54,223,2048
ma v,12,a,68,v,116
wl 12
dl 237,40,72,2048
ma v,16,a,72,v,112
s_add_u32 s72,s72,s7
tr 56,224,0
tr 58,224,2048
ma v,20,a,72,v,116
tr 60,225,0
ma v,24,a,76,v,112
tr 62,225,2048
ma v,28,a,76,v,116
wl 12
tr 64,222,4096
ma v,0,a,80,v,128
tr 66,222,6144
ma v,4,a,80,v,132
tr 68,223,4096
ma v,8,a,84,v,128
)"
R"(tr 70,223,6144
ma v,12,a,84,v,132
wl 12
tr 72,224,4096
ma v,16,a,88,v,128
tr 74,224,6144
ma v,20,a,88,v,132
tr 76,225,4096
ma v,24,a,92,v,128
wl 11
tr 78,225,6144
ma v,28,a,92,v,132
s_waitcnt vmcnt(17)
s_barrier
mz v,56,a,48,v,152
s_add_u32 m0,s75,0x12000
tr 80,222,8192
mz v,60,a,48,v,156
tr 82,222,10240
tr 84,223,8192
mz v,72,a,52,v,152
dl 235,40,72,0
mz v,76,a,52,v,156
wl 11
tr 86,223,10240
tr 88,224,8192
mz v,88,a,56,v,152
tr 90,224,10240
cv 200,0,1
mz v,92,a,56,v,156
tr 92,225,8192
cv 201,2,3
mz v,104,a,60,v,152
tr 94,225,10240
cv 202,8,9
mz v,108,a,60,v,156
wl 12
tr 48,222,12288
cv 203,10,11
ma v,56,a,64,v,160
dl 236,40,72,1024
cv 204,16,17
ma v,60,a,64,v,164
tr 50,222,14336
tr 52,223,12288
cv 205,18,19
ma v,72,a,68,v,160
tr 54,223,14336
cv 206,24,25
ma v,76,a,68,v,164
wl 12
tr 56,224,12288
cv 207,26,27
ma v,88,a,72,v,160
tr 58,224,14336
cv 208,4,5
ma v,92,a,72,v,164
tr 60,225,12288
cv 209,6,7
ma v,104,a,76,v,160
tr 62,225,14336
cv 210,12,13
ma v,108,a,76,v,164
wl 12
tr 64,222,16384
cv 211,14,15
ma v,56,a,80,v,168
dl 237,40,72,2048
cv 212,20,21
ma v,60,a,80,v,172
s_sub_u32 s72,s72,s8
tr 66,222,18432
tr 68,223,16384
cv 213,22,23
ma v,72,a,84,v,168
s_cmp_ge_u32 s72,s74
tr 70,223,18432
cv 214,28,29
ma v,76,a,84,v,172
s_cselect_b32 s72,s73,s72
tr 72,224,16384
cv 215,30,31
wl 13
ma v,88,a,88,v,168
tr 74,224,18432
ma v,92,a,88,v,172
tr 76,225,16384
ma v,104,a,92,v,168
tr 78,225,18432
ma v,108,a,92,v,172
wl 12
tr 80,222,20480
ma v,56,a,48,v,176
tr 82,222,22528
ma v,60,a,48,v,180
buffer_store_dwordx4 v[200:203],v239,s[44:47],s77 offen sc1
ma v,72,a,52,v,176
tr 84,223,20480
tr 86,223,22528
ma v,76,a,52,v,180
wl 12
tr 88,224,20480
ma v,88,a,56,v,176
tr 90,224,22528
ma v,92,a,56,v,180
tr 92,225,20480
ma v,104,a,60,v,176
buffer_store_dwordx4 v[204:207],v239,s[44:47],s77 offen offset:1024 sc1
ma v,108,a,60,v,180
wl 11
tr 94,225,22528
tr 48,222,24576
ma v,56,a,64,v,184
tr 50,222,26624
ma v,60,a,64,v,188
s_add_u32 s89,s77,s9
tr 52,223,24576
ma v,72,a,68,v,184
tr 54,223,26624
ma v,76,a,68,v,188
wl 12
buffer_store_dwordx4 v[208:211],v239,s[44:47],s89 offen sc1
ma v,88,a,72,v,184
tr 56,224,24576
tr 58,224,26624
ma v,92,a,72,v,188
tr 60,225,24576
ma v,104,a,76,v,184
tr 62,225,26624
ma v,108,a,76,v,188
wl 12
tr 64,222,28672
ma v,56,a,80,v,192
buffer_store_dwordx4 v[212:215],v239,s[44:47],s89 offen offset:1024 sc1
ma v,60,a,80,v,196
s_add_u32 s77,s77,s10
tr 66,222,30720
tr 68,223,28672
ma v,72,a,84,v,192
s_cmp_ge_u32 s77,s79
tr 70,223,30720
s_cselect_b32 s77,s78,s77
ma v,76,a,84,v,196
wl 12
tr 72,224,28672
ma v,88,a,88,v,192
tr 74,224,30720
ma v,92,a,88,v,196
tr 76,225,28672
ma v,104,a,92,v,192
wl 11
tr 78,225,30720
ma v,108,a,92,v,196
s_waitcnt vmcnt(17)
s_barrier
ma v,56,a,48,v,48
s_add_u32 s88,s64,1
tr 80,222,32768
tr 82,222,34816
ma v,60,a,48,v,52
s_and_b32 s88,s88,3
tr 84,223,32768
s_lshl_b32 s88,s88,4
ma v,72,a,52,v,48
s_add_u32 s88,s88,s66
tr 86,223,34816
s_lshr_b32 s89,s88,2
ma v,76,a,52,v,52
wl 12
tr 88,224,32768
s_and_b32 s91,s88,1
ma v,88,a,56,v,48
s_lshr_b32 s88,s88,1
tr 90,224,34816
s_and_b32 s88,s88,1
ma v,92,a,56,v,52
s_add_u32 s88,s88,s68
tr 92,225,32768
ma v,104,a,60,v,48
s_add_u32 s88,s88,6
tr 94,225,34816
ma v,108,a,60,v,52
wl 12
tr 48,222,36864
s_cmp_ge_u32 s88,10
ma v,56,a,64,v,64
s_cselect_b32 s82,10,0
tr 50,222,38912
s_sub_u32 s88,s88,s82
ma v,60,a,64,v,68
s_mul_i32 s82,s89,10
tr 52,223,36864
s_add_u32 s82,s82,s88
ma v,72,a,68,v,64
s_lshl_b32 s82,s82,5
tr 54,223,38912
ma v,76,a,68,v,68
wl 12
tr 56,224,36864
s_add_u32 s82,s82,s91
)"
R"(ma v,88,a,72,v,64
s_lshl_b32 s82,s82,10
tr 58,224,38912
s_mul_i32 s85,s89,0x5000
ma v,92,a,72,v,68
s_lshl_b32 s88,s88,1
tr 60,225,36864
ma v,104,a,76,v,64
s_add_u32 s88,s88,s91
tr 62,225,38912
s_lshl_b32 s88,s88,6
ma v,108,a,76,v,68
wl 12
tr 64,222,40960
s_add_u32 s85,s85,s88
ma v,56,a,80,v,80
s_mov_b32 m0,s75
tr 66,222,43008
ma v,60,a,80,v,84
dl 235,40,72,0
ma v,72,a,84,v,80
tr 68,223,40960
tr 70,223,43008
ma v,76,a,84,v,84
wl 12
tr 72,224,40960
ma v,88,a,88,v,80
tr 74,224,43008
ma v,92,a,88,v,84
tr 76,225,40960
ma v,104,a,92,v,80
tr 78,225,43008
ma v,108,a,92,v,84
wl 12
tr 80,222,45056
ma v,56,a,48,v,96
tr 82,222,47104
ma v,60,a,48,v,100
tr 84,223,45056
ma v,72,a,52,v,96
dl 236,40,72,1024
ma v,76,a,52,v,100
wl 11
tr 86,223,47104
tr 88,224,45056
ma v,88,a,56,v,96
tr 90,224,47104
ma v,92,a,56,v,100
tr 92,225,45056
ma v,104,a,60,v,96
tr 94,225,47104
v_add_u32 v222,0x18000,v222
ma v,108,a,60,v,100
wl 12
v_add_u32 v223,0x18000,v223
v_add_u32 v224,0x18000,v224
ma v,56,a,64,v,112
v_add_u32 v225,0x18000,v225
ma v,60,a,64,v,116
tr 48,222,0
tr 50,222,2048
ma v,72,a,68,v,112
tr 52,223,0
tr 54,223,2048
ma v,76,a,68,v,116
wl 12
dl 237,40,72,2048
ma v,88,a,72,v,112
s_add_u32 s72,s72,s7
tr 56,224,0
tr 58,224,2048
ma v,92,a,72,v,116
tr 60,225,0
ma v,104,a,76,v,112
tr 62,225,2048
ma v,108,a,76,v,116
wl 12
tr 64,222,4096
ma v,56,a,80,v,128
tr 66,222,6144
ma v,60,a,80,v,132
tr 68,223,4096
ma v,72,a,84,v,128
tr 70,223,6144
ma v,76,a,84,v,132
wl 12
tr 72,224,4096
ma v,88,a,88,v,128
tr 74,224,6144
ma v,92,a,88,v,132
tr 76,225,4096
ma v,104,a,92,v,128
wl 11
tr 78,225,6144
ma v,108,a,92,v,132
s_waitcnt vmcnt(17)
s_barrier
mz v,0,a,48,v,152
s_add_u32 m0,s75,0x6000
tr 80,222,8192
mz v,4,a,48,v,156
tr 82,222,10240
tr 84,223,8192
mz v,8,a,52,v,152
dl 235,40,72,0
mz v,12,a,52,v,156
wl 11
tr 86,223,10240
tr 88,224,8192
mz v,16,a,56,v,152
tr 90,224,10240
cv 200,56,57
mz v,20,a,56,v,156
tr 92,225,8192
cv 201,58,59
mz v,24,a,60,v,152
tr 94,225,10240
cv 202,72,73
mz v,28,a,60,v,156
wl 12
tr 48,222,12288
cv 203,74,75
ma v,0,a,64,v,160
dl 236,40,72,1024
cv 204,88,89
ma v,4,a,64,v,164
tr 50,222,14336
tr 52,223,12288
cv 205,90,91
ma v,8,a,68,v,160
tr 54,223,14336
cv 206,104,105
ma v,12,a,68,v,164
wl 12
tr 56,224,12288
cv 207,106,107
ma v,16,a,72,v,160
tr 58,224,14336
cv 208,60,61
ma v,20,a,72,v,164
tr 60,225,12288
cv 209,62,63
ma v,24,a,76,v,160
tr 62,225,14336
cv 210,76,77
ma v,28,a,76,v,164
wl 12
tr 64,222,16384
cv 211,78,79
ma v,0,a,80,v,168
dl 237,40,72,2048
cv 212,92,93
ma v,4,a,80,v,172
s_sub_u32 s72,s72,s8
tr 66,222,18432
tr 68,223,16384
cv 213,94,95
ma v,8,a,84,v,168
s_cmp_ge_u32 s72,s74
tr 70,223,18432
cv 214,108,109
ma v,12,a,84,v,172
s_cselect_b32 s72,s73,s72
tr 72,224,16384
cv 215,110,111
wl 13
ma v,16,a,88,v,168
tr 74,224,18432
ma v,20,a,88,v,172
tr 76,225,16384
ma v,24,a,92,v,168
tr 78,225,18432
ma v,28,a,92,v,172
wl 12
tr 80,222,20480
ma v,0,a,48,v,176
tr 82,222,22528
ma v,4,a,48,v,180
buffer_store_dwordx4 v[200:203],v239,s[44:47],s77 offen sc1
ma v,8,a,52,v,176
tr 84,223,20480
tr 86,223,22528
ma v,12,a,52,v,180
wl 12
tr 88,224,20480
ma v,16,a,56,v,176
tr 90,224,22528
ma v,20,a,56,v,180
tr 92,225,20480
ma v,24,a,60,v,176
buffer_store_dwordx4 v[204:207],v239,s[44:47],s77 offen offset:1024 sc1
ma v,28,a,60,v,180
wl 11
tr 94,225,22528
tr 48,222,24576
ma v,0,a,64,v,184
tr 50,222,26624
ma v,4,a,64,v,188
s_add_u32 s89,s77,s9
tr 52,223,24576
)"
R"(ma v,8,a,68,v,184
tr 54,223,26624
ma v,12,a,68,v,188
wl 12
buffer_store_dwordx4 v[208:211],v239,s[44:47],s89 offen sc1
ma v,16,a,72,v,184
tr 56,224,24576
tr 58,224,26624
ma v,20,a,72,v,188
tr 60,225,24576
ma v,24,a,76,v,184
tr 62,225,26624
ma v,28,a,76,v,188
wl 12
tr 64,222,28672
ma v,0,a,80,v,192
buffer_store_dwordx4 v[212:215],v239,s[44:47],s89 offen offset:1024 sc1
ma v,4,a,80,v,196
s_add_u32 s77,s77,s10
tr 66,222,30720
tr 68,223,28672
ma v,8,a,84,v,192
s_cmp_ge_u32 s77,s79
tr 70,223,30720
s_cselect_b32 s77,s78,s77
ma v,12,a,84,v,196
wl 12
tr 72,224,28672
ma v,16,a,88,v,192
tr 74,224,30720
ma v,20,a,88,v,196
tr 76,225,28672
ma v,24,a,92,v,192
wl 11
tr 78,225,30720
ma v,28,a,92,v,196
s_waitcnt vmcnt(0)
s_barrier
s_cmp_lg_u32 s64,0
s_cbranch_scc1 LA_nf0_%=
s_mov_b64 exec,1
global_store_dwordx2 v32,v[36:37],s[52:53] offset:0 sc1
s_mov_b64 exec,-1
LA_nf0_%=:
ma v,0,a,48,v,48
s_add_u32 s88,s64,1
tr 80,222,32768
tr 82,222,34816
ma v,4,a,48,v,52
s_and_b32 s88,s88,3
tr 84,223,32768
s_lshl_b32 s88,s88,4
ma v,8,a,52,v,48
s_add_u32 s88,s88,s66
tr 86,223,34816
s_lshr_b32 s89,s88,2
ma v,12,a,52,v,52
wl 12
tr 88,224,32768
s_and_b32 s91,s88,1
ma v,16,a,56,v,48
s_lshr_b32 s88,s88,1
tr 90,224,34816
s_and_b32 s88,s88,1
ma v,20,a,56,v,52
s_add_u32 s88,s88,s68
tr 92,225,32768
ma v,24,a,60,v,48
s_add_u32 s88,s88,8
tr 94,225,34816
ma v,28,a,60,v,52
wl 12
tr 48,222,36864
s_cmp_ge_u32 s88,10
ma v,0,a,64,v,64
s_cselect_b32 s96,10,0
tr 50,222,38912
s_sub_u32 s88,s88,s96
ma v,4,a,64,v,68
s_mul_i32 s96,s89,10
tr 52,223,36864
s_add_u32 s96,s96,s88
ma v,8,a,68,v,64
s_lshl_b32 s96,s96,5
tr 54,223,38912
ma v,12,a,68,v,68
wl 12
tr 56,224,36864
s_add_u32 s96,s96,s91
ma v,16,a,72,v,64
s_lshl_b32 s96,s96,10
tr 58,224,38912
s_mul_i32 s97,s89,0x5000
ma v,20,a,72,v,68
s_lshl_b32 s88,s88,1
tr 60,225,36864
ma v,24,a,76,v,64
s_add_u32 s88,s88,s91
tr 62,225,38912
s_lshl_b32 s88,s88,6
ma v,28,a,76,v,68
wl 12
tr 64,222,40960
s_add_u32 s97,s97,s88
ma v,0,a,80,v,80
s_add_u32 m0,s75,0x18000
tr 66,222,43008
ma v,4,a,80,v,84
dl 235,40,72,0
ma v,8,a,84,v,80
tr 68,223,40960
tr 70,223,43008
ma v,12,a,84,v,84
wl 12
tr 72,224,40960
ma v,16,a,88,v,80
tr 74,224,43008
ma v,20,a,88,v,84
tr 76,225,40960
ma v,24,a,92,v,80
tr 78,225,43008
ma v,28,a,92,v,84
wl 12
tr 80,222,45056
ma v,0,a,48,v,96
tr 82,222,47104
ma v,4,a,48,v,100
tr 84,223,45056
ma v,8,a,52,v,96
dl 236,40,72,1024
ma v,12,a,52,v,100
wl 11
tr 86,223,47104
tr 88,224,45056
ma v,16,a,56,v,96
tr 90,224,47104
ma v,20,a,56,v,100
tr 92,225,45056
ma v,24,a,60,v,96
tr 94,225,47104
v_add_u32 v222,0xffff4000,v222
ma v,28,a,60,v,100
wl 12
v_add_u32 v223,0xffff4000,v223
v_add_u32 v224,0xffff4000,v224
ma v,0,a,64,v,112
v_add_u32 v225,0xffff4000,v225
ma v,4,a,64,v,116
tr 48,222,0
tr 50,222,2048
ma v,8,a,68,v,112
tr 52,223,0
tr 54,223,2048
ma v,12,a,68,v,116
wl 12
dl 237,40,72,2048
ma v,16,a,72,v,112
s_add_u32 s72,s72,s7
tr 56,224,0
tr 58,224,2048
ma v,20,a,72,v,116
tr 60,225,0
ma v,24,a,76,v,112
tr 62,225,2048
ma v,28,a,76,v,116
wl 12
tr 64,222,4096
ma v,0,a,80,v,128
tr 66,222,6144
ma v,4,a,80,v,132
tr 68,223,4096
ma v,8,a,84,v,128
tr 70,223,6144
ma v,12,a,84,v,132
wl 12
tr 72,224,4096
ma v,16,a,88,v,128
tr 74,224,6144
ma v,20,a,88,v,132
tr 76,225,4096
ma v,24,a,92,v,128
wl 11
tr 78,225,6144
ma v,28,a,92,v,132
s_waitcnt vmcnt(17)
s_barrier
mz v,56,a,48,v,152
s_add_u32 m0,s75,0x1e000
tr 80,222,8192
mz v,60,a,48,v,156
tr 82,222,10240
tr 84,223,8192
mz v,72,a,52,v,152
dl 235,40,72,0
mz v,76,a,52,v,156
wl 11
tr 86,223,10240
tr 88,224,8192
mz v,88,a,56,v,152
tr 90,224,10240
)"
R"(cv 200,0,1
mz v,92,a,56,v,156
tr 92,225,8192
cv 201,2,3
mz v,104,a,60,v,152
tr 94,225,10240
cv 202,8,9
mz v,108,a,60,v,156
wl 12
tr 48,222,12288
cv 203,10,11
ma v,56,a,64,v,160
dl 236,40,72,1024
cv 204,16,17
ma v,60,a,64,v,164
tr 50,222,14336
tr 52,223,12288
cv 205,18,19
ma v,72,a,68,v,160
tr 54,223,14336
cv 206,24,25
ma v,76,a,68,v,164
wl 12
tr 56,224,12288
cv 207,26,27
ma v,88,a,72,v,160
tr 58,224,14336
cv 208,4,5
ma v,92,a,72,v,164
tr 60,225,12288
cv 209,6,7
ma v,104,a,76,v,160
tr 62,225,14336
cv 210,12,13
ma v,108,a,76,v,164
wl 12
tr 64,222,16384
cv 211,14,15
ma v,56,a,80,v,168
dl 237,40,72,2048
cv 212,20,21
ma v,60,a,80,v,172
s_sub_u32 s72,s72,s8
tr 66,222,18432
tr 68,223,16384
cv 213,22,23
ma v,72,a,84,v,168
s_cmp_ge_u32 s72,s74
tr 70,223,18432
cv 214,28,29
ma v,76,a,84,v,172
s_cselect_b32 s72,s73,s72
tr 72,224,16384
cv 215,30,31
wl 13
ma v,88,a,88,v,168
tr 74,224,18432
ma v,92,a,88,v,172
tr 76,225,16384
ma v,104,a,92,v,168
tr 78,225,18432
ma v,108,a,92,v,172
wl 12
tr 80,222,20480
ma v,56,a,48,v,176
tr 82,222,22528
ma v,60,a,48,v,180
buffer_store_dwordx4 v[200:203],v239,s[44:47],s77 offen sc1
ma v,72,a,52,v,176
tr 84,223,20480
tr 86,223,22528
ma v,76,a,52,v,180
wl 12
tr 88,224,20480
ma v,88,a,56,v,176
tr 90,224,22528
ma v,92,a,56,v,180
tr 92,225,20480
ma v,104,a,60,v,176
buffer_store_dwordx4 v[204:207],v239,s[44:47],s77 offen offset:1024 sc1
ma v,108,a,60,v,180
wl 11
tr 94,225,22528
tr 48,222,24576
ma v,56,a,64,v,184
tr 50,222,26624
ma v,60,a,64,v,188
s_add_u32 s89,s77,s9
tr 52,223,24576
ma v,72,a,68,v,184
tr 54,223,26624
ma v,76,a,68,v,188
wl 12
buffer_store_dwordx4 v[208:211],v239,s[44:47],s89 offen sc1
ma v,88,a,72,v,184
tr 56,224,24576
tr 58,224,26624
ma v,92,a,72,v,188
tr 60,225,24576
ma v,104,a,76,v,184
tr 62,225,26624
ma v,108,a,76,v,188
wl 12
tr 64,222,28672
ma v,56,a,80,v,192
buffer_store_dwordx4 v[212:215],v239,s[44:47],s89 offen offset:1024 sc1
ma v,60,a,80,v,196
s_add_u32 s77,s77,s10
tr 66,222,30720
tr 68,223,28672
ma v,72,a,84,v,192
s_cmp_ge_u32 s77,s79
tr 70,223,30720
s_cselect_b32 s77,s78,s77
ma v,76,a,84,v,196
wl 12
tr 72,224,28672
ma v,88,a,88,v,192
tr 74,224,30720
ma v,92,a,88,v,196
tr 76,225,28672
ma v,104,a,92,v,192
wl 11
tr 78,225,30720
ma v,108,a,92,v,196
s_waitcnt vmcnt(17)
s_barrier
ma v,56,a,48,v,48
s_add_u32 m0,s75,0xc000
tr 80,222,32768
ma v,60,a,48,v,52
tr 82,222,34816
tr 84,223,32768
ma v,72,a,52,v,48
dl 235,40,72,0
ma v,76,a,52,v,52
wl 11
tr 86,223,34816
tr 88,224,32768
ma v,88,a,56,v,48
tr 90,224,34816
ma v,92,a,56,v,52
tr 92,225,32768
ma v,104,a,60,v,48
tr 94,225,34816
ma v,108,a,60,v,52
wl 12
tr 48,222,36864
ma v,56,a,64,v,64
tr 50,222,38912
ma v,60,a,64,v,68
tr 52,223,36864
ma v,72,a,68,v,64
tr 54,223,38912
ma v,76,a,68,v,68
wl 12
tr 56,224,36864
ma v,88,a,72,v,64
tr 58,224,38912
ma v,92,a,72,v,68
tr 60,225,36864
ma v,104,a,76,v,64
tr 62,225,38912
ma v,108,a,76,v,68
wl 12
dl 236,40,72,1024
ma v,56,a,80,v,80
tr 64,222,40960
tr 66,222,43008
ma v,60,a,80,v,84
tr 68,223,40960
ma v,72,a,84,v,80
tr 70,223,43008
ma v,76,a,84,v,84
wl 12
tr 72,224,40960
ma v,88,a,88,v,80
tr 74,224,43008
ma v,92,a,88,v,84
tr 76,225,40960
ma v,104,a,92,v,80
tr 78,225,43008
ma v,108,a,92,v,84
wl 12
tr 80,222,45056
ma v,56,a,48,v,96
tr 82,222,47104
ma v,60,a,48,v,100
tr 84,223,45056
ma v,72,a,52,v,96
tr 86,223,47104
ma v,76,a,52,v,100
wl 12
tr 88,224,45056
ma v,88,a,56,v,96
tr 90,224,47104
ma v,92,a,56,v,100
dl 237,40,72,2048
ma v,104,a,60,v,96
s_add_u32 s72,s72,s7
)"
R"(tr 92,225,45056
tr 94,225,47104
ma v,108,a,60,v,100
wl 12
v_add_u32 v222,0xffff4000,v222
v_add_u32 v223,0xffff4000,v223
ma v,56,a,64,v,112
v_add_u32 v224,0xffff4000,v224
v_add_u32 v225,0xffff4000,v225
ma v,60,a,64,v,116
tr 48,222,0
tr 50,222,2048
ma v,72,a,68,v,112
tr 52,223,0
tr 54,223,2048
ma v,76,a,68,v,116
wl 12
tr 56,224,0
ma v,88,a,72,v,112
tr 58,224,2048
ma v,92,a,72,v,116
tr 60,225,0
ma v,104,a,76,v,112
tr 62,225,2048
ma v,108,a,76,v,116
wl 12
tr 64,222,4096
ma v,56,a,80,v,128
tr 66,222,6144
ma v,60,a,80,v,132
tr 68,223,4096
ma v,72,a,84,v,128
tr 70,223,6144
ma v,76,a,84,v,132
wl 12
tr 72,224,4096
ma v,88,a,88,v,128
tr 74,224,6144
ma v,92,a,88,v,132
tr 76,225,4096
ma v,104,a,92,v,128
wl 11
tr 78,225,6144
ma v,108,a,92,v,132
s_waitcnt vmcnt(17)
s_barrier
mz v,0,a,48,v,152
s_add_u32 m0,s75,0x12000
tr 80,222,8192
mz v,4,a,48,v,156
tr 82,222,10240
tr 84,223,8192
mz v,8,a,52,v,152
dl 235,40,72,0
mz v,12,a,52,v,156
wl 11
tr 86,223,10240
tr 88,224,8192
mz v,16,a,56,v,152
tr 90,224,10240
cv 200,56,57
mz v,20,a,56,v,156
dl 236,40,72,1024
cv 201,58,59
mz v,24,a,60,v,152
tr 92,225,8192
tr 94,225,10240
cv 202,72,73
mz v,28,a,60,v,156
wl 12
tr 48,222,12288
cv 203,74,75
ma v,0,a,64,v,160
tr 50,222,14336
cv 204,88,89
ma v,4,a,64,v,164
tr 52,223,12288
cv 205,90,91
ma v,8,a,68,v,160
dl 237,40,72,2048
cv 206,104,105
ma v,12,a,68,v,164
wl 11
tr 54,223,14336
tr 56,224,12288
cv 207,106,107
ma v,16,a,72,v,160
s_sub_u32 s72,s72,s8
tr 58,224,14336
cv 208,60,61
ma v,20,a,72,v,164
s_cmp_ge_u32 s72,s74
tr 60,225,12288
cv 209,62,63
ma v,24,a,76,v,160
s_cselect_b32 s72,s73,s72
tr 62,225,14336
cv 210,76,77
ma v,28,a,76,v,164
wl 12
buffer_load_dwordx4 a[96:99],v239,s[44:47],s80 offen sc1
cv 211,78,79
ma v,0,a,80,v,168
tr 64,222,16384
tr 66,222,18432
cv 212,92,93
ma v,4,a,80,v,172
tr 68,223,16384
cv 213,94,95
ma v,8,a,84,v,168
tr 70,223,18432
cv 214,108,109
ma v,12,a,84,v,172
wl 12
buffer_load_dwordx4 a[100:103],v239,s[44:47],s80 offen offset:2048 sc1
cv 215,110,111
ma v,16,a,88,v,168
s_addk_i32 s80,4096
tr 72,224,16384
tr 74,224,18432
ma v,20,a,88,v,172
tr 76,225,16384
ma v,24,a,92,v,168
tr 78,225,18432
ma v,28,a,92,v,172
wl 12
tr 80,222,20480
ma v,0,a,48,v,176
tr 82,222,22528
ma v,4,a,48,v,180
buffer_store_dwordx4 v[200:203],v239,s[44:47],s77 offen sc1
ma v,8,a,52,v,176
tr 84,223,20480
tr 86,223,22528
ma v,12,a,52,v,180
wl 12
tr 88,224,20480
ma v,16,a,56,v,176
tr 90,224,22528
ma v,20,a,56,v,180
tr 92,225,20480
ma v,24,a,60,v,176
buffer_store_dwordx4 v[204:207],v239,s[44:47],s77 offen offset:1024 sc1
ma v,28,a,60,v,180
wl 11
tr 94,225,22528
tr 48,222,24576
ma v,0,a,64,v,184
tr 50,222,26624
ma v,4,a,64,v,188
s_add_u32 s89,s77,s9
tr 52,223,24576
ma v,8,a,68,v,184
tr 54,223,26624
ma v,12,a,68,v,188
wl 12
buffer_store_dwordx4 v[208:211],v239,s[44:47],s89 offen sc1
ma v,16,a,72,v,184
tr 56,224,24576
tr 58,224,26624
ma v,20,a,72,v,188
tr 60,225,24576
ma v,24,a,76,v,184
tr 62,225,26624
ma v,28,a,76,v,188
wl 12
tr 64,222,28672
ma v,0,a,80,v,192
buffer_store_dwordx4 v[212:215],v239,s[44:47],s89 offen offset:1024 sc1
ma v,4,a,80,v,196
s_add_u32 s77,s77,s10
tr 66,222,30720
tr 68,223,28672
ma v,8,a,84,v,192
s_cmp_ge_u32 s77,s79
tr 70,223,30720
s_cselect_b32 s77,s78,s77
ma v,12,a,84,v,196
wl 12
tr 72,224,28672
ma v,16,a,88,v,192
tr 74,224,30720
ma v,20,a,88,v,196
tr 76,225,28672
ma v,24,a,92,v,192
wl 11
tr 78,225,30720
ma v,28,a,92,v,196
s_waitcnt vmcnt(19)
s_barrier
ma v,0,a,48,v,48
s_mov_b32 m0,s75
tr 80,222,32768
tr 82,222,34816
ma v,4,a,48,v,52
dl 235,40,72,0
ma v,8,a,52,v,48
tr 84,223,32768
tr 86,223,34816
ma v,12,a,52,v,52
)"
R"(wl 12
tr 88,224,32768
ma v,16,a,56,v,48
tr 90,224,34816
ma v,20,a,56,v,52
tr 92,225,32768
ma v,24,a,60,v,48
tr 94,225,34816
ma v,28,a,60,v,52
wl 12
tr 48,222,36864
ma v,0,a,64,v,64
tr 50,222,38912
ma v,4,a,64,v,68
dl 236,40,72,1024
ma v,8,a,68,v,64
tr 52,223,36864
tr 54,223,38912
ma v,12,a,68,v,68
wl 12
tr 56,224,36864
ma v,16,a,72,v,64
tr 58,224,38912
ma v,20,a,72,v,68
tr 60,225,36864
ma v,24,a,76,v,64
tr 62,225,38912
ma v,28,a,76,v,68
wl 12
tr 64,222,40960
ma v,0,a,80,v,80
tr 66,222,43008
ma v,4,a,80,v,84
tr 68,223,40960
ma v,8,a,84,v,80
dl 237,40,72,2048
ma v,12,a,84,v,84
wl 11
tr 70,223,43008
tr 72,224,40960
ma v,16,a,88,v,80
tr 74,224,43008
ma v,20,a,88,v,84
tr 76,225,40960
ma v,24,a,92,v,80
tr 78,225,43008
ma v,28,a,92,v,84
wl 12
tr 80,222,45056
ma v,0,a,48,v,96
s_add_u32 s72,s72,s7
tr 82,222,47104
ma v,4,a,48,v,100
tr 84,223,45056
ma v,8,a,52,v,96
buffer_load_dwordx4 a[104:107],v239,s[44:47],s80 offen sc1
ma v,12,a,52,v,100
wl 11
tr 86,223,47104
tr 88,224,45056
ma v,16,a,56,v,96
tr 90,224,47104
ma v,20,a,56,v,100
tr 92,225,45056
ma v,24,a,60,v,96
tr 94,225,47104
v_add_u32 v222,0x18000,v222
ma v,28,a,60,v,100
wl 12
v_add_u32 v223,0x18000,v223
v_add_u32 v224,0x18000,v224
ma v,0,a,64,v,112
v_add_u32 v225,0x18000,v225
ma v,4,a,64,v,116
tr 48,222,0
tr 50,222,2048
ma v,8,a,68,v,112
tr 52,223,0
tr 54,223,2048
ma v,12,a,68,v,116
wl 12
buffer_load_dwordx4 a[108:111],v239,s[44:47],s80 offen offset:2048 sc1
ma v,16,a,72,v,112
s_addk_i32 s80,4096
tr 56,224,0
tr 58,224,2048
ma v,20,a,72,v,116
tr 60,225,0
ma v,24,a,76,v,112
tr 62,225,2048
ma v,28,a,76,v,116
wl 12
tr 64,222,4096
ma v,0,a,80,v,128
tr 66,222,6144
ma v,4,a,80,v,132
tr 68,223,4096
ma v,8,a,84,v,128
tr 70,223,6144
ma v,12,a,84,v,132
wl 12
tr 72,224,4096
ma v,16,a,88,v,128
tr 74,224,6144
ma v,20,a,88,v,132
tr 76,225,4096
ma v,24,a,92,v,128
wl 11
tr 78,225,6144
ma v,28,a,92,v,132
s_waitcnt vmcnt(5)
s_barrier
s_cmp_lg_u32 s64,0
s_cbranch_scc1 LA_nf1_%=
s_mov_b64 exec,1
global_store_dwordx2 v32,v[36:37],s[52:53] offset:8 sc1
s_mov_b64 exec,-1
LA_nf1_%=:
mz v,56,a,48,v,152
s_add_u32 m0,s75,0x6000
tr 80,222,8192
mz v,60,a,48,v,156
tr 82,222,10240
tr 84,223,8192
mz v,72,a,52,v,152
dl 235,40,72,0
mz v,76,a,52,v,156
wl 11
tr 86,223,10240
tr 88,224,8192
mz v,88,a,56,v,152
tr 90,224,10240
cv 200,0,1
mz v,92,a,56,v,156
dl 236,40,72,1024
cv 201,2,3
mz v,104,a,60,v,152
tr 92,225,8192
tr 94,225,10240
cv 202,8,9
mz v,108,a,60,v,156
wl 12
tr 48,222,12288
cv 203,10,11
ma v,56,a,64,v,160
tr 50,222,14336
cv 204,16,17
ma v,60,a,64,v,164
tr 52,223,12288
cv 205,18,19
ma v,72,a,68,v,160
dl 237,40,72,2048
cv 206,24,25
ma v,76,a,68,v,164
wl 11
tr 54,223,14336
tr 56,224,12288
cv 207,26,27
ma v,88,a,72,v,160
s_sub_u32 s72,s72,s8
tr 58,224,14336
cv 208,4,5
ma v,92,a,72,v,164
s_cmp_ge_u32 s72,s74
tr 60,225,12288
cv 209,6,7
ma v,104,a,76,v,160
s_cselect_b32 s72,s73,s72
tr 62,225,14336
cv 210,12,13
ma v,108,a,76,v,164
wl 12
buffer_load_dwordx4 a[112:115],v239,s[44:47],s80 offen sc1
cv 211,14,15
ma v,56,a,80,v,168
tr 64,222,16384
tr 66,222,18432
cv 212,20,21
ma v,60,a,80,v,172
tr 68,223,16384
cv 213,22,23
ma v,72,a,84,v,168
tr 70,223,18432
cv 214,28,29
ma v,76,a,84,v,172
wl 12
buffer_load_dwordx4 a[116:119],v239,s[44:47],s80 offen offset:2048 sc1
cv 215,30,31
ma v,88,a,88,v,168
s_addk_i32 s80,4096
tr 72,224,16384
tr 74,224,18432
ma v,92,a,88,v,172
tr 76,225,16384
ma v,104,a,92,v,168
tr 78,225,18432
ma v,108,a,92,v,172
wl 12
tr 80,222,20480
ma v,56,a,48,v,176
)"
R"(tr 82,222,22528
ma v,60,a,48,v,180
buffer_store_dwordx4 v[200:203],v239,s[44:47],s77 offen sc1
ma v,72,a,52,v,176
tr 84,223,20480
tr 86,223,22528
ma v,76,a,52,v,180
wl 12
tr 88,224,20480
ma v,88,a,56,v,176
tr 90,224,22528
ma v,92,a,56,v,180
tr 92,225,20480
ma v,104,a,60,v,176
buffer_store_dwordx4 v[204:207],v239,s[44:47],s77 offen offset:1024 sc1
ma v,108,a,60,v,180
wl 11
tr 94,225,22528
tr 48,222,24576
ma v,56,a,64,v,184
tr 50,222,26624
ma v,60,a,64,v,188
s_add_u32 s89,s77,s9
tr 52,223,24576
ma v,72,a,68,v,184
tr 54,223,26624
ma v,76,a,68,v,188
wl 12
buffer_store_dwordx4 v[208:211],v239,s[44:47],s89 offen sc1
ma v,88,a,72,v,184
tr 56,224,24576
tr 58,224,26624
ma v,92,a,72,v,188
tr 60,225,24576
ma v,104,a,76,v,184
tr 62,225,26624
ma v,108,a,76,v,188
wl 12
tr 64,222,28672
ma v,56,a,80,v,192
buffer_store_dwordx4 v[212:215],v239,s[44:47],s89 offen offset:1024 sc1
ma v,60,a,80,v,196
s_add_u32 s77,s77,s10
tr 66,222,30720
tr 68,223,28672
ma v,72,a,84,v,192
s_cmp_ge_u32 s77,s79
tr 70,223,30720
s_cselect_b32 s77,s78,s77
ma v,76,a,84,v,196
wl 12
tr 72,224,28672
ma v,88,a,88,v,192
tr 74,224,30720
ma v,92,a,88,v,196
tr 76,225,28672
ma v,104,a,92,v,192
wl 11
tr 78,225,30720
ma v,108,a,92,v,196
s_waitcnt vmcnt(23)
s_barrier
ma v,56,a,48,v,48
tr 80,222,32768
tr 82,222,34816
ma v,60,a,48,v,52
buffer_load_dwordx4 a[120:123],v239,s[44:47],s80 offen sc1
ma v,72,a,52,v,48
tr 84,223,32768
tr 86,223,34816
ma v,76,a,52,v,52
wl 12
tr 88,224,32768
ma v,88,a,56,v,48
tr 90,224,34816
ma v,92,a,56,v,52
tr 92,225,32768
ma v,104,a,60,v,48
tr 94,225,34816
ma v,108,a,60,v,52
wl 12
tr 48,222,36864
ma v,56,a,64,v,64
tr 50,222,38912
ma v,60,a,64,v,68
tr 52,223,36864
ma v,72,a,68,v,64
tr 54,223,38912
ma v,76,a,68,v,68
wl 12
tr 56,224,36864
ma v,88,a,72,v,64
tr 58,224,38912
ma v,92,a,72,v,68
tr 60,225,36864
ma v,104,a,76,v,64
tr 62,225,38912
ma v,108,a,76,v,68
wl 12
tr 64,222,40960
ma v,56,a,80,v,80
tr 66,222,43008
ma v,60,a,80,v,84
tr 68,223,40960
ma v,72,a,84,v,80
tr 70,223,43008
ma v,76,a,84,v,84
wl 12
tr 72,224,40960
ma v,88,a,88,v,80
tr 74,224,43008
ma v,92,a,88,v,84
tr 76,225,40960
ma v,104,a,92,v,80
buffer_load_dwordx4 a[124:127],v239,s[44:47],s80 offen offset:2048 sc1
ma v,108,a,92,v,84
s_addk_i32 s80,4096
tr 78,225,43008
tr 80,222,45056
wl 13
ma v,56,a,48,v,96
tr 82,222,47104
ma v,60,a,48,v,100
tr 84,223,45056
ma v,72,a,52,v,96
tr 86,223,47104
ma v,76,a,52,v,100
wl 12
tr 88,224,45056
ma v,88,a,56,v,96
tr 90,224,47104
ma v,92,a,56,v,100
tr 92,225,45056
ma v,104,a,60,v,96
tr 94,225,47104
v_add_u32 v222,0xffff4000,v222
ma v,108,a,60,v,100
wl 12
v_add_u32 v223,0xffff4000,v223
v_add_u32 v224,0xffff4000,v224
ma v,56,a,64,v,112
v_add_u32 v225,0xffff4000,v225
ma v,60,a,64,v,116
tr 48,222,0
tr 50,222,2048
ma v,72,a,68,v,112
tr 52,223,0
tr 54,223,2048
ma v,76,a,68,v,116
wl 12
tr 56,224,0
ma v,88,a,72,v,112
tr 58,224,2048
ma v,92,a,72,v,116
tr 60,225,0
ma v,104,a,76,v,112
tr 62,225,2048
ma v,108,a,76,v,116
wl 12
tr 64,222,4096
ma v,56,a,80,v,128
tr 66,222,6144
ma v,60,a,80,v,132
tr 68,223,4096
ma v,72,a,84,v,128
tr 70,223,6144
ma v,76,a,84,v,132
wl 12
tr 72,224,4096
ma v,88,a,88,v,128
tr 74,224,6144
ma v,92,a,88,v,132
tr 76,225,4096
ma v,104,a,92,v,128
wl 11
tr 78,225,6144
ma v,108,a,92,v,132
s_waitcnt vmcnt(22)
s_barrier
mz v,0,a,48,v,152
tr 80,222,8192
tr 82,222,10240
mz v,4,a,48,v,156
buffer_load_dwordx4 a[128:131],v239,s[44:47],s80 offen sc1
mz v,8,a,52,v,152
tr 84,223,8192
tr 86,223,10240
mz v,12,a,52,v,156
wl 12
tr 88,224,8192
mz v,16,a,56,v,152
tr 90,224,10240
cv 200,56,57
mz v,20,a,56,v,156
tr 92,225,8192
cv 201,58,59
mz v,24,a,60,v,152
tr 94,225,10240
cv 202,72,73
mz v,28,a,60,v,156
)"
R"(wl 12
tr 48,222,12288
cv 203,74,75
ma v,0,a,64,v,160
tr 50,222,14336
cv 204,88,89
ma v,4,a,64,v,164
tr 52,223,12288
cv 205,90,91
ma v,8,a,68,v,160
tr 54,223,14336
cv 206,104,105
ma v,12,a,68,v,164
wl 12
tr 56,224,12288
cv 207,106,107
ma v,16,a,72,v,160
buffer_load_dwordx4 a[132:135],v239,s[44:47],s80 offen offset:2048 sc1
cv 208,60,61
ma v,20,a,72,v,164
s_addk_i32 s80,4096
tr 58,224,14336
tr 60,225,12288
cv 209,62,63
ma v,24,a,76,v,160
tr 62,225,14336
cv 210,76,77
ma v,28,a,76,v,164
wl 12
tr 64,222,16384
cv 211,78,79
ma v,0,a,80,v,168
tr 66,222,18432
cv 212,92,93
ma v,4,a,80,v,172
tr 68,223,16384
cv 213,94,95
ma v,8,a,84,v,168
tr 70,223,18432
cv 214,108,109
ma v,12,a,84,v,172
wl 12
tr 72,224,16384
cv 215,110,111
ma v,16,a,88,v,168
tr 74,224,18432
ma v,20,a,88,v,172
tr 76,225,16384
ma v,24,a,92,v,168
tr 78,225,18432
ma v,28,a,92,v,172
wl 12
tr 80,222,20480
ma v,0,a,48,v,176
tr 82,222,22528
ma v,4,a,48,v,180
buffer_store_dwordx4 v[200:203],v239,s[44:47],s77 offen sc1
ma v,8,a,52,v,176
tr 84,223,20480
tr 86,223,22528
ma v,12,a,52,v,180
wl 12
tr 88,224,20480
ma v,16,a,56,v,176
tr 90,224,22528
ma v,20,a,56,v,180
tr 92,225,20480
ma v,24,a,60,v,176
buffer_store_dwordx4 v[204:207],v239,s[44:47],s77 offen offset:1024 sc1
ma v,28,a,60,v,180
wl 11
tr 94,225,22528
tr 48,222,24576
ma v,0,a,64,v,184
tr 50,222,26624
ma v,4,a,64,v,188
s_add_u32 s89,s77,s9
tr 52,223,24576
ma v,8,a,68,v,184
tr 54,223,26624
ma v,12,a,68,v,188
wl 12
buffer_store_dwordx4 v[208:211],v239,s[44:47],s89 offen sc1
ma v,16,a,72,v,184
tr 56,224,24576
tr 58,224,26624
ma v,20,a,72,v,188
tr 60,225,24576
ma v,24,a,76,v,184
tr 62,225,26624
ma v,28,a,76,v,188
wl 12
tr 64,222,28672
ma v,0,a,80,v,192
buffer_store_dwordx4 v[212:215],v239,s[44:47],s89 offen offset:1024 sc1
ma v,4,a,80,v,196
s_add_u32 s77,s77,s10
tr 66,222,30720
tr 68,223,28672
ma v,8,a,84,v,192
s_cmp_ge_u32 s77,s79
tr 70,223,30720
s_cselect_b32 s77,s78,s77
ma v,12,a,84,v,196
wl 12
tr 72,224,28672
ma v,16,a,88,v,192
tr 74,224,30720
ma v,20,a,88,v,196
tr 76,225,28672
ma v,24,a,92,v,192
wl 11
tr 78,225,30720
ma v,28,a,92,v,196
s_waitcnt vmcnt(19)
s_barrier
ma v,0,a,48,v,48
tr 80,222,32768
tr 82,222,34816
ma v,4,a,48,v,52
buffer_load_dwordx4 a[136:139],v239,s[44:47],s80 offen sc1
ma v,8,a,52,v,48
tr 84,223,32768
tr 86,223,34816
ma v,12,a,52,v,52
wl 12
tr 88,224,32768
ma v,16,a,56,v,48
tr 90,224,34816
ma v,20,a,56,v,52
tr 92,225,32768
ma v,24,a,60,v,48
buffer_load_dwordx4 a[140:143],v239,s[44:47],s80 offen offset:2048 sc1
ma v,28,a,60,v,52
wl 11
tr 94,225,34816
tr 48,222,36864
ma v,0,a,64,v,64
tr 50,222,38912
ma v,4,a,64,v,68
s_addk_i32 s80,4096
tr 52,223,36864
ma v,8,a,68,v,64
tr 54,223,38912
ma v,12,a,68,v,68
wl 12
buffer_load_dwordx4 a[160:163],v239,s[44:47],s81 offen sc1
ma v,16,a,72,v,64
tr 56,224,36864
tr 58,224,38912
ma v,20,a,72,v,68
tr 60,225,36864
ma v,24,a,76,v,64
tr 62,225,38912
ma v,28,a,76,v,68
wl 12
tr 64,222,40960
ma v,0,a,80,v,80
tr 66,222,43008
ma v,4,a,80,v,84
buffer_load_dwordx4 a[164:167],v239,s[44:47],s81 offen offset:2048 sc1
ma v,8,a,84,v,80
tr 68,223,40960
tr 70,223,43008
ma v,12,a,84,v,84
wl 12
tr 72,224,40960
ma v,16,a,88,v,80
s_addk_i32 s81,4096
tr 74,224,43008
ma v,20,a,88,v,84
tr 76,225,40960
ma v,24,a,92,v,80
buffer_load_dwordx4 a[168:171],v239,s[44:47],s81 offen sc1
ma v,28,a,92,v,84
wl 11
tr 78,225,43008
tr 80,222,45056
ma v,0,a,48,v,96
tr 82,222,47104
ma v,4,a,48,v,100
tr 84,223,45056
ma v,8,a,52,v,96
tr 86,223,47104
ma v,12,a,52,v,100
wl 12
buffer_load_dwordx4 a[172:175],v239,s[44:47],s81 offen offset:2048 sc1
ma v,16,a,56,v,96
tr 88,224,45056
tr 90,224,47104
ma v,20,a,56,v,100
tr 92,225,45056
ma v,24,a,60,v,96
tr 94,225,47104
v_add_u32 v222,0xffff4000,v222
ma v,28,a,60,v,100
wl 12
v_add_u32 v223,0xffff4000,v223
v_add_u32 v224,0xffff4000,v224
ma v,0,a,64,v,112
)"
R"(s_addk_i32 s81,4096
v_add_u32 v225,0xffff4000,v225
ma v,4,a,64,v,116
tr 48,222,0
tr 50,222,2048
ma v,8,a,68,v,112
tr 52,223,0
tr 54,223,2048
ma v,12,a,68,v,116
wl 12
buffer_load_dwordx4 a[176:179],v239,s[44:47],s81 offen sc1
ma v,16,a,72,v,112
tr 56,224,0
tr 58,224,2048
ma v,20,a,72,v,116
tr 60,225,0
ma v,24,a,76,v,112
buffer_load_dwordx4 a[180:183],v239,s[44:47],s81 offen offset:2048 sc1
ma v,28,a,76,v,116
wl 11
tr 62,225,2048
tr 64,222,4096
ma v,0,a,80,v,128
tr 66,222,6144
ma v,4,a,80,v,132
tr 68,223,4096
ma v,8,a,84,v,128
tr 70,223,6144
ma v,12,a,84,v,132
wl 12
tr 72,224,4096
ma v,16,a,88,v,128
tr 74,224,6144
ma v,20,a,88,v,132
tr 76,225,4096
ma v,24,a,92,v,128
wl 11
tr 78,225,6144
ma v,28,a,92,v,132
s_waitcnt vmcnt(22)
s_barrier
mz v,56,a,48,v,152
tr 80,222,8192
tr 82,222,10240
mz v,60,a,48,v,156
buffer_load_dwordx4 a[144:147],v239,s[44:47],s80 offen sc1
mz v,72,a,52,v,152
tr 84,223,8192
tr 86,223,10240
mz v,76,a,52,v,156
wl 12
tr 88,224,8192
mz v,88,a,56,v,152
buffer_load_dwordx4 a[148:151],v239,s[44:47],s80 offen offset:2048 sc1
cv 200,0,1
mz v,92,a,56,v,156
s_addk_i32 s80,4096
tr 90,224,10240
tr 92,225,8192
cv 201,2,3
mz v,104,a,60,v,152
s_addk_i32 s81,4096
tr 94,225,10240
cv 202,8,9
mz v,108,a,60,v,156
wl 12
buffer_load_dwordx4 a[184:187],v239,s[44:47],s81 offen sc1
cv 203,10,11
ma v,56,a,64,v,160
tr 48,222,12288
tr 50,222,14336
cv 204,16,17
ma v,60,a,64,v,164
tr 52,223,12288
cv 205,18,19
ma v,72,a,68,v,160
buffer_load_dwordx4 a[188:191],v239,s[44:47],s81 offen offset:2048 sc1
cv 206,24,25
ma v,76,a,68,v,164
wl 11
tr 54,223,14336
tr 56,224,12288
cv 207,26,27
ma v,88,a,72,v,160
s_addk_i32 s81,4096
tr 58,224,14336
cv 208,4,5
ma v,92,a,72,v,164
tr 60,225,12288
cv 209,6,7
ma v,104,a,76,v,160
buffer_load_dwordx4 a[192:195],v239,s[44:47],s81 offen sc1
cv 210,12,13
ma v,108,a,76,v,164
wl 11
tr 62,225,14336
tr 64,222,16384
cv 211,14,15
ma v,56,a,80,v,168
tr 66,222,18432
cv 212,20,21
ma v,60,a,80,v,172
buffer_load_dwordx4 a[196:199],v239,s[44:47],s81 offen offset:2048 sc1
cv 213,22,23
ma v,72,a,84,v,168
s_addk_i32 s81,4096
tr 68,223,16384
tr 70,223,18432
cv 214,28,29
ma v,76,a,84,v,172
wl 12
tr 72,224,16384
cv 215,30,31
ma v,88,a,88,v,168
buffer_load_dwordx4 a[200:203],v239,s[44:47],s81 offen sc1
ma v,92,a,88,v,172
tr 74,224,18432
tr 76,225,16384
ma v,104,a,92,v,168
tr 78,225,18432
ma v,108,a,92,v,172
wl 12
tr 80,222,20480
ma v,56,a,48,v,176
tr 82,222,22528
ma v,60,a,48,v,180
buffer_store_dwordx4 v[200:203],v239,s[44:47],s77 offen sc1
ma v,72,a,52,v,176
tr 84,223,20480
tr 86,223,22528
ma v,76,a,52,v,180
wl 12
tr 88,224,20480
ma v,88,a,56,v,176
tr 90,224,22528
ma v,92,a,56,v,180
tr 92,225,20480
ma v,104,a,60,v,176
buffer_store_dwordx4 v[204:207],v239,s[44:47],s77 offen offset:1024 sc1
ma v,108,a,60,v,180
wl 11
tr 94,225,22528
tr 48,222,24576
ma v,56,a,64,v,184
tr 50,222,26624
ma v,60,a,64,v,188
s_add_u32 s89,s77,s9
tr 52,223,24576
ma v,72,a,68,v,184
tr 54,223,26624
ma v,76,a,68,v,188
wl 12
buffer_store_dwordx4 v[208:211],v239,s[44:47],s89 offen sc1
ma v,88,a,72,v,184
tr 56,224,24576
tr 58,224,26624
ma v,92,a,72,v,188
tr 60,225,24576
ma v,104,a,76,v,184
tr 62,225,26624
ma v,108,a,76,v,188
wl 12
tr 64,222,28672
ma v,56,a,80,v,192
buffer_store_dwordx4 v[212:215],v239,s[44:47],s89 offen offset:1024 sc1
ma v,60,a,80,v,196
s_add_u32 s77,s77,s10
tr 66,222,30720
tr 68,223,28672
ma v,72,a,84,v,192
s_cmp_ge_u32 s77,s79
tr 70,223,30720
s_cselect_b32 s77,s78,s77
ma v,76,a,84,v,196
wl 12
tr 72,224,28672
ma v,88,a,88,v,192
tr 74,224,30720
ma v,92,a,88,v,196
tr 76,225,28672
ma v,104,a,92,v,192
wl 11
tr 78,225,30720
ma v,108,a,92,v,196
s_waitcnt vmcnt(19)
s_barrier
s_cmp_lg_u32 s64,0
s_cbranch_scc1 LA_nf2_%=
s_mov_b64 exec,1
global_store_dwordx2 v32,v[36:37],s[52:53] offset:16 sc1
s_mov_b64 exec,-1
LA_nf2_%=:
ma v,56,a,48,v,48
tr 80,222,32768
tr 82,222,34816
ma v,60,a,48,v,52
buffer_load_dwordx4 a[152:155],v239,s[44:47],s80 offen sc1
ma v,72,a,52,v,48
tr 84,223,32768
tr 86,223,34816
)"
R"(ma v,76,a,52,v,52
wl 12
tr 88,224,32768
ma v,88,a,56,v,48
tr 90,224,34816
ma v,92,a,56,v,52
tr 92,225,32768
ma v,104,a,60,v,48
tr 94,225,34816
ma v,108,a,60,v,52
wl 12
buffer_load_dwordx4 a[156:159],v239,s[44:47],s80 offen offset:2048 sc1
ma v,56,a,64,v,64
tr 48,222,36864
tr 50,222,38912
ma v,60,a,64,v,68
tr 52,223,36864
ma v,72,a,68,v,64
tr 54,223,38912
ma v,76,a,68,v,68
wl 12
tr 56,224,36864
ma v,88,a,72,v,64
tr 58,224,38912
ma v,92,a,72,v,68
buffer_load_dwordx4 a[204:207],v239,s[44:47],s81 offen offset:2048 sc1
ma v,104,a,76,v,64
tr 60,225,36864
tr 62,225,38912
ma v,108,a,76,v,68
wl 12
tr 64,222,40960
ma v,56,a,80,v,80
tr 66,222,43008
ma v,60,a,80,v,84
s_addk_i32 s81,4096
tr 68,223,40960
ma v,72,a,84,v,80
tr 70,223,43008
ma v,76,a,84,v,84
wl 12
buffer_load_dwordx4 a[208:211],v239,s[44:47],s81 offen sc1
ma v,88,a,88,v,80
tr 72,224,40960
tr 74,224,43008
ma v,92,a,88,v,84
tr 76,225,40960
ma v,104,a,92,v,80
tr 78,225,43008
ma v,108,a,92,v,84
wl 12
tr 80,222,45056
ma v,56,a,48,v,96
tr 82,222,47104
ma v,60,a,48,v,100
buffer_load_dwordx4 a[212:215],v239,s[44:47],s81 offen offset:2048 sc1
ma v,72,a,52,v,96
tr 84,223,45056
tr 86,223,47104
ma v,76,a,52,v,100
wl 12
tr 88,224,45056
ma v,88,a,56,v,96
tr 90,224,47104
ma v,92,a,56,v,100
s_addk_i32 s81,4096
tr 92,225,45056
ma v,104,a,60,v,96
tr 94,225,47104
ma v,108,a,60,v,100
wl 12
buffer_load_dwordx4 a[216:219],v239,s[44:47],s81 offen sc1
ma v,56,a,64,v,112
ma v,60,a,64,v,116
ma v,72,a,68,v,112
ma v,76,a,68,v,116
wl 8
ma v,88,a,72,v,112
ma v,92,a,72,v,116
buffer_load_dwordx4 a[220:223],v239,s[44:47],s81 offen offset:2048 sc1
ma v,104,a,76,v,112
ma v,108,a,76,v,116
wl 4
ma v,56,a,80,v,128
ma v,60,a,80,v,132
ma v,72,a,84,v,128
ma v,76,a,84,v,132
wl 0
ma v,88,a,88,v,128
ma v,92,a,88,v,132
ma v,104,a,92,v,128
ma v,108,a,92,v,132
s_nop 7
cv 200,56,57
cv 201,58,59
cv 202,72,73
cv 203,74,75
cv 204,88,89
cv 205,90,91
cv 206,104,105
cv 207,106,107
cv 208,60,61
cv 209,62,63
cv 210,76,77
cv 211,78,79
cv 212,92,93
cv 213,94,95
cv 214,108,109
cv 215,110,111
buffer_store_dwordx4 v[200:203],v239,s[44:47],s77 offen sc1
buffer_store_dwordx4 v[204:207],v239,s[44:47],s77 offen offset:1024 sc1
s_add_u32 s89,s77,s9
buffer_store_dwordx4 v[208:211],v239,s[44:47],s89 offen sc1
buffer_store_dwordx4 v[212:215],v239,s[44:47],s89 offen offset:1024 sc1
s_add_u32 s77,s77,s10
s_cmp_ge_u32 s77,s79
s_cselect_b32 s77,s78,s77
s_waitcnt vmcnt(0)
s_barrier
s_cmp_lg_u32 s64,0
s_cbranch_scc1 LA_nf3_%=
s_mov_b64 exec,1
global_store_dwordx2 v32,v[36:37],s[52:53] offset:24 sc1
s_mov_b64 exec,-1
LA_nf3_%=:
buffer_load_dwordx4 v[64:67],v239,s[44:47],s82 offen sc1
buffer_load_dwordx4 v[68:71],v239,s[44:47],s82 offen offset:2048 sc1
s_addk_i32 s82,4096
buffer_load_dwordx4 v[72:75],v239,s[44:47],s82 offen sc1
buffer_load_dwordx4 v[76:79],v239,s[44:47],s82 offen offset:2048 sc1
s_addk_i32 s82,4096
buffer_load_dwordx4 v[80:83],v239,s[44:47],s82 offen sc1
buffer_load_dwordx4 v[84:87],v239,s[44:47],s82 offen offset:2048 sc1
s_addk_i32 s82,4096
buffer_load_dwordx4 v[88:91],v239,s[44:47],s82 offen sc1
buffer_load_dwordx4 v[92:95],v239,s[44:47],s82 offen offset:2048 sc1
s_addk_i32 s82,4096
buffer_load_dwordx4 v[96:99],v239,s[44:47],s82 offen sc1
buffer_load_dwordx4 v[100:103],v239,s[44:47],s82 offen offset:2048 sc1
s_addk_i32 s82,4096
buffer_load_dwordx4 v[104:107],v239,s[44:47],s82 offen sc1
buffer_load_dwordx4 v[108:111],v239,s[44:47],s82 offen offset:2048 sc1
s_addk_i32 s82,4096
buffer_load_dwordx4 v[112:115],v239,s[44:47],s82 offen sc1
buffer_load_dwordx4 v[116:119],v239,s[44:47],s82 offen offset:2048 sc1
s_addk_i32 s82,4096
buffer_load_dwordx4 v[120:123],v239,s[44:47],s82 offen sc1
buffer_load_dwordx4 v[124:127],v239,s[44:47],s82 offen offset:2048 sc1
mz v,0,a,244,a,96
mz v,4,a,248,a,96
ma v,0,a,244,a,100
ma v,4,a,248,a,100
ma v,0,a,244,a,104
ma v,4,a,248,a,104
ma v,0,a,244,a,108
ma v,4,a,248,a,108
ma v,0,a,244,a,112
ma v,4,a,248,a,112
ma v,0,a,244,a,116
ma v,4,a,248,a,116
ma v,0,a,244,a,120
ma v,4,a,248,a,120
ma v,0,a,244,a,124
ma v,4,a,248,a,124
ma v,0,a,244,a,128
ma v,4,a,248,a,128
ma v,0,a,244,a,132
ma v,4,a,248,a,132
ma v,0,a,244,a,136
ma v,4,a,248,a,136
ma v,0,a,244,a,140
ma v,4,a,248,a,140
ma v,0,a,244,a,144
ma v,4,a,248,a,144
ma v,0,a,244,a,148
ma v,4,a,248,a,148
ma v,0,a,244,a,152
ma v,4,a,248,a,152
ma v,0,a,244,a,156
ma v,4,a,248,a,156
mz v,8,a,244,a,160
mz v,12,a,248,a,160
ma v,8,a,244,a,164
ma v,12,a,248,a,164
ma v,8,a,244,a,168
ma v,12,a,248,a,168
ma v,8,a,244,a,172
ma v,12,a,248,a,172
ma v,8,a,244,a,176
ma v,12,a,248,a,176
ma v,8,a,244,a,180
ma v,12,a,248,a,180
ma v,8,a,244,a,184
ma v,12,a,248,a,184
ma v,8,a,244,a,188
ma v,12,a,248,a,188
ma v,8,a,244,a,192
ma v,12,a,248,a,192
ma v,8,a,244,a,196
ma v,12,a,248,a,196
)"
R"(ma v,8,a,244,a,200
ma v,12,a,248,a,200
ma v,8,a,244,a,204
ma v,12,a,248,a,204
ma v,8,a,244,a,208
ma v,12,a,248,a,208
ma v,8,a,244,a,212
ma v,12,a,248,a,212
ma v,8,a,244,a,216
ma v,12,a,248,a,216
ma v,8,a,244,a,220
ma v,12,a,248,a,220
s_waitcnt vmcnt(0)
cv 40,0,1
cv 41,2,3
cv 42,4,5
cv 43,6,7
buffer_store_dwordx4 v[40:43],v34,s[48:51],s83 offen
s_nop 0
cv 44,8,9
cv 45,10,11
cv 46,12,13
cv 47,14,15
buffer_store_dwordx4 v[44:47],v34,s[48:51],s84 offen
s_barrier
buffer_load_dwordx4 v[128:131],v239,s[44:47],s96 offen sc1
buffer_load_dwordx4 v[132:135],v239,s[44:47],s96 offen offset:2048 sc1
s_addk_i32 s96,4096
buffer_load_dwordx4 v[136:139],v239,s[44:47],s96 offen sc1
buffer_load_dwordx4 v[140:143],v239,s[44:47],s96 offen offset:2048 sc1
s_addk_i32 s96,4096
buffer_load_dwordx4 v[144:147],v239,s[44:47],s96 offen sc1
buffer_load_dwordx4 v[148:151],v239,s[44:47],s96 offen offset:2048 sc1
s_addk_i32 s96,4096
buffer_load_dwordx4 v[152:155],v239,s[44:47],s96 offen sc1
buffer_load_dwordx4 v[156:159],v239,s[44:47],s96 offen offset:2048 sc1
s_addk_i32 s96,4096
buffer_load_dwordx4 v[160:163],v239,s[44:47],s96 offen sc1
buffer_load_dwordx4 v[164:167],v239,s[44:47],s96 offen offset:2048 sc1
s_addk_i32 s96,4096
buffer_load_dwordx4 v[168:171],v239,s[44:47],s96 offen sc1
buffer_load_dwordx4 v[172:175],v239,s[44:47],s96 offen offset:2048 sc1
s_addk_i32 s96,4096
buffer_load_dwordx4 v[176:179],v239,s[44:47],s96 offen sc1
buffer_load_dwordx4 v[180:183],v239,s[44:47],s96 offen offset:2048 sc1
s_addk_i32 s96,4096
buffer_load_dwordx4 v[184:187],v239,s[44:47],s96 offen sc1
buffer_load_dwordx4 v[188:191],v239,s[44:47],s96 offen offset:2048 sc1
mz v,16,a,244,v,64
mz v,20,a,248,v,64
ma v,16,a,244,v,68
ma v,20,a,248,v,68
ma v,16,a,244,v,72
ma v,20,a,248,v,72
ma v,16,a,244,v,76
ma v,20,a,248,v,76
ma v,16,a,244,v,80
ma v,20,a,248,v,80
ma v,16,a,244,v,84
ma v,20,a,248,v,84
ma v,16,a,244,v,88
ma v,20,a,248,v,88
ma v,16,a,244,v,92
ma v,20,a,248,v,92
ma v,16,a,244,v,96
ma v,20,a,248,v,96
ma v,16,a,244,v,100
ma v,20,a,248,v,100
ma v,16,a,244,v,104
ma v,20,a,248,v,104
ma v,16,a,244,v,108
ma v,20,a,248,v,108
ma v,16,a,244,v,112
ma v,20,a,248,v,112
ma v,16,a,244,v,116
ma v,20,a,248,v,116
ma v,16,a,244,v,120
ma v,20,a,248,v,120
ma v,16,a,244,v,124
ma v,20,a,248,v,124
s_nop 6
cv 200,16,17
cv 201,18,19
cv 202,20,21
cv 203,22,23
buffer_store_dwordx4 v[200:203],v34,s[48:51],s85 offen
s_waitcnt vmcnt(15)
mz v,24,a,244,v,128
mz v,28,a,248,v,128
ma v,24,a,244,v,132
ma v,28,a,248,v,132
s_waitcnt vmcnt(13)
ma v,24,a,244,v,136
ma v,28,a,248,v,136
ma v,24,a,244,v,140
ma v,28,a,248,v,140
s_waitcnt vmcnt(11)
ma v,24,a,244,v,144
ma v,28,a,248,v,144
ma v,24,a,244,v,148
ma v,28,a,248,v,148
s_waitcnt vmcnt(9)
ma v,24,a,244,v,152
ma v,28,a,248,v,152
ma v,24,a,244,v,156
ma v,28,a,248,v,156
s_waitcnt vmcnt(7)
ma v,24,a,244,v,160
ma v,28,a,248,v,160
ma v,24,a,244,v,164
ma v,28,a,248,v,164
s_waitcnt vmcnt(5)
ma v,24,a,244,v,168
ma v,28,a,248,v,168
ma v,24,a,244,v,172
ma v,28,a,248,v,172
s_waitcnt vmcnt(3)
ma v,24,a,244,v,176
ma v,28,a,248,v,176
ma v,24,a,244,v,180
ma v,28,a,248,v,180
s_waitcnt vmcnt(1)
ma v,24,a,244,v,184
ma v,28,a,248,v,184
ma v,24,a,244,v,188
ma v,28,a,248,v,188
s_nop 6
cv 204,24,25
cv 205,26,27
cv 206,28,29
cv 207,30,31
buffer_store_dwordx4 v[204:207],v34,s[48:51],s97 offen
s_branch Lend_%=
LA_a0i0_%=:
s_getpc_b64 s[60:61]
s_branch LA_rare1_%=
s_branch LA_a0i0_ret_%=
LA_a0i1_%=:
s_getpc_b64 s[60:61]
s_branch LA_rare0_%=
s_branch LA_a0i1_ret_%=
LA_a0i2_%=:
s_getpc_b64 s[60:61]
s_branch LA_rare1_%=
s_branch LA_a0i2_ret_%=
LA_a1i0_%=:
s_getpc_b64 s[60:61]
s_branch LA_rare1_%=
s_branch LA_a1i0_ret_%=
LA_a1i1_%=:
s_getpc_b64 s[60:61]
s_branch LA_rare0_%=
s_branch LA_a1i1_ret_%=
LA_a1i2_%=:
s_getpc_b64 s[60:61]
s_branch LA_rare1_%=
s_branch LA_a1i2_ret_%=
LA_rare0_%=:
s_cselect_b32 s92,1,0
s_nop 7
v_mov_b32 v242,v240
v_mov_b32 v244,v241
s_nop 0
v_permlane32_swap_b32 v240,v242
v_permlane32_swap_b32 v241,v244
v_max_f32 v240,v240,v242
v_max_f32 v241,v241,v244
v_mov_b32 v242,v240
v_mov_b32 v244,v241
s_nop 0
v_permlane16_swap_b32 v240,v242
v_permlane16_swap_b32 v241,v244
v_max_f32 v240,v240,v242
v_max_f32 v241,v241,v244
v_max_f32 v240,0,v240
v_exp_f32 v248,-v240
v_sub_f32 v32,v32,v240
v_sub_f32 v33,v33,v240
v_sub_f32 v34,v34,v240
v_sub_f32 v35,v35,v240
v_sub_f32 v0,v0,v240
v_sub_f32 v1,v1,v240
v_sub_f32 v2,v2,v240
v_sub_f32 v3,v3,v240
v_sub_f32 v8,v8,v240
v_sub_f32 v9,v9,v240
v_sub_f32 v10,v10,v240
v_sub_f32 v11,v11,v240
v_max_f32 v241,0,v241
v_exp_f32 v250,-v241
v_sub_f32 v36,v36,v241
v_sub_f32 v37,v37,v241
v_sub_f32 v38,v38,v241
v_sub_f32 v39,v39,v241
v_sub_f32 v4,v4,v241
v_sub_f32 v5,v5,v241
v_sub_f32 v6,v6,v241
v_sub_f32 v7,v7,v241
v_sub_f32 v12,v12,v241
v_sub_f32 v13,v13,v241
)"
R"(v_sub_f32 v14,v14,v241
v_sub_f32 v15,v15,v241
v_pk_mul_f32 v[48:49],v[48:49],v[248:249] op_sel_hi:[1,0]
v_pk_mul_f32 v[50:51],v[50:51],v[248:249] op_sel_hi:[1,0]
v_pk_mul_f32 v[52:53],v[52:53],v[250:251] op_sel_hi:[1,0]
v_pk_mul_f32 v[54:55],v[54:55],v[250:251] op_sel_hi:[1,0]
v_pk_mul_f32 v[56:57],v[56:57],v[248:249] op_sel_hi:[1,0]
v_pk_mul_f32 v[58:59],v[58:59],v[248:249] op_sel_hi:[1,0]
v_pk_mul_f32 v[60:61],v[60:61],v[250:251] op_sel_hi:[1,0]
v_pk_mul_f32 v[62:63],v[62:63],v[250:251] op_sel_hi:[1,0]
v_pk_mul_f32 v[64:65],v[64:65],v[248:249] op_sel_hi:[1,0]
v_pk_mul_f32 v[66:67],v[66:67],v[248:249] op_sel_hi:[1,0]
v_pk_mul_f32 v[68:69],v[68:69],v[250:251] op_sel_hi:[1,0]
v_pk_mul_f32 v[70:71],v[70:71],v[250:251] op_sel_hi:[1,0]
v_pk_mul_f32 v[72:73],v[72:73],v[248:249] op_sel_hi:[1,0]
v_pk_mul_f32 v[74:75],v[74:75],v[248:249] op_sel_hi:[1,0]
v_pk_mul_f32 v[76:77],v[76:77],v[250:251] op_sel_hi:[1,0]
v_pk_mul_f32 v[78:79],v[78:79],v[250:251] op_sel_hi:[1,0]
v_pk_mul_f32 v[80:81],v[80:81],v[248:249] op_sel_hi:[1,0]
v_pk_mul_f32 v[82:83],v[82:83],v[248:249] op_sel_hi:[1,0]
v_pk_mul_f32 v[84:85],v[84:85],v[250:251] op_sel_hi:[1,0]
v_pk_mul_f32 v[86:87],v[86:87],v[250:251] op_sel_hi:[1,0]
v_pk_mul_f32 v[88:89],v[88:89],v[248:249] op_sel_hi:[1,0]
v_pk_mul_f32 v[90:91],v[90:91],v[248:249] op_sel_hi:[1,0]
v_pk_mul_f32 v[92:93],v[92:93],v[250:251] op_sel_hi:[1,0]
v_pk_mul_f32 v[94:95],v[94:95],v[250:251] op_sel_hi:[1,0]
v_pk_mul_f32 v[96:97],v[96:97],v[248:249] op_sel_hi:[1,0]
v_pk_mul_f32 v[98:99],v[98:99],v[248:249] op_sel_hi:[1,0]
v_pk_mul_f32 v[100:101],v[100:101],v[250:251] op_sel_hi:[1,0]
v_pk_mul_f32 v[102:103],v[102:103],v[250:251] op_sel_hi:[1,0]
v_pk_mul_f32 v[104:105],v[104:105],v[248:249] op_sel_hi:[1,0]
v_pk_mul_f32 v[106:107],v[106:107],v[248:249] op_sel_hi:[1,0]
v_pk_mul_f32 v[108:109],v[108:109],v[250:251] op_sel_hi:[1,0]
v_pk_mul_f32 v[110:111],v[110:111],v[250:251] op_sel_hi:[1,0]
v_pk_mul_f32 v[112:113],v[112:113],v[248:249] op_sel_hi:[1,0]
v_pk_mul_f32 v[114:115],v[114:115],v[248:249] op_sel_hi:[1,0]
v_pk_mul_f32 v[116:117],v[116:117],v[250:251] op_sel_hi:[1,0]
v_pk_mul_f32 v[118:119],v[118:119],v[250:251] op_sel_hi:[1,0]
v_pk_mul_f32 v[120:121],v[120:121],v[248:249] op_sel_hi:[1,0]
v_pk_mul_f32 v[122:123],v[122:123],v[248:249] op_sel_hi:[1,0]
v_pk_mul_f32 v[124:125],v[124:125],v[250:251] op_sel_hi:[1,0]
v_pk_mul_f32 v[126:127],v[126:127],v[250:251] op_sel_hi:[1,0]
v_pk_mul_f32 v[128:129],v[128:129],v[248:249] op_sel_hi:[1,0]
v_pk_mul_f32 v[130:131],v[130:131],v[248:249] op_sel_hi:[1,0]
v_pk_mul_f32 v[132:133],v[132:133],v[250:251] op_sel_hi:[1,0]
v_pk_mul_f32 v[134:135],v[134:135],v[250:251] op_sel_hi:[1,0]
v_pk_mul_f32 v[136:137],v[136:137],v[248:249] op_sel_hi:[1,0]
v_pk_mul_f32 v[138:139],v[138:139],v[248:249] op_sel_hi:[1,0]
v_pk_mul_f32 v[140:141],v[140:141],v[250:251] op_sel_hi:[1,0]
v_pk_mul_f32 v[142:143],v[142:143],v[250:251] op_sel_hi:[1,0]
v_pk_mul_f32 v[144:145],v[144:145],v[248:249] op_sel_hi:[1,0]
v_pk_mul_f32 v[146:147],v[146:147],v[248:249] op_sel_hi:[1,0]
v_pk_mul_f32 v[148:149],v[148:149],v[250:251] op_sel_hi:[1,0]
v_pk_mul_f32 v[150:151],v[150:151],v[250:251] op_sel_hi:[1,0]
s_add_u32 s60,s60,4
s_addc_u32 s61,s61,0
s_cmp_eq_u32 s92,1
s_setpc_b64 s[60:61]
LA_rare1_%=:
s_cselect_b32 s92,1,0
s_nop 7
v_mov_b32 v242,v240
v_mov_b32 v244,v241
s_nop 0
v_permlane32_swap_b32 v240,v242
v_permlane32_swap_b32 v241,v244
v_max_f32 v240,v240,v242
v_max_f32 v241,v241,v244
v_mov_b32 v242,v240
v_mov_b32 v244,v241
s_nop 0
v_permlane16_swap_b32 v240,v242
v_permlane16_swap_b32 v241,v244
v_max_f32 v240,v240,v242
v_max_f32 v241,v241,v244
v_max_f32 v240,0,v240
v_exp_f32 v248,-v240
v_sub_f32 v32,v32,v240
v_sub_f32 v33,v33,v240
v_sub_f32 v34,v34,v240
v_sub_f32 v35,v35,v240
v_sub_f32 v16,v16,v240
v_sub_f32 v17,v17,v240
v_sub_f32 v18,v18,v240
v_sub_f32 v19,v19,v240
v_sub_f32 v24,v24,v240
v_sub_f32 v25,v25,v240
v_sub_f32 v26,v26,v240
v_sub_f32 v27,v27,v240
v_max_f32 v241,0,v241
v_exp_f32 v250,-v241
v_sub_f32 v36,v36,v241
v_sub_f32 v37,v37,v241
v_sub_f32 v38,v38,v241
v_sub_f32 v39,v39,v241
v_sub_f32 v20,v20,v241
v_sub_f32 v21,v21,v241
v_sub_f32 v22,v22,v241
v_sub_f32 v23,v23,v241
v_sub_f32 v28,v28,v241
v_sub_f32 v29,v29,v241
v_sub_f32 v30,v30,v241
v_sub_f32 v31,v31,v241
v_pk_mul_f32 v[48:49],v[48:49],v[248:249] op_sel_hi:[1,0]
v_pk_mul_f32 v[50:51],v[50:51],v[248:249] op_sel_hi:[1,0]
v_pk_mul_f32 v[52:53],v[52:53],v[250:251] op_sel_hi:[1,0]
v_pk_mul_f32 v[54:55],v[54:55],v[250:251] op_sel_hi:[1,0]
v_pk_mul_f32 v[56:57],v[56:57],v[248:249] op_sel_hi:[1,0]
v_pk_mul_f32 v[58:59],v[58:59],v[248:249] op_sel_hi:[1,0]
v_pk_mul_f32 v[60:61],v[60:61],v[250:251] op_sel_hi:[1,0]
v_pk_mul_f32 v[62:63],v[62:63],v[250:251] op_sel_hi:[1,0]
v_pk_mul_f32 v[64:65],v[64:65],v[248:249] op_sel_hi:[1,0]
v_pk_mul_f32 v[66:67],v[66:67],v[248:249] op_sel_hi:[1,0]
v_pk_mul_f32 v[68:69],v[68:69],v[250:251] op_sel_hi:[1,0]
v_pk_mul_f32 v[70:71],v[70:71],v[250:251] op_sel_hi:[1,0]
v_pk_mul_f32 v[72:73],v[72:73],v[248:249] op_sel_hi:[1,0]
v_pk_mul_f32 v[74:75],v[74:75],v[248:249] op_sel_hi:[1,0]
v_pk_mul_f32 v[76:77],v[76:77],v[250:251] op_sel_hi:[1,0]
v_pk_mul_f32 v[78:79],v[78:79],v[250:251] op_sel_hi:[1,0]
v_pk_mul_f32 v[80:81],v[80:81],v[248:249] op_sel_hi:[1,0]
v_pk_mul_f32 v[82:83],v[82:83],v[248:249] op_sel_hi:[1,0]
v_pk_mul_f32 v[84:85],v[84:85],v[250:251] op_sel_hi:[1,0]
v_pk_mul_f32 v[86:87],v[86:87],v[250:251] op_sel_hi:[1,0]
v_pk_mul_f32 v[88:89],v[88:89],v[248:249] op_sel_hi:[1,0]
v_pk_mul_f32 v[90:91],v[90:91],v[248:249] op_sel_hi:[1,0]
v_pk_mul_f32 v[92:93],v[92:93],v[250:251] op_sel_hi:[1,0]
v_pk_mul_f32 v[94:95],v[94:95],v[250:251] op_sel_hi:[1,0]
v_pk_mul_f32 v[96:97],v[96:97],v[248:249] op_sel_hi:[1,0]
v_pk_mul_f32 v[98:99],v[98:99],v[248:249] op_sel_hi:[1,0]
v_pk_mul_f32 v[100:101],v[100:101],v[250:251] op_sel_hi:[1,0]
v_pk_mul_f32 v[102:103],v[102:103],v[250:251] op_sel_hi:[1,0]
v_pk_mul_f32 v[104:105],v[104:105],v[248:249] op_sel_hi:[1,0]
v_pk_mul_f32 v[106:107],v[106:107],v[248:249] op_sel_hi:[1,0]
v_pk_mul_f32 v[108:109],v[108:109],v[250:251] op_sel_hi:[1,0]
v_pk_mul_f32 v[110:111],v[110:111],v[250:251] op_sel_hi:[1,0]
v_pk_mul_f32 v[112:113],v[112:113],v[248:249] op_sel_hi:[1,0]
v_pk_mul_f32 v[114:115],v[114:115],v[248:249] op_sel_hi:[1,0]
v_pk_mul_f32 v[116:117],v[116:117],v[250:251] op_sel_hi:[1,0]
v_pk_mul_f32 v[118:119],v[118:119],v[250:251] op_sel_hi:[1,0]
v_pk_mul_f32 v[120:121],v[120:121],v[248:249] op_sel_hi:[1,0]
v_pk_mul_f32 v[122:123],v[122:123],v[248:249] op_sel_hi:[1,0]
v_pk_mul_f32 v[124:125],v[124:125],v[250:251] op_sel_hi:[1,0]
v_pk_mul_f32 v[126:127],v[126:127],v[250:251] op_sel_hi:[1,0]
v_pk_mul_f32 v[128:129],v[128:129],v[248:249] op_sel_hi:[1,0]
v_pk_mul_f32 v[130:131],v[130:131],v[248:249] op_sel_hi:[1,0]
v_pk_mul_f32 v[132:133],v[132:133],v[250:251] op_sel_hi:[1,0]
v_pk_mul_f32 v[134:135],v[134:135],v[250:251] op_sel_hi:[1,0]
v_pk_mul_f32 v[136:137],v[136:137],v[248:249] op_sel_hi:[1,0]
v_pk_mul_f32 v[138:139],v[138:139],v[248:249] op_sel_hi:[1,0]
v_pk_mul_f32 v[140:141],v[140:141],v[250:251] op_sel_hi:[1,0]
v_pk_mul_f32 v[142:143],v[142:143],v[250:251] op_sel_hi:[1,0]
v_pk_mul_f32 v[144:145],v[144:145],v[248:249] op_sel_hi:[1,0]
v_pk_mul_f32 v[146:147],v[146:147],v[248:249] op_sel_hi:[1,0]
v_pk_mul_f32 v[148:149],v[148:149],v[250:251] op_sel_hi:[1,0]
v_pk_mul_f32 v[150:151],v[150:151],v[250:251] op_sel_hi:[1,0]
s_add_u32 s60,s60,4
s_addc_u32 s61,s61,0
s_cmp_eq_u32 s92,1
s_setpc_b64 s[60:61]
LB_start_%=:
v_mbcnt_lo_u32_b32 v16,-1,0
v_mbcnt_hi_u32_b32 v16,-1,v16
s_mov_b32 s64,%2
s_add_u32 s88,%1,0
s_and_b32 s88,s88,7
s_lshr_b32 s89,%1,3
s_and_b32 s90,s89,1
s_lshr_b32 s65,s89,1
s_lshl_b32 s66,s88,1
s_add_u32 s66,s66,s90
s_and_b32 s92,s64,1
v_lshrrev_b32 v17,4,v16
v_lshrrev_b32 v21,3,v16
v_and_b32 v22,7,v16
s_mul_i32 s88,s65,5
s_lshr_b32 s68,s88,3
s_lshr_b32 s88,s65,2
s_mul_i32 s88,s88,3
s_mul_i32 s89,s90,5
s_add_u32 s67,s88,s89
s_cmp_ge_u32 s67,10
s_cselect_b32 s89,10,0
s_sub_u32 s67,s67,s89
v_mul_u32_u24 v24,0x500,v21
s_xor_b32 s89,s92,0
s_lshl_b32 s89,s89,2
v_add_u32 v23,s89,v17
v_xor_b32 v23,v23,v22
v_lshl_add_u32 v235,v23,4,v24
s_xor_b32 s89,s92,1
s_lshl_b32 s89,s89,2
v_add_u32 v23,s89,v17
v_xor_b32 v23,v23,v22
v_lshl_add_u32 v236,v23,4,v24
v_add_u32 v237,0x4800,v235
v_add_u32 v236,0x2400,v236
v_add_u32 v238,0x4800,v236
s_mul_i32 s73,s64,0x18000
s_add_u32 s74,s73,0x780000
s_mul_i32 s88,s67,0xc0000
)"
R"(s_add_u32 s72,s73,s88
s_mul_i32 s75,s64,0xc00
s_mul_i32 s77,s64,0x7800
s_add_u32 s78,s77,0x500
s_lshl_b32 s89,s67,7
s_add_u32 s76,s77,s89
s_add_u32 s79,s75,0xf000
s_mov_b32 s69,64
s_mov_b32 s79,s75
v_and_b32 v23,2,v21
s_lshl_b32 s89,s92,2
v_or_b32 v23,s89,v23
v_xor_b32 v23,v23,v22
v_mul_u32_u24 v24,0x3000,v21
v_lshl_add_u32 v234,v23,4,v24
v_add_u32 v233,0x880,v234
v_add_u32 v232,0xc00,v234
v_add_u32 v252,0x500,v234
s_getreg_b32 s69,hwreg(20,0,4)
s_xor_b32 s69,s69,1
s_mov_b32 s95,0x60000
s_mul_i32 s88,s65,0x23000
s_mul_i32 s91,s66,0x300
s_mov_b32 s22,0x23000
s_mov_b32 s23,0x20000
s_mov_b32 s26,0x77df00
s_mov_b32 s27,0x20000
wl 0
s_add_u32 s20,s4,s88
s_addc_u32 s21,s5,0
s_and_b32 s21,s21,0xffff
s_add_u32 s24,s10,s91
s_addc_u32 s25,s11,0
s_sub_u32 s24,s24,0xc00
s_subb_u32 s25,s25,0
s_and_b32 s25,s25,0xffff
s_mov_b32 m0,s79
s_nop 0
dl 235,20,76,0
dl 236,20,76,1024
dl 237,20,76,2048
dl 238,20,76,3072
s_add_u32 s90,s76,0xa000
s_add_u32 m0,s79,0x1000
s_nop 0
dl 235,20,90,0
s_addk_i32 s76,128
s_cmp_ge_u32 s76,s78
s_cselect_b32 s76,s77,s76
s_add_u32 m0,s75,0x13000
s_nop 0
dl 232,24,72,0
dl 233,24,72,1024
dl 252,24,72,2048
s_add_u32 s90,s72,0x180
s_add_u32 m0,s75,0x16000
s_nop 0
dl 232,24,90,0
dl 233,24,90,1024
dl 252,24,90,2048
s_add_u32 s72,s72,s95
s_lshl_b32 s88,s65,7
s_add_u32 s54,s16,s88
s_addc_u32 s55,s17,0
v_mov_b32 v23,0
global_load_dwordx2 v[250:251],v23,s[54:55] sc1
s_lshl_b32 s88,s65,4
s_add_u32 s88,s88,s66
s_mul_i32 s88,s88,0x60000
s_add_u32 s28,s6,s88
s_addc_u32 s29,s7,0
s_and_b32 s29,s29,0xffff
s_mov_b32 s30,0x60000
s_mov_b32 s31,0x20000
s_add_u32 s36,s8,s88
s_addc_u32 s37,s9,0
s_and_b32 s37,s37,0xffff
s_mov_b32 s38,0x60000
s_mov_b32 s39,0x20000
s_mul_i32 s35,s64,0xc00
s_lshl_b32 s89,s92,3
v_add_u32 v23,0,v16
v_mul_u32_u24 v24,0xaab,v23
v_lshrrev_b32 v24,16,v24
v_and_b32 v25,6,v24
v_xor_b32 v25,v25,v16
v_lshl_add_u32 v231,v25,4,s35
v_add_u32 v24,s89,v24
v_lshrrev_b32 v24,1,v24
v_xor_b32 v24,v24,v16
v_lshl_add_u32 v228,v24,4,s35
v_add_u32 v23,64,v16
v_mul_u32_u24 v24,0xaab,v23
v_lshrrev_b32 v24,16,v24
v_and_b32 v25,6,v24
v_xor_b32 v25,v25,v16
v_lshl_add_u32 v246,v25,4,s35
v_add_u32 v24,s89,v24
v_lshrrev_b32 v24,1,v24
v_xor_b32 v24,v24,v16
v_lshl_add_u32 v229,v24,4,s35
v_add_u32 v23,0x80,v16
v_mul_u32_u24 v24,0xaab,v23
v_lshrrev_b32 v24,16,v24
v_and_b32 v25,6,v24
v_xor_b32 v25,v25,v16
v_lshl_add_u32 v247,v25,4,s35
v_add_u32 v24,s89,v24
v_lshrrev_b32 v24,1,v24
v_xor_b32 v24,v24,v16
v_lshl_add_u32 v230,v24,4,s35
s_mov_b32 s80,0
s_mov_b32 s83,0
s_mov_b32 s82,0x1f000
s_mov_b32 s84,0x13000
s_mov_b32 s96,0x3000
s_mov_b32 s97,0xc000
s_mov_b32 s98,0xffff7000
s_mov_b32 s99,0x28000
s_mov_b32 s34,0x1c000
s_lshl_b32 s88,s65,11
s_add_u32 s88,s88,0x1000
s_add_u32 s52,s16,s88
s_addc_u32 s53,s17,0
s_mul_i32 s88,s66,0x78000
s_add_u32 s40,s12,s88
s_addc_u32 s41,s13,0
s_and_b32 s41,s41,0xffff
s_mov_b32 s42,0x78000
s_mov_b32 s43,0x20000
s_mul_i32 s88,s65,0x230000
s_add_u32 s88,s88,0x10000
s_add_u32 s44,s16,s88
s_addc_u32 s45,s17,0
s_and_b32 s45,s45,0xffff
s_mov_b32 s46,0x230000
s_mov_b32 s47,0x20000
s_mul_i32 s88,s65,0x23000
s_add_u32 s48,s14,s88
s_addc_u32 s49,s15,0
s_and_b32 s49,s49,0xffff
s_mov_b32 s50,0x23000
s_mov_b32 s51,0x20000
v_and_b32 v18,15,v16
v_lshrrev_b32 v19,2,v18
v_and_b32 v20,3,v16
v_lshlrev_b32 v239,4,v16
v_and_b32 v23,1,v17
v_lshlrev_b32 v24,1,v23
v_lshrrev_b32 v25,1,v19
v_or_b32 v24,v24,v25
v_lshlrev_b32 v21,3,v20
v_lshl_add_u32 v21,v19,7,v21
v_mul_u32_u24 v22,0xc00,v17
v_add_u32 v22,v22,v21
v_xor_b32 v25,0,v24
v_lshl_add_u32 v222,v25,5,v22
v_xor_b32 v25,1,v24
v_lshl_add_u32 v223,v25,5,v22
v_xor_b32 v25,2,v24
v_lshl_add_u32 v224,v25,5,v22
v_xor_b32 v25,3,v24
v_lshl_add_u32 v225,v25,5,v22
v_lshl_add_u32 v22,v17,2,v19
v_mul_u32_u24 v22,0x180,v22
v_lshl_add_u32 v22,v20,3,v22
v_add_u32 v22,0x13000,v22
v_xor_b32 v25,0,v24
v_lshl_add_u32 v218,v25,5,v22
v_xor_b32 v25,1,v24
v_lshl_add_u32 v219,v25,5,v22
v_xor_b32 v25,2,v24
v_lshl_add_u32 v220,v25,5,v22
v_xor_b32 v25,3,v24
v_lshl_add_u32 v221,v25,5,v22
s_lshl_b32 s88,s64,12
s_add_u32 s88,s88,0xf000
v_lshlrev_b32 v21,7,v18
v_add_u32 v21,s88,v21
v_lshrrev_b32 v24,1,v18
v_mul_u32_u24 v23,0x180,v18
v_add_u32 v23,0x1f000,v23
v_add_u32 v25,0,v17
v_xor_b32 v25,v25,v24
v_lshl_add_u32 v226,v25,4,v21
v_lshl_add_u32 v216,v25,4,v23
v_add_u32 v32,0xe000,v226
v_add_u32 v25,4,v17
v_xor_b32 v25,v25,v24
v_lshl_add_u32 v227,v25,4,v21
v_lshl_add_u32 v217,v25,4,v23
v_add_u32 v33,0xe000,v227
v_mov_b32 v23,0x3f803f80
v_accvgpr_write_b32 a240,v23
v_accvgpr_write_b32 a241,v23
v_accvgpr_write_b32 a242,v23
v_accvgpr_write_b32 a243,v23
s_mov_b32 s85,0x3000
s_mov_b32 s86,0x3000
s_mov_b32 s58,0x3dd53b95
)"
R"(s_mov_b32 s59,0x3dd53b95
s_mov_b32 s87,0x41000000
v_xor_b32 v52,64,v228
v_add_u32 v52,0xffffdc00,v52
v_add_u32 v76,0xffffdc00,v231
v_xor_b32 v53,64,v229
v_add_u32 v53,0xffffdc00,v53
v_add_u32 v77,0xffffdc00,v246
v_xor_b32 v54,64,v230
v_add_u32 v54,0xffffdc00,v54
v_add_u32 v78,0xffffdc00,v247
v_add_u32 v55,0xffffdc00,v228
v_add_u32 v79,0xffffdc00,v231
v_add_u32 v60,0xffffec00,v229
v_add_u32 v84,0xffffec00,v246
v_add_u32 v61,0xffffec00,v230
v_add_u32 v85,0xffffec00,v247
v_xor_b32 v62,64,v228
v_add_u32 v62,0xffffec00,v62
v_add_u32 v86,0xffffec00,v231
v_xor_b32 v63,64,v229
v_add_u32 v63,0xffffec00,v63
v_add_u32 v87,0xffffec00,v246
v_xor_b32 v68,64,v230
v_add_u32 v68,0xfffffc00,v68
v_add_u32 v92,0xfffffc00,v247
v_add_u32 v69,0xfffffc00,v228
v_add_u32 v93,0xfffffc00,v231
v_add_u32 v70,0xfffffc00,v229
v_add_u32 v94,0xfffffc00,v246
v_add_u32 v71,0xfffffc00,v230
v_add_u32 v95,0xfffffc00,v247
v_xor_b32 v16,64,v234
v_add_u32 v100,0xc00,v16
v_add_u32 v101,0x880,v16
v_add_u32 v102,0x500,v16
v_add_u32 v103,0x18000,v234
v_add_u32 v108,0x18c80,v234
v_add_u32 v109,0x18900,v234
v_add_u32 v110,0x30400,v16
v_add_u32 v111,0x30080,v16
v_add_u32 v116,0x30d00,v16
v_add_u32 v117,0x48800,v234
v_add_u32 v118,0x48480,v234
v_add_u32 v119,0x48100,v234
v_add_u32 v124,0xffffdc00,v236
v_add_u32 v125,0x2400,v235
v_add_u32 v126,0x2400,v236
v_add_u32 v127,0x6c00,v235
v_add_u32 v132,0x7c00,v236
v_add_u32 v133,0xc400,v235
v_add_u32 v134,0xc400,v236
v_add_u32 v135,0x10c00,v235
v_add_u32 v140,0x11c00,v236
v_add_u32 v141,0x16400,v235
v_add_u32 v142,0x16400,v236
v_add_u32 v143,0x1ac00,v235
v_add_u32 v148,0x1bc00,v236
v_add_u32 v149,0x20400,v235
s_add_u32 m0,s75,0x19000
s_nop 0
dl 232,24,72,0
dl 233,24,72,1024
dl 252,24,72,2048
s_add_u32 s90,s72,0x180
s_add_u32 m0,s75,0x1c000
s_nop 0
dl 232,24,90,0
dl 233,24,90,1024
dl 252,24,90,2048
s_add_u32 s72,s72,s95
s_cmp_ge_u32 s72,s74
s_cselect_b32 s72,s73,s72
s_add_u32 m0,s79,0x3800
s_nop 0
dl 235,20,76,0
dl 236,20,76,1024
dl 237,20,76,2048
dl 238,20,76,3072
s_add_u32 s90,s76,0xa000
s_add_u32 m0,s79,0x4800
s_nop 0
dl 235,20,90,0
s_addk_i32 s76,128
s_cmp_ge_u32 s76,s78
s_cselect_b32 s76,s77,s76
v_add_u32 v0,0x7000,v222
v_add_u32 v1,0x7000,v223
v_add_u32 v2,0x7000,v224
v_add_u32 v3,0x7000,v225
v_add_u32 v8,0x13000,v222
v_add_u32 v9,0x13000,v223
v_add_u32 v10,0x13000,v224
v_add_u32 v11,0x13000,v225
v_add_u32 v16,0xffff1000,v226
v_add_u32 v17,0xffff1000,v227
v_add_u32 v24,0x1f000,v16
v_add_u32 v25,0x1f000,v17
s_waitcnt vmcnt(15)
s_barrier
rd v,200,16,0
tr 48,8,0
tr 50,8,512
tr 52,9,0
tr 54,9,512
tr 56,10,0
tr 58,10,512
tr 60,11,0
tr 62,11,512
tr 64,8,1024
tr 66,8,1536
tr 68,9,1024
tr 70,9,1536
tr 72,10,1024
tr 74,10,1536
tr 76,11,1024
tr 78,11,1536
wl 12
mz v,48,a,48,v,200
tr 80,8,2048
tr 82,8,2560
mz v,56,a,52,v,200
wl 10
rd v,208,17,0
tr 84,9,2048
tr 86,9,2560
mz v,64,a,56,v,200
s_waitcnt vmcnt(12)
tr 88,10,2048
tr 90,10,2560
s_barrier
mz v,72,a,60,v,200
wl 11
tr 92,11,2048
tr 94,11,2560
mz v,80,a,64,v,200
tr 48,8,12288
tr 50,8,12800
mz v,88,a,68,v,200
wl 11
tr 52,9,12288
tr 54,9,12800
mz v,96,a,72,v,200
tr 56,10,12288
tr 58,10,12800
mz v,104,a,76,v,200
wl 10
tr 60,11,12288
tr 62,11,12800
mz v,112,a,80,v,200
tr 64,8,13312
tr 66,8,13824
mz v,120,a,84,v,200
wl 10
tr 68,9,13312
tr 70,9,13824
mz v,128,a,88,v,200
tr 72,10,13312
tr 74,10,13824
mz v,136,a,92,v,200
wl 10
tr 76,11,13312
tr 78,11,13824
mz a,96,a,48,v,200
tr 80,8,14336
tr 82,8,14848
mz a,104,a,52,v,200
wl 10
tr 84,9,14336
tr 86,9,14848
mz a,112,a,56,v,200
s_waitcnt vmcnt(5)
tr 88,10,14336
tr 90,10,14848
s_barrier
mz a,120,a,60,v,200
wl 10
tr 92,11,14336
tr 94,11,14848
mz a,128,a,64,v,200
s_add_u32 m0,s75,0x7000
tr 48,8,24576
tr 50,8,25088
dl 232,24,72,0
mz a,136,a,68,v,200
wl 10
dl 233,24,72,1024
tr 52,9,24576
tr 54,9,25088
dl 252,24,72,2048
s_add_u32 s90,s72,0x180
s_add_u32 m0,s75,0xa000
mz a,144,a,72,v,200
dl 232,24,90,0
tr 56,10,24576
tr 58,10,25088
dl 233,24,90,1024
mz a,152,a,76,v,200
wl 10
dl 252,24,90,2048
)"
R"(tr 60,11,24576
tr 62,11,25088
s_add_u32 s72,s72,s95
s_add_u32 m0,s75,0xd000
s_nop 0
dl 232,24,72,0
dl 233,24,72,1024
mz a,160,a,80,v,200
dl 252,24,72,2048
tr 64,8,25600
tr 66,8,26112
s_add_u32 s90,s72,0x180
s_add_u32 m0,s75,0x10000
s_nop 0
dl 232,24,90,0
mz a,168,a,84,v,200
wl 10
dl 233,24,90,1024
tr 68,9,25600
tr 70,9,26112
dl 252,24,90,2048
s_add_u32 s72,s72,s95
s_cmp_ge_u32 s72,s74
s_cselect_b32 s72,s73,s72
s_add_u32 m0,s79,0x1f000
s_nop 0
dl 235,20,76,0
mz a,176,a,88,v,200
wl 8
dl 236,20,76,1024
tr 72,10,25600
tr 74,10,26112
dl 237,20,76,2048
mz a,184,a,92,v,200
tr 76,11,25600
tr 78,11,26112
dl 238,20,76,3072
s_add_u32 s90,s76,0xa000
s_add_u32 m0,s79,0x20000
s_nop 0
dl 235,20,90,0
s_addk_i32 s76,128
s_cmp_ge_u32 s76,s78
s_cselect_b32 s76,s77,s76
s_waitcnt vmcnt(11)
s_barrier
ma v,48,a,48,v,208
s_add_u32 m0,s75,0x13000
tr 80,8,26624
tr 82,8,27136
ma v,56,a,52,v,208
wl 10
rd v,200,16,14336
tr 84,9,26624
tr 86,9,27136
ma v,64,a,56,v,208
dl 232,24,72,0
tr 88,10,26624
tr 90,10,27136
ma v,72,a,60,v,208
wl 11
dl 233,24,72,1024
tr 92,11,26624
tr 94,11,27136
ma v,80,a,64,v,208
tr 48,8,36864
tr 50,8,37376
ma v,88,a,68,v,208
wl 11
tr 52,9,36864
tr 54,9,37376
ma v,96,a,72,v,208
dl 252,24,72,2048
tr 56,10,36864
tr 58,10,37376
ma v,104,a,76,v,208
wl 10
tr 60,11,36864
tr 62,11,37376
ma v,112,a,80,v,208
s_add_u32 s90,s72,0x180
tr 64,8,37888
tr 66,8,38400
ma v,120,a,84,v,208
s_add_u32 m0,s75,0x16000
tr 68,9,37888
tr 70,9,38400
wl 12
ma v,128,a,88,v,208
dl 232,24,90,0
tr 72,10,37888
tr 74,10,38400
ma v,136,a,92,v,208
wl 10
tr 76,11,37888
tr 78,11,38400
ma a,96,a,48,v,208
dl 233,24,90,1024
tr 80,8,38912
tr 82,8,39424
ma a,104,a,52,v,208
wl 10
tr 84,9,38912
tr 86,9,39424
ma a,112,a,56,v,208
tr 88,10,38912
tr 90,10,39424
ma a,120,a,60,v,208
wl 10
dl 252,24,90,2048
tr 92,11,38912
tr 94,11,39424
ma a,128,a,64,v,208
s_add_u32 s72,s72,s95
tr 48,0,0
tr 50,0,512
ma a,136,a,68,v,208
wl 10
tr 52,1,0
tr 54,1,512
ma a,144,a,72,v,208
tr 56,2,0
tr 58,2,512
ma a,152,a,76,v,208
wl 10
tr 60,3,0
tr 62,3,512
ma a,160,a,80,v,208
tr 64,0,1024
tr 66,0,1536
ma a,168,a,84,v,208
wl 10
tr 68,1,1024
tr 70,1,1536
ma a,176,a,88,v,208
wl 8
tr 72,2,1024
tr 74,2,1536
ma a,184,a,92,v,208
s_waitcnt vmcnt(11)
tr 76,3,1024
tr 78,3,1536
s_barrier
ma v,48,a,48,v,200
s_add_u32 m0,s75,0x19000
tr 80,0,2048
tr 82,0,2560
ma v,56,a,52,v,200
wl 10
rd v,208,17,14336
tr 84,1,2048
tr 86,1,2560
ma v,64,a,56,v,200
dl 232,24,72,0
tr 88,2,2048
tr 90,2,2560
ma v,72,a,60,v,200
wl 11
dl 233,24,72,1024
tr 92,3,2048
tr 94,3,2560
ma v,80,a,64,v,200
dl 252,24,72,2048
tr 48,0,12288
tr 50,0,12800
ma v,88,a,68,v,200
s_add_u32 s90,s72,0x180
tr 52,1,12288
tr 54,1,12800
wl 13
ma v,96,a,72,v,200
s_add_u32 m0,s75,0x1c000
tr 56,2,12288
tr 58,2,12800
ma v,104,a,76,v,200
wl 10
dl 232,24,90,0
tr 60,3,12288
tr 62,3,12800
ma v,112,a,80,v,200
dl 233,24,90,1024
tr 64,0,13312
tr 66,0,13824
ma v,120,a,84,v,200
wl 10
dl 252,24,90,2048
tr 68,1,13312
tr 70,1,13824
ma v,128,a,88,v,200
s_add_u32 s72,s72,s95
tr 72,2,13312
tr 74,2,13824
s_cmp_ge_u32 s72,s74
s_cselect_b32 s72,s73,s72
s_add_u32 m0,s79,0x22800
ma v,136,a,92,v,200
wl 10
dl 235,20,76,0
tr 76,3,13312
tr 78,3,13824
)"
R"(ma a,96,a,48,v,200
dl 236,20,76,1024
tr 80,0,14336
tr 82,0,14848
ma a,104,a,52,v,200
wl 10
dl 237,20,76,2048
tr 84,1,14336
tr 86,1,14848
ma a,112,a,56,v,200
dl 238,20,76,3072
tr 88,2,14336
tr 90,2,14848
ma a,120,a,60,v,200
s_add_u32 s90,s76,0xa000
tr 92,3,14336
tr 94,3,14848
wl 12
ma a,128,a,64,v,200
s_add_u32 m0,s79,0x23800
tr 48,0,24576
tr 50,0,25088
ma a,136,a,68,v,200
wl 10
dl 235,20,90,0
tr 52,1,24576
tr 54,1,25088
ma a,144,a,72,v,200
s_addk_i32 s76,128
tr 56,2,24576
tr 58,2,25088
s_cmp_ge_u32 s76,s78
s_cselect_b32 s76,s77,s76
ma a,152,a,76,v,200
wl 10
tr 60,3,24576
tr 62,3,25088
ma a,160,a,80,v,200
tr 64,0,25600
tr 66,0,26112
ma a,168,a,84,v,200
wl 10
tr 68,1,25600
tr 70,1,26112
ma a,176,a,88,v,200
wl 8
tr 72,2,25600
tr 74,2,26112
ma a,184,a,92,v,200
s_waitcnt vmcnt(11)
tr 76,3,25600
tr 78,3,26112
s_barrier
ma v,48,a,48,v,208
s_add_u32 m0,s75,0x7000
tr 80,0,26624
tr 82,0,27136
ma v,56,a,52,v,208
wl 10
rd v,200,24,0
tr 84,1,26624
tr 86,1,27136
ma v,64,a,56,v,208
dl 232,24,72,0
tr 88,2,26624
tr 90,2,27136
ma v,72,a,60,v,208
wl 11
dl 233,24,72,1024
tr 92,3,26624
tr 94,3,27136
ma v,80,a,64,v,208
tr 48,0,36864
tr 50,0,37376
ma v,88,a,68,v,208
wl 11
tr 52,1,36864
tr 54,1,37376
ma v,96,a,72,v,208
dl 252,24,72,2048
tr 56,2,36864
tr 58,2,37376
ma v,104,a,76,v,208
wl 10
tr 60,3,36864
tr 62,3,37376
ma v,112,a,80,v,208
s_add_u32 s90,s72,0x180
tr 64,0,37888
tr 66,0,38400
ma v,120,a,84,v,208
s_add_u32 m0,s75,0xa000
tr 68,1,37888
tr 70,1,38400
wl 12
ma v,128,a,88,v,208
dl 232,24,90,0
tr 72,2,37888
tr 74,2,38400
ma v,136,a,92,v,208
wl 10
tr 76,3,37888
tr 78,3,38400
ma a,96,a,48,v,208
dl 233,24,90,1024
tr 80,0,38912
tr 82,0,39424
ma a,104,a,52,v,208
wl 10
tr 84,1,38912
tr 86,1,39424
ma a,112,a,56,v,208
tr 88,2,38912
tr 90,2,39424
ma a,120,a,60,v,208
wl 10
dl 252,24,90,2048
tr 92,3,38912
tr 94,3,39424
ma a,128,a,64,v,208
s_add_u32 s72,s72,s95
tr 48,8,0
tr 50,8,512
ma a,136,a,68,v,208
wl 10
tr 52,9,0
tr 54,9,512
ma a,144,a,72,v,208
tr 56,10,0
tr 58,10,512
ma a,152,a,76,v,208
wl 10
tr 60,11,0
tr 62,11,512
ma a,160,a,80,v,208
tr 64,8,1024
tr 66,8,1536
ma a,168,a,84,v,208
wl 10
tr 68,9,1024
tr 70,9,1536
ma a,176,a,88,v,208
wl 8
tr 72,10,1024
tr 74,10,1536
ma a,184,a,92,v,208
s_waitcnt vmcnt(11)
tr 76,11,1024
tr 78,11,1536
s_barrier
ma v,48,a,48,v,200
s_add_u32 m0,s75,0xd000
tr 80,8,2048
tr 82,8,2560
ma v,56,a,52,v,200
wl 10
rd v,208,25,0
tr 84,9,2048
tr 86,9,2560
ma v,64,a,56,v,200
dl 232,24,72,0
tr 88,10,2048
tr 90,10,2560
ma v,72,a,60,v,200
wl 11
dl 233,24,72,1024
tr 92,11,2048
tr 94,11,2560
ma v,80,a,64,v,200
dl 252,24,72,2048
tr 48,8,12288
tr 50,8,12800
ma v,88,a,68,v,200
s_add_u32 s90,s72,0x180
tr 52,9,12288
tr 54,9,12800
wl 13
ma v,96,a,72,v,200
s_add_u32 m0,s75,0x10000
tr 56,10,12288
tr 58,10,12800
ma v,104,a,76,v,200
wl 10
dl 232,24,90,0
tr 60,11,12288
tr 62,11,12800
ma v,112,a,80,v,200
dl 233,24,90,1024
tr 64,8,13312
tr 66,8,13824
ma v,120,a,84,v,200
wl 10
dl 252,24,90,2048
tr 68,9,13312
tr 70,9,13824
ma v,128,a,88,v,200
s_add_u32 s72,s72,s95
tr 72,10,13312
tr 74,10,13824
s_cmp_ge_u32 s72,s74
)"
R"(s_cselect_b32 s72,s73,s72
s_mov_b32 m0,s79
ma v,136,a,92,v,200
wl 10
dl 235,20,76,0
tr 76,11,13312
tr 78,11,13824
ma a,96,a,48,v,200
dl 236,20,76,1024
tr 80,8,14336
tr 82,8,14848
ma a,104,a,52,v,200
wl 10
dl 237,20,76,2048
tr 84,9,14336
tr 86,9,14848
ma a,112,a,56,v,200
dl 238,20,76,3072
tr 88,10,14336
tr 90,10,14848
ma a,120,a,60,v,200
s_add_u32 s90,s76,0xa000
tr 92,11,14336
tr 94,11,14848
wl 12
ma a,128,a,64,v,200
s_add_u32 m0,s79,0x1000
tr 48,8,24576
tr 50,8,25088
ma a,136,a,68,v,200
wl 10
dl 235,20,90,0
tr 52,9,24576
tr 54,9,25088
ma a,144,a,72,v,200
s_addk_i32 s76,128
tr 56,10,24576
tr 58,10,25088
s_cmp_ge_u32 s76,s78
s_cselect_b32 s76,s77,s76
ma a,152,a,76,v,200
wl 10
tr 60,11,24576
tr 62,11,25088
ma a,160,a,80,v,200
tr 64,8,25600
tr 66,8,26112
ma a,168,a,84,v,200
wl 10
tr 68,9,25600
tr 70,9,26112
ma a,176,a,88,v,200
wl 8
tr 72,10,25600
tr 74,10,26112
ma a,184,a,92,v,200
s_waitcnt vmcnt(11)
tr 76,11,25600
tr 78,11,26112
s_barrier
ma v,48,a,48,v,208
s_add_u32 m0,s75,0x13000
tr 80,8,26624
tr 82,8,27136
ma v,56,a,52,v,208
wl 10
rd v,200,24,14336
tr 84,9,26624
tr 86,9,27136
ma v,64,a,56,v,208
dl 232,24,72,0
tr 88,10,26624
tr 90,10,27136
ma v,72,a,60,v,208
wl 11
dl 233,24,72,1024
tr 92,11,26624
tr 94,11,27136
ma v,80,a,64,v,208
tr 48,8,36864
tr 50,8,37376
ma v,88,a,68,v,208
wl 11
tr 52,9,36864
tr 54,9,37376
ma v,96,a,72,v,208
dl 252,24,72,2048
tr 56,10,36864
tr 58,10,37376
ma v,104,a,76,v,208
wl 10
tr 60,11,36864
tr 62,11,37376
ma v,112,a,80,v,208
s_add_u32 s90,s72,0x180
tr 64,8,37888
tr 66,8,38400
ma v,120,a,84,v,208
s_add_u32 m0,s75,0x16000
tr 68,9,37888
tr 70,9,38400
wl 12
ma v,128,a,88,v,208
dl 232,24,90,0
tr 72,10,37888
tr 74,10,38400
ma v,136,a,92,v,208
wl 10
tr 76,11,37888
tr 78,11,38400
ma a,96,a,48,v,208
dl 233,24,90,1024
tr 80,8,38912
tr 82,8,39424
ma a,104,a,52,v,208
wl 10
tr 84,9,38912
tr 86,9,39424
ma a,112,a,56,v,208
tr 88,10,38912
tr 90,10,39424
ma a,120,a,60,v,208
wl 10
dl 252,24,90,2048
tr 92,11,38912
tr 94,11,39424
ma a,128,a,64,v,208
s_add_u32 s72,s72,s95
tr 48,0,0
tr 50,0,512
ma a,136,a,68,v,208
wl 10
tr 52,1,0
tr 54,1,512
ma a,144,a,72,v,208
tr 56,2,0
tr 58,2,512
ma a,152,a,76,v,208
wl 10
tr 60,3,0
tr 62,3,512
ma a,160,a,80,v,208
tr 64,0,1024
tr 66,0,1536
ma a,168,a,84,v,208
wl 10
tr 68,1,1024
tr 70,1,1536
ma a,176,a,88,v,208
wl 8
tr 72,2,1024
tr 74,2,1536
ma a,184,a,92,v,208
s_waitcnt vmcnt(11)
tr 76,3,1024
tr 78,3,1536
s_barrier
ma v,48,a,48,v,200
s_add_u32 m0,s75,0x19000
tr 80,0,2048
tr 82,0,2560
ma v,56,a,52,v,200
wl 10
rd v,208,25,14336
tr 84,1,2048
tr 86,1,2560
ma v,64,a,56,v,200
dl 232,24,72,0
tr 88,2,2048
tr 90,2,2560
ma v,72,a,60,v,200
wl 11
dl 233,24,72,1024
tr 92,3,2048
tr 94,3,2560
ma v,80,a,64,v,200
dl 252,24,72,2048
tr 48,0,12288
tr 50,0,12800
ma v,88,a,68,v,200
s_add_u32 s90,s72,0x180
tr 52,1,12288
tr 54,1,12800
wl 13
ma v,96,a,72,v,200
s_add_u32 m0,s75,0x1c000
tr 56,2,12288
tr 58,2,12800
ma v,104,a,76,v,200
wl 10
dl 232,24,90,0
tr 60,3,12288
tr 62,3,12800
ma v,112,a,80,v,200
dl 233,24,90,1024
tr 64,0,13312
tr 66,0,13824
ma v,120,a,84,v,200
wl 10
dl 252,24,90,2048
)"
R"(tr 68,1,13312
tr 70,1,13824
ma v,128,a,88,v,200
s_add_u32 s72,s72,s95
tr 72,2,13312
tr 74,2,13824
s_cmp_ge_u32 s72,s74
s_cselect_b32 s72,s73,s72
s_add_u32 m0,s79,0x3800
ma v,136,a,92,v,200
wl 10
dl 235,20,76,0
tr 76,3,13312
tr 78,3,13824
ma a,96,a,48,v,200
dl 236,20,76,1024
tr 80,0,14336
tr 82,0,14848
ma a,104,a,52,v,200
wl 10
dl 237,20,76,2048
tr 84,1,14336
tr 86,1,14848
ma a,112,a,56,v,200
dl 238,20,76,3072
tr 88,2,14336
tr 90,2,14848
ma a,120,a,60,v,200
s_add_u32 s90,s76,0xa000
tr 92,3,14336
tr 94,3,14848
wl 12
ma a,128,a,64,v,200
s_add_u32 m0,s79,0x4800
tr 48,0,24576
tr 50,0,25088
ma a,136,a,68,v,200
wl 10
dl 235,20,90,0
tr 52,1,24576
tr 54,1,25088
ma a,144,a,72,v,200
s_addk_i32 s76,128
tr 56,2,24576
tr 58,2,25088
s_cmp_ge_u32 s76,s78
s_cselect_b32 s76,s77,s76
ma a,152,a,76,v,200
wl 10
tr 60,3,24576
tr 62,3,25088
ma a,160,a,80,v,200
tr 64,0,25600
tr 66,0,26112
ma a,168,a,84,v,200
wl 10
tr 68,1,25600
tr 70,1,26112
ma a,176,a,88,v,200
wl 8
tr 72,2,25600
tr 74,2,26112
ma a,184,a,92,v,200
s_waitcnt vmcnt(11)
tr 76,3,25600
tr 78,3,26112
s_barrier
ma v,48,a,48,v,208
s_add_u32 m0,s75,0x7000
tr 80,0,26624
tr 82,0,27136
ma v,56,a,52,v,208
wl 10
rd v,200,16,0
tr 84,1,26624
tr 86,1,27136
ma v,64,a,56,v,208
dl 232,24,72,0
tr 88,2,26624
tr 90,2,27136
ma v,72,a,60,v,208
wl 11
dl 233,24,72,1024
tr 92,3,26624
tr 94,3,27136
ma v,80,a,64,v,208
tr 48,0,36864
tr 50,0,37376
ma v,88,a,68,v,208
wl 11
tr 52,1,36864
tr 54,1,37376
ma v,96,a,72,v,208
dl 252,24,72,2048
tr 56,2,36864
tr 58,2,37376
ma v,104,a,76,v,208
wl 10
tr 60,3,36864
tr 62,3,37376
ma v,112,a,80,v,208
s_add_u32 s90,s72,0x180
tr 64,0,37888
tr 66,0,38400
ma v,120,a,84,v,208
s_add_u32 m0,s75,0xa000
tr 68,1,37888
tr 70,1,38400
wl 12
ma v,128,a,88,v,208
dl 232,24,90,0
tr 72,2,37888
tr 74,2,38400
ma v,136,a,92,v,208
wl 10
tr 76,3,37888
tr 78,3,38400
ma a,96,a,48,v,208
dl 233,24,90,1024
tr 80,0,38912
tr 82,0,39424
ma a,104,a,52,v,208
wl 10
tr 84,1,38912
tr 86,1,39424
ma a,112,a,56,v,208
tr 88,2,38912
tr 90,2,39424
ma a,120,a,60,v,208
wl 10
dl 252,24,90,2048
tr 92,3,38912
tr 94,3,39424
ma a,128,a,64,v,208
s_add_u32 s72,s72,s95
tr 48,8,0
tr 50,8,512
ma a,136,a,68,v,208
wl 10
tr 52,9,0
tr 54,9,512
ma a,144,a,72,v,208
tr 56,10,0
tr 58,10,512
ma a,152,a,76,v,208
wl 10
tr 60,11,0
tr 62,11,512
ma a,160,a,80,v,208
tr 64,8,1024
tr 66,8,1536
ma a,168,a,84,v,208
wl 10
tr 68,9,1024
tr 70,9,1536
ma a,176,a,88,v,208
wl 8
tr 72,10,1024
tr 74,10,1536
ma a,184,a,92,v,208
s_waitcnt vmcnt(11)
tr 76,11,1024
tr 78,11,1536
s_barrier
ma v,48,a,48,v,200
s_add_u32 m0,s75,0xd000
tr 80,8,2048
tr 82,8,2560
ma v,56,a,52,v,200
wl 10
rd v,208,17,0
tr 84,9,2048
tr 86,9,2560
ma v,64,a,56,v,200
dl 232,24,72,0
tr 88,10,2048
tr 90,10,2560
ma v,72,a,60,v,200
wl 11
dl 233,24,72,1024
tr 92,11,2048
tr 94,11,2560
ma v,80,a,64,v,200
tr 48,8,12288
tr 50,8,12800
ma v,88,a,68,v,200
wl 11
tr 52,9,12288
tr 54,9,12800
ma v,96,a,72,v,200
dl 252,24,72,2048
tr 56,10,12288
tr 58,10,12800
ma v,104,a,76,v,200
wl 10
tr 60,11,12288
tr 62,11,12800
ma v,112,a,80,v,200
s_add_u32 s90,s72,0x180
tr 64,8,13312
)"
R"(tr 66,8,13824
ma v,120,a,84,v,200
s_add_u32 m0,s75,0x10000
tr 68,9,13312
tr 70,9,13824
wl 12
ma v,128,a,88,v,200
dl 232,24,90,0
tr 72,10,13312
tr 74,10,13824
ma v,136,a,92,v,200
wl 10
tr 76,11,13312
tr 78,11,13824
ma a,96,a,48,v,200
dl 233,24,90,1024
tr 80,8,14336
tr 82,8,14848
ma a,104,a,52,v,200
wl 10
tr 84,9,14336
tr 86,9,14848
ma a,112,a,56,v,200
tr 88,10,14336
tr 90,10,14848
ma a,120,a,60,v,200
wl 10
dl 252,24,90,2048
tr 92,11,14336
tr 94,11,14848
ma a,128,a,64,v,200
s_add_u32 s72,s72,s95
tr 48,8,24576
tr 50,8,25088
ma a,136,a,68,v,200
s_cmp_ge_u32 s72,s74
tr 52,9,24576
tr 54,9,25088
wl 12
s_cselect_b32 s72,s73,s72
ma a,144,a,72,v,200
tr 56,10,24576
tr 58,10,25088
ma a,152,a,76,v,200
wl 10
tr 60,11,24576
tr 62,11,25088
ma a,160,a,80,v,200
tr 64,8,25600
tr 66,8,26112
ma a,168,a,84,v,200
wl 10
tr 68,9,25600
tr 70,9,26112
ma a,176,a,88,v,200
wl 8
tr 72,10,25600
tr 74,10,26112
ma a,184,a,92,v,200
s_waitcnt vmcnt(6)
tr 76,11,25600
tr 78,11,26112
s_barrier
ma v,48,a,48,v,208
s_add_u32 m0,s75,0x13000
tr 80,8,26624
tr 82,8,27136
ma v,56,a,52,v,208
wl 10
rd v,200,16,14336
tr 84,9,26624
tr 86,9,27136
ma v,64,a,56,v,208
dl 232,24,72,0
tr 88,10,26624
tr 90,10,27136
ma v,72,a,60,v,208
wl 11
dl 233,24,72,1024
tr 92,11,26624
tr 94,11,27136
ma v,80,a,64,v,208
dl 252,24,72,2048
tr 48,8,36864
tr 50,8,37376
ma v,88,a,68,v,208
s_add_u32 s90,s72,0x180
tr 52,9,36864
tr 54,9,37376
wl 13
ma v,96,a,72,v,208
s_add_u32 m0,s75,0x16000
tr 56,10,36864
tr 58,10,37376
ma v,104,a,76,v,208
wl 10
dl 232,24,90,0
tr 60,11,36864
tr 62,11,37376
ma v,112,a,80,v,208
dl 233,24,90,1024
tr 64,8,37888
tr 66,8,38400
ma v,120,a,84,v,208
wl 10
dl 252,24,90,2048
tr 68,9,37888
tr 70,9,38400
ma v,128,a,88,v,208
s_add_u32 s72,s72,s95
tr 72,10,37888
tr 74,10,38400
s_mov_b32 m0,s79
ma v,136,a,92,v,208
wl 10
dl 235,20,76,0
tr 76,11,37888
tr 78,11,38400
ma a,96,a,48,v,208
dl 236,20,76,1024
tr 80,8,38912
tr 82,8,39424
ma a,104,a,52,v,208
wl 10
dl 237,20,76,2048
tr 84,9,38912
tr 86,9,39424
ma a,112,a,56,v,208
dl 238,20,76,3072
tr 88,10,38912
tr 90,10,39424
ma a,120,a,60,v,208
s_add_u32 s90,s76,0xa000
tr 92,11,38912
tr 94,11,39424
wl 12
ma a,128,a,64,v,208
s_add_u32 m0,s79,0x1000
tr 48,0,0
tr 50,0,512
ma a,136,a,68,v,208
wl 10
dl 235,20,90,0
tr 52,1,0
tr 54,1,512
ma a,144,a,72,v,208
s_addk_i32 s76,128
tr 56,2,0
tr 58,2,512
s_cmp_ge_u32 s76,s78
s_cselect_b32 s76,s77,s76
ma a,152,a,76,v,208
wl 10
tr 60,3,0
tr 62,3,512
ma a,160,a,80,v,208
tr 64,0,1024
tr 66,0,1536
ma a,168,a,84,v,208
wl 10
tr 68,1,1024
tr 70,1,1536
ma a,176,a,88,v,208
wl 8
tr 72,2,1024
tr 74,2,1536
ma a,184,a,92,v,208
s_waitcnt vmcnt(11)
tr 76,3,1024
tr 78,3,1536
s_barrier
ma v,48,a,48,v,200
s_add_u32 m0,s75,0x19000
tr 80,0,2048
tr 82,0,2560
ma v,56,a,52,v,200
wl 10
rd v,208,17,14336
tr 84,1,2048
tr 86,1,2560
ma v,64,a,56,v,200
dl 232,24,72,0
tr 88,2,2048
tr 90,2,2560
ma v,72,a,60,v,200
wl 11
dl 233,24,72,1024
tr 92,3,2048
tr 94,3,2560
ma v,80,a,64,v,200
dl 252,24,72,2048
tr 48,0,12288
tr 50,0,12800
ma v,88,a,68,v,200
s_add_u32 s90,s72,0x180
tr 52,1,12288
tr 54,1,12800
wl 13
ma v,96,a,72,v,200
s_add_u32 m0,s75,0x1c000
)"
R"(tr 56,2,12288
tr 58,2,12800
ma v,104,a,76,v,200
wl 10
dl 232,24,90,0
tr 60,3,12288
tr 62,3,12800
ma v,112,a,80,v,200
dl 233,24,90,1024
tr 64,0,13312
tr 66,0,13824
ma v,120,a,84,v,200
wl 10
tr 68,1,13312
tr 70,1,13824
ma v,128,a,88,v,200
dl 252,24,90,2048
tr 72,2,13312
tr 74,2,13824
ma v,136,a,92,v,200
s_add_u32 s72,s72,s95
tr 76,3,13312
tr 78,3,13824
wl 12
ma a,96,a,48,v,200
s_cmp_ge_u32 s72,s74
tr 80,0,14336
tr 82,0,14848
s_cselect_b32 s72,s73,s72
s_add_u32 m0,s82,s35
ma a,104,a,52,v,200
wl 10
dn 228,28,80,0
tr 84,1,14336
tr 86,1,14848
ma a,112,a,56,v,200
dn 229,28,80,1024
tr 88,2,14336
tr 90,2,14848
ma a,120,a,60,v,200
wl 10
tr 92,3,14336
tr 94,3,14848
ma a,128,a,64,v,200
dn 230,28,80,2048
tr 48,0,24576
tr 50,0,25088
ma a,136,a,68,v,200
s_add_u32 s80,s80,s96
tr 52,1,24576
tr 54,1,25088
wl 12
ma a,144,a,72,v,200
s_add_u32 s82,s82,s96
tr 56,2,24576
tr 58,2,25088
s_cmp_ge_u32 s82,s99
s_cselect_b32 s89,s97,0
s_sub_u32 s82,s82,s89
ma a,152,a,76,v,200
wl 10
tr 60,3,24576
tr 62,3,25088
ma a,160,a,80,v,200
tr 64,0,25600
tr 66,0,26112
ma a,168,a,84,v,200
wl 10
tr 68,1,25600
tr 70,1,26112
ma a,176,a,88,v,200
wl 8
tr 72,2,25600
tr 74,2,26112
ma a,184,a,92,v,200
s_waitcnt vmcnt(9)
tr 76,3,25600
tr 78,3,26112
s_barrier
ma v,48,a,48,v,208
s_add_u32 m0,s75,0x7000
tr 80,0,26624
tr 82,0,27136
ma v,56,a,52,v,208
wl 10
rd v,200,16,0
tr 84,1,26624
tr 86,1,27136
ma v,64,a,56,v,208
dl 232,24,72,0
tr 88,2,26624
tr 90,2,27136
ma v,72,a,60,v,208
wl 11
dl 233,24,72,1024
tr 92,3,26624
tr 94,3,27136
ma v,80,a,64,v,208
dl 252,24,72,2048
tr 48,0,36864
tr 50,0,37376
ma v,88,a,68,v,208
s_add_u32 s90,s72,0x180
tr 52,1,36864
tr 54,1,37376
wl 13
ma v,96,a,72,v,208
s_add_u32 m0,s75,0xa000
tr 56,2,36864
tr 58,2,37376
ma v,104,a,76,v,208
wl 10
dl 232,24,90,0
tr 60,3,36864
tr 62,3,37376
ma v,112,a,80,v,208
dl 233,24,90,1024
tr 64,0,37888
tr 66,0,38400
ma v,120,a,84,v,208
wl 10
dl 252,24,90,2048
tr 68,1,37888
tr 70,1,38400
ma v,128,a,88,v,208
s_add_u32 s72,s72,s95
tr 72,2,37888
tr 74,2,38400
s_add_u32 m0,s79,0x3800
ma v,136,a,92,v,208
wl 10
dl 235,20,76,0
tr 76,3,37888
tr 78,3,38400
ma a,96,a,48,v,208
dl 236,20,76,1024
tr 80,0,38912
tr 82,0,39424
ma a,104,a,52,v,208
wl 10
dl 237,20,76,2048
tr 84,1,38912
tr 86,1,39424
ma a,112,a,56,v,208
dl 238,20,76,3072
tr 88,2,38912
tr 90,2,39424
ma a,120,a,60,v,208
s_add_u32 s90,s76,0xa000
tr 92,3,38912
tr 94,3,39424
wl 12
ma a,128,a,64,v,208
s_add_u32 m0,s79,0x4800
tr 48,8,0
tr 50,8,512
ma a,136,a,68,v,208
wl 10
dl 235,20,90,0
tr 52,9,0
tr 54,9,512
ma a,144,a,72,v,208
s_addk_i32 s76,128
tr 56,10,0
tr 58,10,512
s_cmp_ge_u32 s76,s78
s_cselect_b32 s76,s77,s76
ma a,152,a,76,v,208
wl 10
tr 60,11,0
tr 62,11,512
ma a,160,a,80,v,208
tr 64,8,1024
tr 66,8,1536
ma a,168,a,84,v,208
wl 10
tr 68,9,1024
tr 70,9,1536
ma a,176,a,88,v,208
wl 8
tr 72,10,1024
tr 74,10,1536
ma a,184,a,92,v,208
s_waitcnt vmcnt(14)
tr 76,11,1024
tr 78,11,1536
s_barrier
ma v,48,a,48,v,200
s_add_u32 m0,s75,0xd000
tr 80,8,2048
tr 82,8,2560
ma v,56,a,52,v,200
wl 10
rd v,208,17,0
tr 84,9,2048
tr 86,9,2560
ma v,64,a,56,v,200
dl 232,24,72,0
tr 88,10,2048
tr 90,10,2560
)"
R"(ma v,72,a,60,v,200
wl 11
dl 233,24,72,1024
tr 92,11,2048
tr 94,11,2560
ma v,80,a,64,v,200
dl 252,24,72,2048
tr 48,8,12288
tr 50,8,12800
ma v,88,a,68,v,200
s_add_u32 s90,s72,0x180
tr 52,9,12288
tr 54,9,12800
wl 13
ma v,96,a,72,v,200
s_add_u32 m0,s75,0x10000
tr 56,10,12288
tr 58,10,12800
ma v,104,a,76,v,200
wl 10
dl 232,24,90,0
tr 60,11,12288
tr 62,11,12800
ma v,112,a,80,v,200
dl 233,24,90,1024
tr 64,8,13312
tr 66,8,13824
ma v,120,a,84,v,200
wl 10
tr 68,9,13312
tr 70,9,13824
ma v,128,a,88,v,200
dl 252,24,90,2048
tr 72,10,13312
tr 74,10,13824
ma v,136,a,92,v,200
s_add_u32 s72,s72,s95
tr 76,11,13312
tr 78,11,13824
wl 12
ma a,96,a,48,v,200
s_cmp_ge_u32 s72,s74
tr 80,8,14336
tr 82,8,14848
s_cselect_b32 s72,s73,s72
s_add_u32 m0,s82,s35
ma a,104,a,52,v,200
wl 10
dn 228,28,80,0
tr 84,9,14336
tr 86,9,14848
ma a,112,a,56,v,200
dn 229,28,80,1024
tr 88,10,14336
tr 90,10,14848
ma a,120,a,60,v,200
wl 10
tr 92,11,14336
tr 94,11,14848
ma a,128,a,64,v,200
dn 230,28,80,2048
tr 48,8,24576
tr 50,8,25088
ma a,136,a,68,v,200
s_add_u32 s80,s80,s96
tr 52,9,24576
tr 54,9,25088
wl 12
ma a,144,a,72,v,200
s_add_u32 s82,s82,s96
tr 56,10,24576
tr 58,10,25088
s_cmp_ge_u32 s82,s99
s_cselect_b32 s89,s97,0
s_sub_u32 s82,s82,s89
ma a,152,a,76,v,200
wl 10
tr 60,11,24576
tr 62,11,25088
ma a,160,a,80,v,200
tr 64,8,25600
tr 66,8,26112
ma a,168,a,84,v,200
wl 10
tr 68,9,25600
tr 70,9,26112
ma a,176,a,88,v,200
wl 8
tr 72,10,25600
tr 74,10,26112
ma a,184,a,92,v,200
s_waitcnt vmcnt(9)
tr 76,11,25600
tr 78,11,26112
s_barrier
ma v,48,a,48,v,208
s_add_u32 m0,s75,0x13000
tr 80,8,26624
tr 82,8,27136
ma v,56,a,52,v,208
wl 10
rd v,200,16,14336
tr 84,9,26624
tr 86,9,27136
ma v,64,a,56,v,208
dl 232,24,72,0
tr 88,10,26624
tr 90,10,27136
ma v,72,a,60,v,208
wl 11
dl 233,24,72,1024
tr 92,11,26624
tr 94,11,27136
ma v,80,a,64,v,208
dl 252,24,72,2048
tr 48,8,36864
tr 50,8,37376
ma v,88,a,68,v,208
s_add_u32 s90,s72,0x180
tr 52,9,36864
tr 54,9,37376
wl 13
ma v,96,a,72,v,208
s_add_u32 m0,s75,0x16000
tr 56,10,36864
tr 58,10,37376
ma v,104,a,76,v,208
wl 10
dl 232,24,90,0
tr 60,11,36864
tr 62,11,37376
ma v,112,a,80,v,208
dl 233,24,90,1024
tr 64,8,37888
tr 66,8,38400
ma v,120,a,84,v,208
wl 10
dl 252,24,90,2048
tr 68,9,37888
tr 70,9,38400
ma v,128,a,88,v,208
s_add_u32 s72,s72,s95
tr 72,10,37888
tr 74,10,38400
s_mov_b32 m0,s79
ma v,136,a,92,v,208
wl 10
dl 235,20,76,0
tr 76,11,37888
tr 78,11,38400
ma a,96,a,48,v,208
dl 236,20,76,1024
tr 80,8,38912
tr 82,8,39424
ma a,104,a,52,v,208
wl 10
dl 237,20,76,2048
tr 84,9,38912
tr 86,9,39424
ma a,112,a,56,v,208
dl 238,20,76,3072
tr 88,10,38912
tr 90,10,39424
ma a,120,a,60,v,208
s_add_u32 s90,s76,0xa000
tr 92,11,38912
tr 94,11,39424
wl 12
ma a,128,a,64,v,208
s_add_u32 m0,s79,0x1000
tr 48,0,0
tr 50,0,512
ma a,136,a,68,v,208
wl 10
dl 235,20,90,0
tr 52,1,0
tr 54,1,512
ma a,144,a,72,v,208
s_addk_i32 s76,128
tr 56,2,0
tr 58,2,512
s_cmp_ge_u32 s76,s78
s_cselect_b32 s76,s77,s76
ma a,152,a,76,v,208
wl 10
tr 60,3,0
tr 62,3,512
ma a,160,a,80,v,208
tr 64,0,1024
tr 66,0,1536
ma a,168,a,84,v,208
wl 10
tr 68,1,1024
tr 70,1,1536
ma a,176,a,88,v,208
wl 8
tr 72,2,1024
tr 74,2,1536
ma a,184,a,92,v,208
s_waitcnt vmcnt(14)
)"
R"(tr 76,3,1024
tr 78,3,1536
s_barrier
ma v,48,a,48,v,200
s_add_u32 m0,s75,0x19000
tr 80,0,2048
tr 82,0,2560
ma v,56,a,52,v,200
wl 10
rd v,208,17,14336
tr 84,1,2048
tr 86,1,2560
ma v,64,a,56,v,200
dl 232,24,72,0
tr 88,2,2048
tr 90,2,2560
ma v,72,a,60,v,200
wl 11
dl 233,24,72,1024
tr 92,3,2048
tr 94,3,2560
ma v,80,a,64,v,200
dl 252,24,72,2048
tr 48,0,12288
tr 50,0,12800
ma v,88,a,68,v,200
s_add_u32 s90,s72,0x180
tr 52,1,12288
tr 54,1,12800
wl 13
ma v,96,a,72,v,200
s_add_u32 m0,s75,0x1c000
tr 56,2,12288
tr 58,2,12800
ma v,104,a,76,v,200
wl 10
dl 232,24,90,0
tr 60,3,12288
tr 62,3,12800
ma v,112,a,80,v,200
dl 233,24,90,1024
tr 64,0,13312
tr 66,0,13824
ma v,120,a,84,v,200
wl 10
tr 68,1,13312
tr 70,1,13824
ma v,128,a,88,v,200
dl 252,24,90,2048
tr 72,2,13312
tr 74,2,13824
ma v,136,a,92,v,200
s_add_u32 s72,s72,s95
tr 76,3,13312
tr 78,3,13824
wl 12
ma a,96,a,48,v,200
s_cmp_ge_u32 s72,s74
tr 80,0,14336
tr 82,0,14848
s_cselect_b32 s72,s73,s72
s_add_u32 m0,s82,s35
ma a,104,a,52,v,200
wl 10
dn 228,28,80,0
tr 84,1,14336
tr 86,1,14848
ma a,112,a,56,v,200
dn 229,28,80,1024
tr 88,2,14336
tr 90,2,14848
ma a,120,a,60,v,200
wl 10
tr 92,3,14336
tr 94,3,14848
ma a,128,a,64,v,200
dn 230,28,80,2048
tr 48,0,24576
tr 50,0,25088
ma a,136,a,68,v,200
s_add_u32 s80,s80,s96
tr 52,1,24576
tr 54,1,25088
wl 12
ma a,144,a,72,v,200
s_add_u32 s82,s82,s96
tr 56,2,24576
tr 58,2,25088
s_cmp_ge_u32 s82,s99
s_cselect_b32 s89,s97,0
s_sub_u32 s82,s82,s89
ma a,152,a,76,v,200
wl 10
tr 60,3,24576
tr 62,3,25088
ma a,160,a,80,v,200
tr 64,0,25600
tr 66,0,26112
ma a,168,a,84,v,200
wl 10
tr 68,1,25600
tr 70,1,26112
ma a,176,a,88,v,200
wl 8
tr 72,2,25600
tr 74,2,26112
ma a,184,a,92,v,200
s_waitcnt vmcnt(9)
tr 76,3,25600
tr 78,3,26112
s_barrier
ma v,48,a,48,v,208
s_add_u32 m0,s75,0x7000
tr 80,0,26624
tr 82,0,27136
ma v,56,a,52,v,208
wl 10
rd v,200,16,0
tr 84,1,26624
tr 86,1,27136
ma v,64,a,56,v,208
dl 232,24,72,0
tr 88,2,26624
tr 90,2,27136
ma v,72,a,60,v,208
wl 11
dl 233,24,72,1024
tr 92,3,26624
tr 94,3,27136
ma v,80,a,64,v,208
dl 252,24,72,2048
tr 48,0,36864
tr 50,0,37376
ma v,88,a,68,v,208
s_add_u32 s90,s72,0x180
tr 52,1,36864
tr 54,1,37376
wl 13
ma v,96,a,72,v,208
s_add_u32 m0,s75,0xa000
tr 56,2,36864
tr 58,2,37376
ma v,104,a,76,v,208
wl 10
dl 232,24,90,0
tr 60,3,36864
tr 62,3,37376
ma v,112,a,80,v,208
dl 233,24,90,1024
tr 64,0,37888
tr 66,0,38400
ma v,120,a,84,v,208
wl 10
dl 252,24,90,2048
tr 68,1,37888
tr 70,1,38400
ma v,128,a,88,v,208
s_add_u32 s72,s72,s95
tr 72,2,37888
tr 74,2,38400
s_add_u32 m0,s79,0x3800
ma v,136,a,92,v,208
wl 10
dl 235,20,76,0
tr 76,3,37888
tr 78,3,38400
ma a,96,a,48,v,208
dl 236,20,76,1024
tr 80,0,38912
tr 82,0,39424
ma a,104,a,52,v,208
wl 10
dl 237,20,76,2048
tr 84,1,38912
tr 86,1,39424
ma a,112,a,56,v,208
dl 238,20,76,3072
tr 88,2,38912
tr 90,2,39424
ma a,120,a,60,v,208
s_add_u32 s90,s76,0xa000
tr 92,3,38912
tr 94,3,39424
wl 12
ma a,128,a,64,v,208
s_add_u32 m0,s79,0x4800
tr 48,8,0
tr 50,8,512
ma a,136,a,68,v,208
wl 10
dl 235,20,90,0
tr 52,9,0
tr 54,9,512
ma a,144,a,72,v,208
s_addk_i32 s76,128
tr 56,10,0
tr 58,10,512
s_cmp_ge_u32 s76,s78
s_cselect_b32 s76,s77,s76
ma a,152,a,76,v,208
)"
R"(wl 10
tr 60,11,0
tr 62,11,512
ma a,160,a,80,v,208
tr 64,8,1024
tr 66,8,1536
ma a,168,a,84,v,208
wl 10
tr 68,9,1024
tr 70,9,1536
ma a,176,a,88,v,208
wl 8
tr 72,10,1024
tr 74,10,1536
ma a,184,a,92,v,208
s_waitcnt vmcnt(14)
tr 76,11,1024
tr 78,11,1536
s_barrier
ma v,48,a,48,v,200
s_add_u32 m0,s75,0xd000
tr 80,8,2048
tr 82,8,2560
ma v,56,a,52,v,200
wl 10
rd v,208,17,0
tr 84,9,2048
tr 86,9,2560
ma v,64,a,56,v,200
dl 232,24,72,0
tr 88,10,2048
tr 90,10,2560
ma v,72,a,60,v,200
wl 11
dl 233,24,72,1024
tr 92,11,2048
tr 94,11,2560
ma v,80,a,64,v,200
tr 48,8,12288
tr 50,8,12800
ma v,88,a,68,v,200
wl 11
tr 52,9,12288
tr 54,9,12800
ma v,96,a,72,v,200
dl 252,24,72,2048
tr 56,10,12288
tr 58,10,12800
ma v,104,a,76,v,200
wl 10
tr 60,11,12288
tr 62,11,12800
ma v,112,a,80,v,200
s_add_u32 s90,s72,0x180
tr 64,8,13312
tr 66,8,13824
ma v,120,a,84,v,200
s_add_u32 m0,s75,0x10000
tr 68,9,13312
tr 70,9,13824
wl 12
ma v,128,a,88,v,200
dl 232,24,90,0
tr 72,10,13312
tr 74,10,13824
ma v,136,a,92,v,200
wl 10
tr 76,11,13312
tr 78,11,13824
ma a,96,a,48,v,200
dl 233,24,90,1024
tr 80,8,14336
tr 82,8,14848
ma a,104,a,52,v,200
wl 10
tr 84,9,14336
tr 86,9,14848
ma a,112,a,56,v,200
tr 88,10,14336
tr 90,10,14848
ma a,120,a,60,v,200
wl 10
dl 252,24,90,2048
tr 92,11,14336
tr 94,11,14848
ma a,128,a,64,v,200
s_add_u32 s72,s72,s95
tr 48,8,24576
tr 50,8,25088
ma a,136,a,68,v,200
s_cmp_ge_u32 s72,s74
tr 52,9,24576
tr 54,9,25088
wl 12
s_cselect_b32 s72,s73,s72
ma a,144,a,72,v,200
tr 56,10,24576
tr 58,10,25088
ma a,152,a,76,v,200
wl 10
tr 60,11,24576
tr 62,11,25088
ma a,160,a,80,v,200
tr 64,8,25600
tr 66,8,26112
ma a,168,a,84,v,200
wl 10
tr 68,9,25600
tr 70,9,26112
ma a,176,a,88,v,200
wl 8
tr 72,10,25600
tr 74,10,26112
ma a,184,a,92,v,200
s_waitcnt vmcnt(6)
tr 76,11,25600
tr 78,11,26112
s_barrier
ma v,48,a,48,v,208
s_add_u32 m0,s84,s35
tr 80,8,26624
tr 82,8,27136
ma v,56,a,52,v,208
wl 10
rd v,200,16,14336
tr 84,9,26624
tr 86,9,27136
ma v,64,a,56,v,208
dn 231,36,83,0
tr 88,10,26624
tr 90,10,27136
ma v,72,a,60,v,208
wl 11
dn 246,36,83,1024
tr 92,11,26624
tr 94,11,27136
ma v,80,a,64,v,208
tr 48,8,36864
tr 50,8,37376
ma v,88,a,68,v,208
wl 11
tr 52,9,36864
tr 54,9,37376
ma v,96,a,72,v,208
dn 247,36,83,2048
tr 56,10,36864
tr 58,10,37376
ma v,104,a,76,v,208
wl 10
tr 60,11,36864
tr 62,11,37376
ma v,112,a,80,v,208
s_add_u32 s83,s83,s96
tr 64,8,37888
tr 66,8,38400
ma v,120,a,84,v,208
s_add_u32 s84,s84,s96
tr 68,9,37888
tr 70,9,38400
wl 12
s_cmp_ge_u32 s84,s34
s_cselect_b32 s89,s97,0
s_sub_u32 s84,s84,s89
s_add_u32 m0,s84,s35
ma v,128,a,88,v,208
dn 231,36,83,0
tr 72,10,37888
tr 74,10,38400
ma v,136,a,92,v,208
wl 10
tr 76,11,37888
tr 78,11,38400
ma a,96,a,48,v,208
dn 246,36,83,1024
tr 80,8,38912
tr 82,8,39424
ma a,104,a,52,v,208
wl 10
tr 84,9,38912
tr 86,9,39424
ma a,112,a,56,v,208
tr 88,10,38912
tr 90,10,39424
ma a,120,a,60,v,208
wl 10
dn 247,36,83,2048
tr 92,11,38912
tr 94,11,39424
ma a,128,a,64,v,208
s_add_u32 s83,s83,s96
tr 48,0,0
tr 50,0,512
ma a,136,a,68,v,208
s_add_u32 s84,s84,s96
tr 52,1,0
tr 54,1,512
wl 12
s_cmp_ge_u32 s84,s34
s_cselect_b32 s89,s97,0
s_sub_u32 s84,s84,s89
)"
R"(ma a,144,a,72,v,208
tr 56,2,0
tr 58,2,512
ma a,152,a,76,v,208
wl 10
tr 60,3,0
tr 62,3,512
ma a,160,a,80,v,208
tr 64,0,1024
tr 66,0,1536
ma a,168,a,84,v,208
wl 10
tr 68,1,1024
tr 70,1,1536
ma a,176,a,88,v,208
wl 8
tr 72,2,1024
tr 74,2,1536
ma a,184,a,92,v,208
s_waitcnt vmcnt(6)
tr 76,3,1024
tr 78,3,1536
s_barrier
ma v,48,a,48,v,200
s_add_u32 m0,s82,s35
tr 80,0,2048
tr 82,0,2560
ma v,56,a,52,v,200
wl 10
rd v,208,17,14336
tr 84,1,2048
tr 86,1,2560
ma v,64,a,56,v,200
dn 228,28,80,0
tr 88,2,2048
tr 90,2,2560
ma v,72,a,60,v,200
wl 11
tr 92,3,2048
tr 94,3,2560
ma v,80,a,64,v,200
tr 48,0,12288
tr 50,0,12800
ma v,88,a,68,v,200
wl 11
tr 52,1,12288
tr 54,1,12800
ma v,96,a,72,v,200
dn 229,28,80,1024
tr 56,2,12288
tr 58,2,12800
ma v,104,a,76,v,200
wl 10
tr 60,3,12288
tr 62,3,12800
ma v,112,a,80,v,200
tr 64,0,13312
tr 66,0,13824
ma v,120,a,84,v,200
wl 10
tr 68,1,13312
tr 70,1,13824
ma v,128,a,88,v,200
tr 72,2,13312
tr 74,2,13824
ma v,136,a,92,v,200
wl 10
tr 76,3,13312
tr 78,3,13824
ma a,96,a,48,v,200
dn 230,28,80,2048
tr 80,0,14336
tr 82,0,14848
ma a,104,a,52,v,200
s_add_u32 s80,s80,s96
tr 84,1,14336
tr 86,1,14848
wl 12
ma a,112,a,56,v,200
s_add_u32 s82,s82,s96
tr 88,2,14336
tr 90,2,14848
s_cmp_ge_u32 s82,s99
s_cselect_b32 s89,s97,0
s_sub_u32 s82,s82,s89
ma a,120,a,60,v,200
wl 10
tr 92,3,14336
tr 94,3,14848
ma a,128,a,64,v,200
tr 48,0,24576
tr 50,0,25088
ma a,136,a,68,v,200
wl 10
tr 52,1,24576
tr 54,1,25088
ma a,144,a,72,v,200
tr 56,2,24576
tr 58,2,25088
ma a,152,a,76,v,200
wl 10
tr 60,3,24576
tr 62,3,25088
ma a,160,a,80,v,200
tr 64,0,25600
tr 66,0,26112
ma a,168,a,84,v,200
wl 10
tr 68,1,25600
tr 70,1,26112
ma a,176,a,88,v,200
wl 8
tr 72,2,25600
tr 74,2,26112
ma a,184,a,92,v,200
tr 76,3,25600
tr 78,3,26112
ma v,48,a,48,v,208
s_add_u32 m0,s84,s35
tr 80,0,26624
tr 82,0,27136
ma v,56,a,52,v,208
wl 10
dn 231,36,83,0
tr 84,1,26624
tr 86,1,27136
ma v,64,a,56,v,208
tr 88,2,26624
tr 90,2,27136
ma v,72,a,60,v,208
wl 10
tr 92,3,26624
tr 94,3,27136
ma v,80,a,64,v,208
tr 48,0,36864
tr 50,0,37376
ma v,88,a,68,v,208
wl 10
tr 52,1,36864
tr 54,1,37376
ma v,96,a,72,v,208
dn 246,36,83,1024
tr 56,2,36864
tr 58,2,37376
ma v,104,a,76,v,208
wl 10
tr 60,3,36864
tr 62,3,37376
ma v,112,a,80,v,208
tr 64,0,37888
tr 66,0,38400
ma v,120,a,84,v,208
wl 10
tr 68,1,37888
tr 70,1,38400
ma v,128,a,88,v,208
tr 72,2,37888
tr 74,2,38400
ma v,136,a,92,v,208
wl 10
tr 76,3,37888
tr 78,3,38400
ma a,96,a,48,v,208
dn 247,36,83,2048
tr 80,0,38912
tr 82,0,39424
ma a,104,a,52,v,208
s_add_u32 s83,s83,s96
tr 84,1,38912
tr 86,1,39424
wl 12
ma a,112,a,56,v,208
s_add_u32 s84,s84,s96
tr 88,2,38912
tr 90,2,39424
s_cmp_ge_u32 s84,s34
s_cselect_b32 s89,s97,0
s_sub_u32 s84,s84,s89
ma a,120,a,60,v,208
wl 10
tr 92,3,38912
tr 94,3,39424
ma a,128,a,64,v,208
ma a,136,a,68,v,208
wl 8
ma a,144,a,72,v,208
ma a,152,a,76,v,208
wl 4
ma a,160,a,80,v,208
ma a,168,a,84,v,208
wl 0
ma a,176,a,88,v,208
ma a,184,a,92,v,208
s_waitcnt vmcnt(49)
s_barrier
rd a,0,216,0
rd a,4,216,6144
rd a,8,217,0
rd a,12,217,6144
rd a,16,216,128
)"
R"(rd a,20,216,6272
rd a,24,217,128
rd a,28,217,6272
rd a,32,216,256
rd a,36,216,6400
rd a,40,217,256
rd a,44,217,6400
s_nop 7
v_pk_mul_f32 v[48:49],v[48:49],s[58:59] op_sel_hi:[1,0]
v_pk_mul_f32 v[50:51],v[50:51],s[58:59] op_sel_hi:[1,0]
v_pk_mul_f32 v[56:57],v[56:57],s[58:59] op_sel_hi:[1,0]
v_pk_mul_f32 v[58:59],v[58:59],s[58:59] op_sel_hi:[1,0]
cv 152,48,49
cv 153,50,51
cv 154,56,57
cv 155,58,59
v_pk_mul_f32 v[64:65],v[64:65],s[58:59] op_sel_hi:[1,0]
v_pk_mul_f32 v[66:67],v[66:67],s[58:59] op_sel_hi:[1,0]
v_pk_mul_f32 v[72:73],v[72:73],s[58:59] op_sel_hi:[1,0]
v_pk_mul_f32 v[74:75],v[74:75],s[58:59] op_sel_hi:[1,0]
cv 160,64,65
cv 161,66,67
cv 162,72,73
cv 163,74,75
wl 0
s_barrier
s_mov_b32 m0,s82
s_bitcmp1_b32 s69,0
s_cbranch_scc0 LB_win1_%=
s_waitcnt vmcnt(24)
LB_win1_%=:
dn 52,28,80,0
dn 53,28,80,1024
dn 54,28,80,2048
dn 55,28,80,3072
s_add_u32 m0,s82,0x1000
s_bitcmp1_b32 s69,0
s_cbranch_scc0 LB_win2_%=
s_waitcnt vmcnt(24)
LB_win2_%=:
dn 60,28,80,0
dn 61,28,80,1024
dn 62,28,80,2048
dn 63,28,80,3072
s_add_u32 m0,s82,0x2000
s_bitcmp1_b32 s69,0
s_cbranch_scc0 LB_win3_%=
s_waitcnt vmcnt(24)
LB_win3_%=:
dn 68,28,80,0
dn 69,28,80,1024
dn 70,28,80,2048
dn 71,28,80,3072
s_add_u32 s80,s80,s96
s_add_u32 s82,s82,s96
s_cmp_ge_u32 s82,s99
s_cselect_b32 s89,s97,0
s_sub_u32 s82,s82,s89
v_pk_mul_f32 v[80:81],v[80:81],s[58:59] op_sel_hi:[1,0]
v_pk_mul_f32 v[82:83],v[82:83],s[58:59] op_sel_hi:[1,0]
v_pk_mul_f32 v[88:89],v[88:89],s[58:59] op_sel_hi:[1,0]
v_pk_mul_f32 v[90:91],v[90:91],s[58:59] op_sel_hi:[1,0]
cv 168,80,81
cv 169,82,83
cv 170,88,89
cv 171,90,91
v_pk_mul_f32 v[96:97],v[96:97],s[58:59] op_sel_hi:[1,0]
v_pk_mul_f32 v[98:99],v[98:99],s[58:59] op_sel_hi:[1,0]
v_pk_mul_f32 v[104:105],v[104:105],s[58:59] op_sel_hi:[1,0]
v_pk_mul_f32 v[106:107],v[106:107],s[58:59] op_sel_hi:[1,0]
cv 176,96,97
cv 177,98,99
cv 178,104,105
cv 179,106,107
v_pk_mul_f32 v[112:113],v[112:113],s[58:59] op_sel_hi:[1,0]
v_pk_mul_f32 v[114:115],v[114:115],s[58:59] op_sel_hi:[1,0]
v_pk_mul_f32 v[120:121],v[120:121],s[58:59] op_sel_hi:[1,0]
v_pk_mul_f32 v[122:123],v[122:123],s[58:59] op_sel_hi:[1,0]
cv 184,112,113
cv 185,114,115
cv 186,120,121
cv 187,122,123
v_pk_mul_f32 v[128:129],v[128:129],s[58:59] op_sel_hi:[1,0]
v_pk_mul_f32 v[130:131],v[130:131],s[58:59] op_sel_hi:[1,0]
v_pk_mul_f32 v[136:137],v[136:137],s[58:59] op_sel_hi:[1,0]
v_pk_mul_f32 v[138:139],v[138:139],s[58:59] op_sel_hi:[1,0]
cv 192,128,129
cv 193,130,131
cv 194,136,137
cv 195,138,139
v_permlane32_swap_b32 v152,v154
v_permlane32_swap_b32 v153,v155
s_nop 0
v_permlane16_swap_b32 v152,v154
v_permlane16_swap_b32 v153,v155
s_nop 1
mz v,0,a,0,v,152
v_permlane32_swap_b32 v160,v162
v_permlane32_swap_b32 v161,v163
mz v,8,a,4,v,152
v_permlane16_swap_b32 v160,v162
v_permlane16_swap_b32 v161,v163
s_nop 1
ma v,0,a,8,v,160
v_permlane32_swap_b32 v168,v170
v_permlane32_swap_b32 v169,v171
ma v,8,a,12,v,160
v_permlane16_swap_b32 v168,v170
v_permlane16_swap_b32 v169,v171
s_nop 1
ma v,0,a,16,v,168
v_permlane32_swap_b32 v176,v178
v_permlane32_swap_b32 v177,v179
ma v,8,a,20,v,168
v_permlane16_swap_b32 v176,v178
v_permlane16_swap_b32 v177,v179
s_nop 1
ma v,0,a,24,v,176
v_permlane32_swap_b32 v184,v186
v_permlane32_swap_b32 v185,v187
ma v,8,a,28,v,176
v_permlane16_swap_b32 v184,v186
v_permlane16_swap_b32 v185,v187
s_nop 1
ma v,0,a,32,v,184
v_permlane32_swap_b32 v192,v194
v_permlane32_swap_b32 v193,v195
ma v,8,a,36,v,184
v_permlane16_swap_b32 v192,v194
v_permlane16_swap_b32 v193,v195
s_nop 1
ma v,0,a,40,v,192
ma v,8,a,44,v,192
v_readfirstlane_b32 s56,v250
v_readfirstlane_b32 s57,v251
s_add_u32 s56,s56,1
s_addc_u32 s57,s57,0
s_add_u32 s85,s85,s96
s_cmp_ge_u32 s85,s97
s_cselect_b32 s93,s98,s96
s_cselect_b32 s85,0,s85
v_add_u32 v216,s93,v216
v_add_u32 v217,s93,v217
rd a,0,216,0
rd a,4,216,6144
rd a,8,217,0
rd a,12,217,6144
rd a,16,216,128
rd a,20,216,6272
rd a,24,217,128
rd a,28,217,6272
rd a,32,216,256
rd a,36,216,6400
rd a,40,217,256
rd a,44,217,6400
v_max3_f32 v240,v0,v1,v2
v_max3_f32 v240,v240,v3,v8
v_max3_f32 v240,v240,v9,v10
v_max_f32 v240,v240,v11
v_mov_b32 v242,v240
s_nop 1
v_permlane32_swap_b32 v240,v242
v_max_f32 v240,v240,v242
v_mov_b32 v242,v240
s_nop 1
v_permlane16_swap_b32 v240,v242
v_max_f32 v240,v240,v242
v_sub_f32 v32,0,v240
v_sub_f32 v33,0,v240
v_sub_f32 v34,0,v240
v_sub_f32 v35,0,v240
v_sub_f32 v0,v0,v240
v_sub_f32 v1,v1,v240
v_sub_f32 v2,v2,v240
v_sub_f32 v3,v3,v240
v_sub_f32 v8,v8,v240
v_sub_f32 v9,v9,v240
v_sub_f32 v10,v10,v240
v_sub_f32 v11,v11,v240
s_waitcnt vmcnt(21) lgkmcnt(0)
s_barrier
mc v,16,a,0,v,152,32
s_mov_b32 m0,s84
v_exp_f32 v0,v0
s_bitcmp1_b32 s69,0
s_cbranch_scc0 LB_win4_%=
s_waitcnt vmcnt(24)
LB_win4_%=:
v_exp_f32 v1,v1
v_exp_f32 v2,v2
v_exp_f32 v3,v3
mc v,24,a,4,v,152,32
dn 76,36,83,0
v_exp_f32 v8,v8
ma v,16,a,8,v,160
dn 77,36,83,1024
tr 48,218,0
dn 78,36,83,2048
v_exp_f32 v9,v9
ma v,24,a,12,v,160
dn 79,36,83,3072
s_add_u32 m0,s84,0x1000
s_bitcmp1_b32 s69,0
s_cbranch_scc0 LB_win5_%=
s_waitcnt vmcnt(24)
LB_win5_%=:
tr 50,218,6144
dn 84,36,83,0
tr 52,219,0
dn 85,36,83,1024
v_exp_f32 v10,v10
ma v,16,a,16,v,168
dn 86,36,83,2048
tr 54,219,6144
dn 87,36,83,3072
)"
R"(s_add_u32 m0,s84,0x2000
s_bitcmp1_b32 s69,0
s_cbranch_scc0 LB_win6_%=
s_waitcnt vmcnt(24)
LB_win6_%=:
v_exp_f32 v11,v11
cv 40,0,1
ma v,24,a,20,v,168
dn 92,36,83,0
tr 56,220,0
dn 93,36,83,1024
tr 58,220,6144
dn 94,36,83,2048
tr 60,221,0
dn 95,36,83,3072
s_add_u32 s83,s83,s96
s_add_u32 s84,s84,s96
s_cmp_ge_u32 s84,s34
s_cselect_b32 s89,s97,0
ma v,16,a,24,v,176
s_sub_u32 s84,s84,s89
s_mov_b32 m0,s82
s_bitcmp1_b32 s69,0
s_cbranch_scc0 LB_win7_%=
s_waitcnt vmcnt(24)
LB_win7_%=:
tr 62,221,6144
dn 52,28,80,0
ma v,24,a,28,v,176
dn 53,28,80,1024
cv 41,2,3
cv 42,8,9
cv 43,10,11
ma v,16,a,32,v,184
dn 54,28,80,2048
tr 64,218,128
tr 66,218,6272
dn 55,28,80,3072
s_add_u32 m0,s82,0x1000
s_bitcmp1_b32 s69,0
s_cbranch_scc0 LB_win8_%=
s_waitcnt vmcnt(24)
LB_win8_%=:
tr 68,219,128
dn 60,28,80,0
tr 70,219,6272
tr 72,220,128
dn 61,28,80,1024
ma v,24,a,36,v,184
dn 62,28,80,2048
tr 74,220,6272
dn 63,28,80,3072
s_add_u32 m0,s82,0x2000
ma v,16,a,40,v,192
s_bitcmp1_b32 s69,0
s_cbranch_scc0 LB_win9_%=
s_waitcnt vmcnt(24)
LB_win9_%=:
tr 76,221,128
dn 68,28,80,0
tr 78,221,6272
dn 69,28,80,1024
tr 80,218,256
dn 70,28,80,2048
ma v,24,a,44,v,192
dn 71,28,80,3072
s_add_u32 s80,s80,s96
s_add_u32 s82,s82,s96
s_cmp_ge_u32 s82,s99
mz v,144,a,240,v,40
wl 9
s_cselect_b32 s89,s97,0
s_sub_u32 s82,s82,s89
tr 82,218,6400
s_add_u32 s85,s85,s96
mz v,48,a,48,v,40
s_cmp_ge_u32 s85,s97
tr 84,219,256
s_cselect_b32 s93,s98,s96
tr 86,219,6400
s_cselect_b32 s85,0,s85
s_add_u32 s86,s86,s96
mz v,56,a,52,v,40
s_cmp_ge_u32 s86,s97
tr 88,220,256
s_cselect_b32 s94,s98,s96
tr 90,220,6400
s_cselect_b32 s86,0,s86
mz v,64,a,56,v,40
tr 92,221,256
tr 94,221,6400
mz v,72,a,60,v,40
wl 8
v_max3_f32 v240,v16,v17,v18
v_max3_f32 v240,v240,v19,v24
v_max3_f32 v240,v240,v25,v26
v_max_f32 v240,v240,v27
v_cmp_lt_f32 vcc,s87,v240
v_add_u32 v216,s93,v216
v_add_u32 v217,s93,v217
mz v,80,a,64,v,40
rd a,0,216,0
rd a,4,216,6144
rd a,8,217,0
rd a,12,217,6144
rd a,16,216,128
mz v,88,a,68,v,40
rd a,20,216,6272
rd a,24,217,128
rd a,28,217,6272
mz v,96,a,72,v,40
rd a,32,216,256
rd a,36,216,6400
rd a,40,217,256
rd a,44,217,6400
mz v,104,a,76,v,40
wl 12
v_add_u32 v218,s94,v218
mz v,112,a,80,v,40
v_add_u32 v219,s94,v219
v_add_u32 v220,s94,v220
v_add_u32 v221,s94,v221
mz v,120,a,84,v,40
mz v,128,a,88,v,40
mz v,136,a,92,v,40
s_waitcnt vmcnt(36) lgkmcnt(0)
s_cbranch_vccnz LB_a0i0_%=
LB_a0i0_ret_%=:
s_barrier
s_mov_b32 s70,0
LB_a0top_%=:
s_cmp_lg_u32 s70,0
s_cbranch_scc1 LB_de0_%=
v_accvgpr_read_b32 v200,a96
v_accvgpr_read_b32 v201,a97
v_accvgpr_read_b32 v202,a98
v_accvgpr_read_b32 v203,a99
v_accvgpr_read_b32 v204,a104
v_accvgpr_read_b32 v205,a105
v_accvgpr_read_b32 v206,a106
v_accvgpr_read_b32 v207,a107
v_pk_mul_f32 v[200:201],v[200:201],s[58:59] op_sel_hi:[1,0]
v_pk_mul_f32 v[202:203],v[202:203],s[58:59] op_sel_hi:[1,0]
v_pk_mul_f32 v[204:205],v[204:205],s[58:59] op_sel_hi:[1,0]
v_pk_mul_f32 v[206:207],v[206:207],s[58:59] op_sel_hi:[1,0]
cv 40,200,201
cv 41,202,203
cv 42,204,205
cv 43,206,207
s_nop 0
v_permlane32_swap_b32 v40,v42
v_permlane32_swap_b32 v41,v43
s_nop 0
v_permlane16_swap_b32 v40,v42
v_permlane16_swap_b32 v41,v43
v_accvgpr_write_b32 a192,v40
v_accvgpr_write_b32 a193,v41
v_accvgpr_write_b32 a194,v42
v_accvgpr_write_b32 a195,v43
s_branch LB_de6_%=
LB_de0_%=:
s_cmp_lg_u32 s70,1
s_cbranch_scc1 LB_de1_%=
v_accvgpr_read_b32 v200,a112
v_accvgpr_read_b32 v201,a113
v_accvgpr_read_b32 v202,a114
v_accvgpr_read_b32 v203,a115
v_accvgpr_read_b32 v204,a120
v_accvgpr_read_b32 v205,a121
v_accvgpr_read_b32 v206,a122
v_accvgpr_read_b32 v207,a123
v_pk_mul_f32 v[200:201],v[200:201],s[58:59] op_sel_hi:[1,0]
v_pk_mul_f32 v[202:203],v[202:203],s[58:59] op_sel_hi:[1,0]
v_pk_mul_f32 v[204:205],v[204:205],s[58:59] op_sel_hi:[1,0]
v_pk_mul_f32 v[206:207],v[206:207],s[58:59] op_sel_hi:[1,0]
cv 40,200,201
cv 41,202,203
cv 42,204,205
cv 43,206,207
s_nop 0
v_permlane32_swap_b32 v40,v42
v_permlane32_swap_b32 v41,v43
s_nop 0
v_permlane16_swap_b32 v40,v42
v_permlane16_swap_b32 v41,v43
v_accvgpr_write_b32 a200,v40
v_accvgpr_write_b32 a201,v41
v_accvgpr_write_b32 a202,v42
v_accvgpr_write_b32 a203,v43
s_branch LB_de6_%=
LB_de1_%=:
s_cmp_lg_u32 s70,2
s_cbranch_scc1 LB_de2_%=
v_accvgpr_read_b32 v200,a128
v_accvgpr_read_b32 v201,a129
v_accvgpr_read_b32 v202,a130
v_accvgpr_read_b32 v203,a131
v_accvgpr_read_b32 v204,a136
v_accvgpr_read_b32 v205,a137
v_accvgpr_read_b32 v206,a138
v_accvgpr_read_b32 v207,a139
v_pk_mul_f32 v[200:201],v[200:201],s[58:59] op_sel_hi:[1,0]
v_pk_mul_f32 v[202:203],v[202:203],s[58:59] op_sel_hi:[1,0]
v_pk_mul_f32 v[204:205],v[204:205],s[58:59] op_sel_hi:[1,0]
v_pk_mul_f32 v[206:207],v[206:207],s[58:59] op_sel_hi:[1,0]
cv 40,200,201
cv 41,202,203
cv 42,204,205
cv 43,206,207
s_nop 0
v_permlane32_swap_b32 v40,v42
v_permlane32_swap_b32 v41,v43
)"
R"(s_nop 0
v_permlane16_swap_b32 v40,v42
v_permlane16_swap_b32 v41,v43
v_accvgpr_write_b32 a208,v40
v_accvgpr_write_b32 a209,v41
v_accvgpr_write_b32 a210,v42
v_accvgpr_write_b32 a211,v43
s_branch LB_de6_%=
LB_de2_%=:
s_cmp_lg_u32 s70,3
s_cbranch_scc1 LB_de3_%=
v_accvgpr_read_b32 v200,a144
v_accvgpr_read_b32 v201,a145
v_accvgpr_read_b32 v202,a146
v_accvgpr_read_b32 v203,a147
v_accvgpr_read_b32 v204,a152
v_accvgpr_read_b32 v205,a153
v_accvgpr_read_b32 v206,a154
v_accvgpr_read_b32 v207,a155
v_pk_mul_f32 v[200:201],v[200:201],s[58:59] op_sel_hi:[1,0]
v_pk_mul_f32 v[202:203],v[202:203],s[58:59] op_sel_hi:[1,0]
v_pk_mul_f32 v[204:205],v[204:205],s[58:59] op_sel_hi:[1,0]
v_pk_mul_f32 v[206:207],v[206:207],s[58:59] op_sel_hi:[1,0]
cv 40,200,201
cv 41,202,203
cv 42,204,205
cv 43,206,207
s_nop 0
v_permlane32_swap_b32 v40,v42
v_permlane32_swap_b32 v41,v43
s_nop 0
v_permlane16_swap_b32 v40,v42
v_permlane16_swap_b32 v41,v43
v_accvgpr_write_b32 a216,v40
v_accvgpr_write_b32 a217,v41
v_accvgpr_write_b32 a218,v42
v_accvgpr_write_b32 a219,v43
s_branch LB_de6_%=
LB_de3_%=:
s_cmp_lg_u32 s70,4
s_cbranch_scc1 LB_de4_%=
v_accvgpr_read_b32 v200,a160
v_accvgpr_read_b32 v201,a161
v_accvgpr_read_b32 v202,a162
v_accvgpr_read_b32 v203,a163
v_accvgpr_read_b32 v204,a168
v_accvgpr_read_b32 v205,a169
v_accvgpr_read_b32 v206,a170
v_accvgpr_read_b32 v207,a171
v_pk_mul_f32 v[200:201],v[200:201],s[58:59] op_sel_hi:[1,0]
v_pk_mul_f32 v[202:203],v[202:203],s[58:59] op_sel_hi:[1,0]
v_pk_mul_f32 v[204:205],v[204:205],s[58:59] op_sel_hi:[1,0]
v_pk_mul_f32 v[206:207],v[206:207],s[58:59] op_sel_hi:[1,0]
cv 40,200,201
cv 41,202,203
cv 42,204,205
cv 43,206,207
s_nop 0
v_permlane32_swap_b32 v40,v42
v_permlane32_swap_b32 v41,v43
s_nop 0
v_permlane16_swap_b32 v40,v42
v_permlane16_swap_b32 v41,v43
v_accvgpr_write_b32 a224,v40
v_accvgpr_write_b32 a225,v41
v_accvgpr_write_b32 a226,v42
v_accvgpr_write_b32 a227,v43
s_branch LB_de6_%=
LB_de4_%=:
s_cmp_lg_u32 s70,5
s_cbranch_scc1 LB_de5_%=
v_accvgpr_read_b32 v200,a176
v_accvgpr_read_b32 v201,a177
v_accvgpr_read_b32 v202,a178
v_accvgpr_read_b32 v203,a179
v_accvgpr_read_b32 v204,a184
v_accvgpr_read_b32 v205,a185
v_accvgpr_read_b32 v206,a186
v_accvgpr_read_b32 v207,a187
v_pk_mul_f32 v[200:201],v[200:201],s[58:59] op_sel_hi:[1,0]
v_pk_mul_f32 v[202:203],v[202:203],s[58:59] op_sel_hi:[1,0]
v_pk_mul_f32 v[204:205],v[204:205],s[58:59] op_sel_hi:[1,0]
v_pk_mul_f32 v[206:207],v[206:207],s[58:59] op_sel_hi:[1,0]
cv 40,200,201
cv 41,202,203
cv 42,204,205
cv 43,206,207
s_nop 0
v_permlane32_swap_b32 v40,v42
v_permlane32_swap_b32 v41,v43
s_nop 0
v_permlane16_swap_b32 v40,v42
v_permlane16_swap_b32 v41,v43
v_accvgpr_write_b32 a232,v40
v_accvgpr_write_b32 a233,v41
v_accvgpr_write_b32 a234,v42
v_accvgpr_write_b32 a235,v43
s_branch LB_de6_%=
LB_de5_%=:
v_mbcnt_lo_u32_b32 v200,-1,0
v_mbcnt_hi_u32_b32 v200,-1,v200
v_lshrrev_b32 v201,3,v200
v_and_b32 v202,7,v200
v_and_b32 v203,6,v201
v_xor_b32 v203,v203,v202
v_mul_u32_u24 v204,0x500,v201
v_lshl_add_u32 v235,v203,4,v204
v_add_u32 v236,0x2400,v235
v_add_u32 v237,0x4800,v235
v_add_u32 v238,0x6c00,v235
v_mbcnt_lo_u32_b32 v200,-1,0
v_mbcnt_hi_u32_b32 v200,-1,v200
v_lshrrev_b32 v201,4,v200
v_and_b32 v202,15,v200
v_lshrrev_b32 v202,2,v202
v_and_b32 v203,3,v200
v_and_b32 v204,1,v201
v_lshlrev_b32 v205,1,v204
v_lshrrev_b32 v206,1,v202
v_add_u32 v205,v205,v206
v_lshlrev_b32 v204,9,v204
v_lshrrev_b32 v206,1,v201
v_lshl_add_u32 v204,v206,10,v204
v_lshl_add_u32 v204,v202,7,v204
v_lshl_add_u32 v204,v203,3,v204
v_xor_b32 v206,0,v205
v_lshl_add_u32 v222,v206,5,v204
v_xor_b32 v206,1,v205
v_lshl_add_u32 v223,v206,5,v204
v_xor_b32 v206,2,v205
v_lshl_add_u32 v224,v206,5,v204
v_xor_b32 v206,3,v205
v_lshl_add_u32 v225,v206,5,v204
s_mov_b32 s7,0x3c000
s_mov_b32 s8,0x3bf80
s_mov_b32 s9,0x50000
s_mov_b32 s10,0x8000
s_add_u32 s88,s68,1
s_cmp_eq_u32 s88,10
s_cselect_b32 s88,0,s88
s_lshl_b32 s91,s88,7
s_mul_i32 s89,s64,3
s_mul_i32 s73,s89,0x2800
s_add_u32 s74,s73,0x500
s_add_u32 s72,s73,s91
s_add_u32 s72,s72,0
s_lshl_b32 s75,s89,10
s_mul_i32 s89,s64,0x140
s_add_u32 s89,s89,s66
s_lshl_b32 s78,s89,11
s_add_u32 s79,s78,s9
s_lshl_b32 s88,s68,15
s_add_u32 s77,s78,s88
v_mbcnt_lo_u32_b32 v200,-1,0
v_mbcnt_hi_u32_b32 v200,-1,v200
v_lshrrev_b32 v201,4,v200
v_and_b32 v202,15,v200
v_and_b32 v205,1,v202
v_lshlrev_b32 v205,4,v205
v_mov_b32 v204,0x3f80
v_lshlrev_b32 v204,v205,v204
v_lshrrev_b32 v203,3,v202
v_lshlrev_b32 v203,1,v203
v_lshrrev_b32 v205,1,v202
v_and_b32 v205,1,v205
v_add_u32 v203,v203,v205
v_lshrrev_b32 v202,2,v202
v_lshlrev_b32 v202,1,v202
v_add_u32 v205,0,v202
v_and_b32 v205,3,v205
v_cmp_eq_u32 vcc,v205,v201
s_nop 1
v_cndmask_b32 v206,0,v204,vcc
v_cmp_eq_u32 vcc,0,v203
s_nop 1
v_cndmask_b32 v205,0,v206,vcc
v_accvgpr_write_b32 a244,v205
v_cmp_eq_u32 vcc,1,v203
s_nop 1
v_cndmask_b32 v205,0,v206,vcc
v_accvgpr_write_b32 a245,v205
v_cmp_eq_u32 vcc,2,v203
s_nop 1
v_cndmask_b32 v205,0,v206,vcc
v_accvgpr_write_b32 a246,v205
v_cmp_eq_u32 vcc,3,v203
s_nop 1
v_cndmask_b32 v205,0,v206,vcc
v_accvgpr_write_b32 a247,v205
v_add_u32 v205,1,v202
v_and_b32 v205,3,v205
v_cmp_eq_u32 vcc,v205,v201
s_nop 1
v_cndmask_b32 v206,0,v204,vcc
v_cmp_eq_u32 vcc,0,v203
s_nop 1
v_cndmask_b32 v205,0,v206,vcc
v_accvgpr_write_b32 a248,v205
v_cmp_eq_u32 vcc,1,v203
s_nop 1
)"
R"(v_cndmask_b32 v205,0,v206,vcc
v_accvgpr_write_b32 a249,v205
v_cmp_eq_u32 vcc,2,v203
s_nop 1
v_cndmask_b32 v205,0,v206,vcc
v_accvgpr_write_b32 a250,v205
v_cmp_eq_u32 vcc,3,v203
s_nop 1
v_cndmask_b32 v205,0,v206,vcc
v_accvgpr_write_b32 a251,v205
LB_de6_%=:
mc v,0,a,0,v,152,32
s_mov_b32 m0,s84
v_exp_f32 v16,v16
s_bitcmp1_b32 s69,0
s_cbranch_scc0 LB_win10_%=
s_waitcnt vmcnt(24)
LB_win10_%=:
v_exp_f32 v17,v17
v_exp_f32 v18,v18
v_exp_f32 v19,v19
mc v,8,a,4,v,152,32
dn 76,36,83,0
v_exp_f32 v24,v24
ma v,0,a,8,v,160
dn 77,36,83,1024
tr 48,218,0
dn 78,36,83,2048
v_exp_f32 v25,v25
ma v,8,a,12,v,160
dn 79,36,83,3072
s_add_u32 m0,s84,0x1000
s_bitcmp1_b32 s69,0
s_cbranch_scc0 LB_win11_%=
s_waitcnt vmcnt(24)
LB_win11_%=:
tr 50,218,6144
dn 84,36,83,0
tr 52,219,0
dn 85,36,83,1024
v_exp_f32 v26,v26
ma v,0,a,16,v,168
dn 86,36,83,2048
tr 54,219,6144
dn 87,36,83,3072
s_add_u32 m0,s84,0x2000
s_bitcmp1_b32 s69,0
s_cbranch_scc0 LB_win12_%=
s_waitcnt vmcnt(24)
LB_win12_%=:
v_exp_f32 v27,v27
cv 40,16,17
ma v,8,a,20,v,168
dn 92,36,83,0
tr 56,220,0
dn 93,36,83,1024
tr 58,220,6144
dn 94,36,83,2048
tr 60,221,0
dn 95,36,83,3072
s_add_u32 s83,s83,s96
s_add_u32 s84,s84,s96
s_cmp_ge_u32 s84,s34
s_cselect_b32 s89,s97,0
ma v,0,a,24,v,176
s_sub_u32 s84,s84,s89
s_mov_b32 m0,s82
s_bitcmp1_b32 s69,0
s_cbranch_scc0 LB_win13_%=
s_waitcnt vmcnt(24)
LB_win13_%=:
tr 62,221,6144
dn 52,28,80,0
ma v,8,a,28,v,176
dn 53,28,80,1024
cv 41,18,19
cv 42,24,25
cv 43,26,27
ma v,0,a,32,v,184
dn 54,28,80,2048
tr 64,218,128
tr 66,218,6272
dn 55,28,80,3072
s_add_u32 m0,s82,0x1000
s_bitcmp1_b32 s69,0
s_cbranch_scc0 LB_win14_%=
s_waitcnt vmcnt(24)
LB_win14_%=:
tr 68,219,128
dn 60,28,80,0
tr 70,219,6272
tr 72,220,128
dn 61,28,80,1024
ma v,8,a,36,v,184
dn 62,28,80,2048
tr 74,220,6272
dn 63,28,80,3072
s_add_u32 m0,s82,0x2000
ma v,0,a,40,v,192
s_bitcmp1_b32 s69,0
s_cbranch_scc0 LB_win15_%=
s_waitcnt vmcnt(24)
LB_win15_%=:
tr 76,221,128
dn 68,28,80,0
tr 78,221,6272
dn 69,28,80,1024
tr 80,218,256
dn 70,28,80,2048
ma v,8,a,44,v,192
dn 71,28,80,3072
s_add_u32 s80,s80,s96
s_add_u32 s82,s82,s96
s_cmp_ge_u32 s82,s99
ma v,144,a,240,v,40
wl 9
s_cselect_b32 s89,s97,0
s_sub_u32 s82,s82,s89
tr 82,218,6400
s_add_u32 s85,s85,s96
ma v,48,a,48,v,40
s_cmp_ge_u32 s85,s97
tr 84,219,256
s_cselect_b32 s93,s98,s96
tr 86,219,6400
s_cselect_b32 s85,0,s85
s_add_u32 s86,s86,s96
ma v,56,a,52,v,40
s_cmp_ge_u32 s86,s97
tr 88,220,256
s_cselect_b32 s94,s98,s96
tr 90,220,6400
s_cselect_b32 s86,0,s86
ma v,64,a,56,v,40
tr 92,221,256
tr 94,221,6400
ma v,72,a,60,v,40
wl 8
v_max3_f32 v240,v0,v1,v2
v_max3_f32 v240,v240,v3,v8
v_max3_f32 v240,v240,v9,v10
v_max_f32 v240,v240,v11
v_cmp_lt_f32 vcc,s87,v240
v_add_u32 v216,s93,v216
v_add_u32 v217,s93,v217
ma v,80,a,64,v,40
rd a,0,216,0
rd a,4,216,6144
rd a,8,217,0
rd a,12,217,6144
rd a,16,216,128
ma v,88,a,68,v,40
rd a,20,216,6272
rd a,24,217,128
rd a,28,217,6272
ma v,96,a,72,v,40
rd a,32,216,256
rd a,36,216,6400
rd a,40,217,256
rd a,44,217,6400
ma v,104,a,76,v,40
wl 12
v_add_u32 v218,s94,v218
ma v,112,a,80,v,40
v_add_u32 v219,s94,v219
v_add_u32 v220,s94,v220
v_add_u32 v221,s94,v221
ma v,120,a,84,v,40
ma v,128,a,88,v,40
ma v,136,a,92,v,40
s_waitcnt vmcnt(38) lgkmcnt(0)
s_cbranch_vccnz LB_a0i1_%=
LB_a0i1_ret_%=:
s_barrier
mc v,16,a,0,v,152,32
s_mov_b32 m0,s84
v_exp_f32 v0,v0
s_bitcmp1_b32 s69,0
s_cbranch_scc0 LB_win16_%=
s_waitcnt vmcnt(24)
LB_win16_%=:
v_exp_f32 v1,v1
v_exp_f32 v2,v2
v_exp_f32 v3,v3
mc v,24,a,4,v,152,32
dn 76,36,83,0
v_exp_f32 v8,v8
ma v,16,a,8,v,160
dn 77,36,83,1024
tr 48,218,0
dn 78,36,83,2048
v_exp_f32 v9,v9
ma v,24,a,12,v,160
dn 79,36,83,3072
s_add_u32 m0,s84,0x1000
s_bitcmp1_b32 s69,0
s_cbranch_scc0 LB_win17_%=
s_waitcnt vmcnt(24)
LB_win17_%=:
tr 50,218,6144
dn 84,36,83,0
tr 52,219,0
dn 85,36,83,1024
v_exp_f32 v10,v10
ma v,16,a,16,v,168
dn 86,36,83,2048
tr 54,219,6144
dn 87,36,83,3072
s_add_u32 m0,s84,0x2000
s_bitcmp1_b32 s69,0
s_cbranch_scc0 LB_win18_%=
s_waitcnt vmcnt(24)
LB_win18_%=:
v_exp_f32 v11,v11
cv 40,0,1
ma v,24,a,20,v,168
dn 92,36,83,0
tr 56,220,0
dn 93,36,83,1024
tr 58,220,6144
dn 94,36,83,2048
tr 60,221,0
dn 95,36,83,3072
s_add_u32 s83,s83,s96
s_add_u32 s84,s84,s96
s_cmp_ge_u32 s84,s34
s_cselect_b32 s89,s97,0
)"
R"(s_sub_u32 s84,s84,s89
ma v,16,a,24,v,176
s_mov_b32 m0,s82
s_bitcmp1_b32 s69,0
s_cbranch_scc0 LB_win19_%=
s_waitcnt vmcnt(24)
LB_win19_%=:
tr 62,221,6144
dn 52,28,80,0
cv 41,2,3
cv 42,8,9
ma v,24,a,28,v,176
dn 53,28,80,1024
tr 64,218,128
tr 66,218,6272
dn 54,28,80,2048
tr 68,219,128
dn 55,28,80,3072
s_add_u32 m0,s82,0x1000
ma v,16,a,32,v,184
s_bitcmp1_b32 s69,0
s_cbranch_scc0 LB_win20_%=
s_waitcnt vmcnt(24)
LB_win20_%=:
cv 43,10,11
ma v,24,a,36,v,184
dn 60,28,80,0
tr 70,219,6272
dn 61,28,80,1024
tr 72,220,128
dn 62,28,80,2048
tr 74,220,6272
dn 63,28,80,3072
s_add_u32 m0,s82,0x2000
s_bitcmp1_b32 s69,0
s_cbranch_scc0 LB_win21_%=
s_waitcnt vmcnt(24)
LB_win21_%=:
tr 76,221,128
dn 68,28,80,0
ma v,16,a,40,v,192
dn 69,28,80,1024
tr 78,221,6272
dn 70,28,80,2048
tr 80,218,256
dn 71,28,80,3072
s_add_u32 s80,s80,s96
ma v,24,a,44,v,192
s_add_u32 s82,s82,s96
s_cmp_ge_u32 s82,s99
s_cselect_b32 s89,s97,0
ma v,144,a,240,v,40
wl 9
s_sub_u32 s82,s82,s89
s_add_u32 s85,s85,s96
tr 82,218,6400
s_cmp_ge_u32 s85,s97
s_cselect_b32 s93,s98,s96
ma v,48,a,48,v,40
s_cselect_b32 s85,0,s85
tr 84,219,256
s_add_u32 s86,s86,s96
tr 86,219,6400
s_cmp_ge_u32 s86,s97
s_cselect_b32 s94,s98,s96
ma v,56,a,52,v,40
s_cselect_b32 s86,0,s86
tr 88,220,256
s_add_u32 s70,s70,1
tr 90,220,6400
s_cmp_lt_u32 s70,7
ma v,64,a,56,v,40
tr 92,221,256
tr 94,221,6400
ma v,72,a,60,v,40
wl 8
v_max3_f32 v240,v16,v17,v18
v_max3_f32 v240,v240,v19,v24
v_max3_f32 v240,v240,v25,v26
v_max_f32 v240,v240,v27
v_cmp_lt_f32 vcc,s87,v240
v_add_u32 v216,s93,v216
v_add_u32 v217,s93,v217
ma v,80,a,64,v,40
rd a,0,216,0
rd a,4,216,6144
rd a,8,217,0
rd a,12,217,6144
rd a,16,216,128
ma v,88,a,68,v,40
rd a,20,216,6272
rd a,24,217,128
rd a,28,217,6272
ma v,96,a,72,v,40
rd a,32,216,256
rd a,36,216,6400
rd a,40,217,256
rd a,44,217,6400
ma v,104,a,76,v,40
wl 12
v_add_u32 v218,s94,v218
ma v,112,a,80,v,40
v_add_u32 v219,s94,v219
v_add_u32 v220,s94,v220
v_add_u32 v221,s94,v221
ma v,120,a,84,v,40
ma v,128,a,88,v,40
ma v,136,a,92,v,40
s_waitcnt vmcnt(38) lgkmcnt(0)
s_cbranch_vccnz LB_a0i2_%=
LB_a0i2_ret_%=:
s_barrier
s_cbranch_scc1 LB_a0top_%=
tr 48,218,0
tr 50,218,6144
tr 52,219,0
tr 54,219,6144
tr 56,220,0
tr 58,220,6144
tr 60,221,0
tr 62,221,6144
s_mov_b32 m0,s84
s_bitcmp1_b32 s69,0
s_cbranch_scc0 LB_win22_%=
s_waitcnt vmcnt(24)
LB_win22_%=:
dn 76,36,83,0
dn 77,36,83,1024
dn 78,36,83,2048
dn 79,36,83,3072
s_add_u32 m0,s84,0x1000
s_bitcmp1_b32 s69,0
s_cbranch_scc0 LB_win23_%=
s_waitcnt vmcnt(24)
LB_win23_%=:
dn 84,36,83,0
dn 85,36,83,1024
dn 86,36,83,2048
dn 87,36,83,3072
s_add_u32 m0,s84,0x2000
s_bitcmp1_b32 s69,0
s_cbranch_scc0 LB_win24_%=
s_waitcnt vmcnt(24)
LB_win24_%=:
dn 92,36,83,0
dn 93,36,83,1024
dn 94,36,83,2048
dn 95,36,83,3072
s_add_u32 s83,s83,s96
s_add_u32 s84,s84,s96
s_cmp_ge_u32 s84,s34
s_cselect_b32 s89,s97,0
s_sub_u32 s84,s84,s89
s_mov_b32 m0,s82
s_bitcmp1_b32 s69,0
s_cbranch_scc0 LB_win25_%=
s_waitcnt vmcnt(24)
LB_win25_%=:
dn 52,28,80,0
dn 53,28,80,1024
dn 54,28,80,2048
dn 55,28,80,3072
s_add_u32 m0,s82,0x1000
s_bitcmp1_b32 s69,0
s_cbranch_scc0 LB_win26_%=
s_waitcnt vmcnt(24)
LB_win26_%=:
dn 60,28,80,0
dn 61,28,80,1024
dn 62,28,80,2048
dn 63,28,80,3072
s_add_u32 m0,s82,0x2000
s_bitcmp1_b32 s69,0
s_cbranch_scc0 LB_win27_%=
s_waitcnt vmcnt(24)
LB_win27_%=:
dn 68,28,80,0
dn 69,28,80,1024
dn 70,28,80,2048
dn 71,28,80,3072
s_add_u32 s80,s80,s96
s_add_u32 s82,s82,s96
s_cmp_ge_u32 s82,s99
s_cselect_b32 s89,s97,0
s_sub_u32 s82,s82,s89
v_exp_f32 v16,v16
v_exp_f32 v17,v17
v_exp_f32 v18,v18
v_exp_f32 v19,v19
v_exp_f32 v24,v24
v_exp_f32 v25,v25
v_exp_f32 v26,v26
v_exp_f32 v27,v27
cv 40,16,17
cv 41,18,19
cv 42,24,25
cv 43,26,27
tr 64,218,128
tr 66,218,6272
tr 68,219,128
tr 70,219,6272
tr 72,220,128
tr 74,220,6272
tr 76,221,128
tr 78,221,6272
tr 80,218,256
tr 82,218,6400
tr 84,219,256
tr 86,219,6400
tr 88,220,256
tr 90,220,6400
tr 92,221,256
tr 94,221,6400
ma v,144,a,240,v,40
s_add_u32 s86,s86,s96
wl 15
ma v,48,a,48,v,40
s_cmp_ge_u32 s86,s97
ma v,56,a,52,v,40
ma v,64,a,56,v,40
s_cselect_b32 s94,s98,s96
ma v,72,a,60,v,40
s_cselect_b32 s86,0,s86
wl 14
ma v,80,a,64,v,40
wl 12
ma v,88,a,68,v,40
)"
R"(v_add_u32 v218,s94,v218
wl 10
ma v,96,a,72,v,40
wl 8
ma v,104,a,76,v,40
v_add_u32 v219,s94,v219
wl 6
ma v,112,a,80,v,40
v_add_u32 v220,s94,v220
wl 4
ma v,120,a,84,v,40
wl 2
ma v,128,a,88,v,40
v_add_u32 v221,s94,v221
wl 0
ma v,136,a,92,v,40
s_barrier
s_waitcnt vmcnt(48)
s_mov_b32 m0,s84
s_bitcmp1_b32 s69,0
s_cbranch_scc0 LB_win28_%=
s_waitcnt vmcnt(24)
LB_win28_%=:
dn 76,36,83,0
dn 77,36,83,1024
dn 78,36,83,2048
dn 79,36,83,3072
s_add_u32 m0,s84,0x1000
s_bitcmp1_b32 s69,0
s_cbranch_scc0 LB_win29_%=
s_waitcnt vmcnt(24)
LB_win29_%=:
dn 84,36,83,0
dn 85,36,83,1024
dn 86,36,83,2048
dn 87,36,83,3072
s_add_u32 m0,s84,0x2000
s_bitcmp1_b32 s69,0
s_cbranch_scc0 LB_win30_%=
s_waitcnt vmcnt(24)
LB_win30_%=:
dn 92,36,83,0
dn 93,36,83,1024
dn 94,36,83,2048
dn 95,36,83,3072
s_add_u32 s83,s83,s96
s_add_u32 s84,s84,s96
s_cmp_ge_u32 s84,s34
s_cselect_b32 s89,s97,0
s_sub_u32 s84,s84,s89
s_nop 7
v_rcp_f32 v242,v144
s_nop 0
v_pk_mul_f32 v[48:49],v[48:49],v[242:243] op_sel_hi:[1,0]
v_pk_mul_f32 v[50:51],v[50:51],v[242:243] op_sel_hi:[1,0]
v_pk_mul_f32 v[56:57],v[56:57],v[242:243] op_sel_hi:[1,0]
v_pk_mul_f32 v[58:59],v[58:59],v[242:243] op_sel_hi:[1,0]
cv 152,48,49
cv 153,50,51
cv 154,56,57
cv 155,58,59
v_pk_mul_f32 v[64:65],v[64:65],v[242:243] op_sel_hi:[1,0]
v_pk_mul_f32 v[66:67],v[66:67],v[242:243] op_sel_hi:[1,0]
v_pk_mul_f32 v[72:73],v[72:73],v[242:243] op_sel_hi:[1,0]
v_pk_mul_f32 v[74:75],v[74:75],v[242:243] op_sel_hi:[1,0]
cv 160,64,65
cv 161,66,67
cv 162,72,73
cv 163,74,75
v_pk_mul_f32 v[80:81],v[80:81],v[242:243] op_sel_hi:[1,0]
v_pk_mul_f32 v[82:83],v[82:83],v[242:243] op_sel_hi:[1,0]
v_pk_mul_f32 v[88:89],v[88:89],v[242:243] op_sel_hi:[1,0]
v_pk_mul_f32 v[90:91],v[90:91],v[242:243] op_sel_hi:[1,0]
cv 168,80,81
cv 169,82,83
cv 170,88,89
cv 171,90,91
v_pk_mul_f32 v[96:97],v[96:97],v[242:243] op_sel_hi:[1,0]
v_pk_mul_f32 v[98:99],v[98:99],v[242:243] op_sel_hi:[1,0]
v_pk_mul_f32 v[104:105],v[104:105],v[242:243] op_sel_hi:[1,0]
v_pk_mul_f32 v[106:107],v[106:107],v[242:243] op_sel_hi:[1,0]
cv 176,96,97
cv 177,98,99
cv 178,104,105
cv 179,106,107
v_pk_mul_f32 v[112:113],v[112:113],v[242:243] op_sel_hi:[1,0]
v_pk_mul_f32 v[114:115],v[114:115],v[242:243] op_sel_hi:[1,0]
v_pk_mul_f32 v[120:121],v[120:121],v[242:243] op_sel_hi:[1,0]
v_pk_mul_f32 v[122:123],v[122:123],v[242:243] op_sel_hi:[1,0]
cv 184,112,113
cv 185,114,115
cv 186,120,121
cv 187,122,123
v_pk_mul_f32 v[128:129],v[128:129],v[242:243] op_sel_hi:[1,0]
v_pk_mul_f32 v[130:131],v[130:131],v[242:243] op_sel_hi:[1,0]
v_pk_mul_f32 v[136:137],v[136:137],v[242:243] op_sel_hi:[1,0]
v_pk_mul_f32 v[138:139],v[138:139],v[242:243] op_sel_hi:[1,0]
cv 192,128,129
cv 193,130,131
cv 194,136,137
cv 195,138,139
mz v,0,a,0,a,192
s_add_u32 s85,s85,s96
mz v,8,a,4,a,192
s_cmp_ge_u32 s85,s97
ma v,0,a,8,a,200
s_cselect_b32 s93,s98,s96
ma v,8,a,12,a,200
s_cselect_b32 s85,0,s85
ma v,0,a,16,a,208
v_add_u32 v216,s93,v216
ma v,8,a,20,a,208
v_add_u32 v217,s93,v217
ma v,0,a,24,a,216
ma v,8,a,28,a,216
ma v,0,a,32,a,224
ma v,8,a,36,a,224
ma v,0,a,40,a,232
ma v,8,a,44,a,232
rd a,0,216,0
rd a,4,216,6144
rd a,8,217,0
rd a,12,217,6144
rd a,16,216,128
rd a,20,216,6272
rd a,24,217,128
rd a,28,217,6272
rd a,32,216,256
rd a,36,216,6400
rd a,40,217,256
rd a,44,217,6400
v_max3_f32 v240,v0,v1,v2
v_max3_f32 v240,v240,v3,v8
v_max3_f32 v240,v240,v9,v10
v_max_f32 v240,v240,v11
v_mov_b32 v242,v240
s_nop 1
v_permlane32_swap_b32 v240,v242
v_max_f32 v240,v240,v242
v_mov_b32 v242,v240
s_nop 1
v_permlane16_swap_b32 v240,v242
v_max_f32 v240,v240,v242
v_sub_f32 v32,0,v240
v_sub_f32 v33,0,v240
v_sub_f32 v34,0,v240
v_sub_f32 v35,0,v240
v_sub_f32 v0,v0,v240
v_sub_f32 v1,v1,v240
v_sub_f32 v2,v2,v240
v_sub_f32 v3,v3,v240
v_sub_f32 v8,v8,v240
v_sub_f32 v9,v9,v240
v_sub_f32 v10,v10,v240
v_sub_f32 v11,v11,v240
s_waitcnt vmcnt(36) lgkmcnt(0)
s_barrier
mc v,16,a,0,a,192,32
s_mov_b32 m0,s82
v_exp_f32 v0,v0
s_bitcmp1_b32 s69,0
s_cbranch_scc0 LB_win34_%=
s_waitcnt vmcnt(24)
LB_win34_%=:
v_exp_f32 v1,v1
v_exp_f32 v2,v2
v_exp_f32 v3,v3
mc v,24,a,4,a,192,32
dn 52,28,80,0
v_exp_f32 v8,v8
ma v,16,a,8,a,200
dn 53,28,80,1024
tr 48,218,0
dn 54,28,80,2048
v_exp_f32 v9,v9
ma v,24,a,12,a,200
dn 55,28,80,3072
s_add_u32 m0,s82,0x1000
s_bitcmp1_b32 s69,0
s_cbranch_scc0 LB_win35_%=
s_waitcnt vmcnt(24)
LB_win35_%=:
tr 50,218,6144
dn 60,28,80,0
ma v,16,a,16,a,208
dn 61,28,80,1024
tr 52,219,0
v_exp_f32 v10,v10
v_exp_f32 v11,v11
cv 40,0,1
ma v,24,a,20,a,208
dn 62,28,80,2048
tr 54,219,6144
tr 56,220,0
dn 63,28,80,3072
s_add_u32 m0,s82,0x2000
ma v,16,a,24,a,216
s_bitcmp1_b32 s69,0
s_cbranch_scc0 LB_win36_%=
s_waitcnt vmcnt(24)
LB_win36_%=:
tr 58,220,6144
dn 68,28,80,0
ma v,24,a,28,a,216
tr 60,221,0
dn 69,28,80,1024
tr 62,221,6144
cv 41,2,3
cv 42,8,9
cv 43,10,11
ma v,16,a,32,a,224
dn 70,28,80,2048
tr 64,218,128
tr 66,218,6272
dn 71,28,80,3072
s_add_u32 s80,s80,s96
ma v,24,a,36,a,224
s_add_u32 s82,s82,s96
)"
R"(tr 68,219,128
tr 70,219,6272
s_cmp_ge_u32 s82,s99
ma v,16,a,40,a,232
s_cselect_b32 s89,s97,0
tr 72,220,128
tr 74,220,6272
s_sub_u32 s82,s82,s89
ma v,24,a,44,a,232
s_add_u32 s85,s85,s96
tr 76,221,128
tr 78,221,6272
s_cmp_ge_u32 s85,s97
mz v,144,a,240,v,40
wl 8
tr 80,218,256
s_cselect_b32 s93,s98,s96
tr 82,218,6400
s_cselect_b32 s85,0,s85
mz v,48,a,48,v,40
s_add_u32 s86,s86,s96
tr 84,219,256
tr 86,219,6400
s_cmp_ge_u32 s86,s97
mz v,56,a,52,v,40
s_cselect_b32 s94,s98,s96
tr 88,220,256
tr 90,220,6400
s_cselect_b32 s86,0,s86
mz v,64,a,56,v,40
tr 92,221,256
tr 94,221,6400
mz v,72,a,60,v,40
wl 8
v_max3_f32 v240,v16,v17,v18
v_max3_f32 v240,v240,v19,v24
v_max3_f32 v240,v240,v25,v26
v_max_f32 v240,v240,v27
v_cmp_lt_f32 vcc,s87,v240
v_add_u32 v216,s93,v216
v_add_u32 v217,s93,v217
mz v,80,a,64,v,40
rd a,0,216,0
rd a,4,216,6144
rd a,8,217,0
rd a,12,217,6144
rd a,16,216,128
mz v,88,a,68,v,40
rd a,20,216,6272
rd a,24,217,128
rd a,28,217,6272
mz v,96,a,72,v,40
rd a,32,216,256
rd a,36,216,6400
rd a,40,217,256
rd a,44,217,6400
mz v,104,a,76,v,40
wl 12
v_add_u32 v218,s94,v218
mz v,112,a,80,v,40
v_add_u32 v219,s94,v219
v_add_u32 v220,s94,v220
v_add_u32 v221,s94,v221
mz v,120,a,84,v,40
mz v,128,a,88,v,40
mz v,136,a,92,v,40
s_waitcnt vmcnt(38) lgkmcnt(0)
s_cbranch_vccnz LB_a1i0_%=
LB_a1i0_ret_%=:
s_barrier
s_mov_b32 s70,0
LB_a1top_%=:
LB_a1seg0_%=:
mc v,0,a,0,a,192,32
s_mov_b32 m0,s84
v_exp_f32 v16,v16
s_bitcmp1_b32 s69,0
s_cbranch_scc0 LB_win37_%=
s_waitcnt vmcnt(24)
LB_win37_%=:
v_exp_f32 v17,v17
v_exp_f32 v18,v18
v_exp_f32 v19,v19
mc v,8,a,4,a,192,32
dn 76,36,83,0
v_exp_f32 v24,v24
ma v,0,a,8,a,200
dn 77,36,83,1024
tr 48,218,0
dn 78,36,83,2048
v_exp_f32 v25,v25
ma v,8,a,12,a,200
dn 79,36,83,3072
s_add_u32 m0,s84,0x1000
s_bitcmp1_b32 s69,0
s_cbranch_scc0 LB_win38_%=
s_waitcnt vmcnt(24)
LB_win38_%=:
tr 50,218,6144
dn 84,36,83,0
tr 52,219,0
dn 85,36,83,1024
v_exp_f32 v26,v26
ma v,0,a,16,a,208
dn 86,36,83,2048
tr 54,219,6144
dn 87,36,83,3072
s_add_u32 m0,s84,0x2000
s_bitcmp1_b32 s69,0
s_cbranch_scc0 LB_win39_%=
s_waitcnt vmcnt(24)
LB_win39_%=:
v_exp_f32 v27,v27
cv 40,16,17
ma v,8,a,20,a,208
dn 92,36,83,0
tr 56,220,0
dn 93,36,83,1024
tr 58,220,6144
dn 94,36,83,2048
tr 60,221,0
dn 95,36,83,3072
s_add_u32 s83,s83,s96
s_add_u32 s84,s84,s96
s_cmp_ge_u32 s84,s34
s_cselect_b32 s89,s97,0
ma v,0,a,24,a,216
s_sub_u32 s84,s84,s89
s_mov_b32 m0,s82
s_bitcmp1_b32 s69,0
s_cbranch_scc0 LB_win40_%=
s_waitcnt vmcnt(24)
LB_win40_%=:
tr 62,221,6144
dn 52,28,80,0
ma v,8,a,28,a,216
dn 53,28,80,1024
cv 41,18,19
cv 42,24,25
cv 43,26,27
ma v,0,a,32,a,224
dn 54,28,80,2048
tr 64,218,128
tr 66,218,6272
dn 55,28,80,3072
s_add_u32 m0,s82,0x1000
s_bitcmp1_b32 s69,0
s_cbranch_scc0 LB_win41_%=
s_waitcnt vmcnt(24)
LB_win41_%=:
tr 68,219,128
dn 60,28,80,0
tr 70,219,6272
tr 72,220,128
dn 61,28,80,1024
ma v,8,a,36,a,224
dn 62,28,80,2048
tr 74,220,6272
dn 63,28,80,3072
s_add_u32 m0,s82,0x2000
ma v,0,a,40,a,232
s_bitcmp1_b32 s69,0
s_cbranch_scc0 LB_win42_%=
s_waitcnt vmcnt(24)
LB_win42_%=:
tr 76,221,128
dn 68,28,80,0
tr 78,221,6272
dn 69,28,80,1024
tr 80,218,256
dn 70,28,80,2048
ma v,8,a,44,a,232
dn 71,28,80,3072
s_add_u32 s80,s80,s96
s_add_u32 s82,s82,s96
s_cmp_ge_u32 s82,s99
ma v,144,a,240,v,40
wl 9
s_cselect_b32 s89,s97,0
s_sub_u32 s82,s82,s89
tr 82,218,6400
s_add_u32 s85,s85,s96
ma v,48,a,48,v,40
s_cmp_ge_u32 s85,s97
tr 84,219,256
s_cselect_b32 s93,s98,s96
tr 86,219,6400
s_cselect_b32 s85,0,s85
s_add_u32 s86,s86,s96
ma v,56,a,52,v,40
s_cmp_ge_u32 s86,s97
tr 88,220,256
s_cselect_b32 s94,s98,s96
tr 90,220,6400
s_cselect_b32 s86,0,s86
ma v,64,a,56,v,40
tr 92,221,256
tr 94,221,6400
ma v,72,a,60,v,40
wl 8
v_max3_f32 v240,v0,v1,v2
v_max3_f32 v240,v240,v3,v8
v_max3_f32 v240,v240,v9,v10
v_max_f32 v240,v240,v11
v_cmp_lt_f32 vcc,s87,v240
v_add_u32 v216,s93,v216
v_add_u32 v217,s93,v217
ma v,80,a,64,v,40
rd a,0,216,0
rd a,4,216,6144
rd a,8,217,0
rd a,12,217,6144
rd a,16,216,128
ma v,88,a,68,v,40
rd a,20,216,6272
rd a,24,217,128
rd a,28,217,6272
ma v,96,a,72,v,40
)"
R"(rd a,32,216,256
rd a,36,216,6400
rd a,40,217,256
rd a,44,217,6400
ma v,104,a,76,v,40
wl 12
v_add_u32 v218,s94,v218
ma v,112,a,80,v,40
v_add_u32 v219,s94,v219
v_add_u32 v220,s94,v220
v_add_u32 v221,s94,v221
ma v,120,a,84,v,40
ma v,128,a,88,v,40
ma v,136,a,92,v,40
s_waitcnt vmcnt(38) lgkmcnt(0)
s_cbranch_vccnz LB_a1s0i1_%=
LB_a1s0i1_ret_%=:
s_barrier
mc v,16,a,0,a,192,32
s_mov_b32 m0,s84
v_exp_f32 v0,v0
s_bitcmp1_b32 s69,0
s_cbranch_scc0 LB_win43_%=
s_waitcnt vmcnt(24)
LB_win43_%=:
v_exp_f32 v1,v1
v_exp_f32 v2,v2
v_exp_f32 v3,v3
mc v,24,a,4,a,192,32
dn 76,36,83,0
v_exp_f32 v8,v8
ma v,16,a,8,a,200
dn 77,36,83,1024
tr 48,218,0
dn 78,36,83,2048
v_exp_f32 v9,v9
ma v,24,a,12,a,200
dn 79,36,83,3072
s_add_u32 m0,s84,0x1000
s_bitcmp1_b32 s69,0
s_cbranch_scc0 LB_win44_%=
s_waitcnt vmcnt(24)
LB_win44_%=:
tr 50,218,6144
dn 84,36,83,0
tr 52,219,0
dn 85,36,83,1024
v_exp_f32 v10,v10
ma v,16,a,16,a,208
dn 86,36,83,2048
tr 54,219,6144
dn 87,36,83,3072
s_add_u32 m0,s84,0x2000
s_bitcmp1_b32 s69,0
s_cbranch_scc0 LB_win45_%=
s_waitcnt vmcnt(24)
LB_win45_%=:
v_exp_f32 v11,v11
cv 40,0,1
ma v,24,a,20,a,208
dn 92,36,83,0
tr 56,220,0
dn 93,36,83,1024
tr 58,220,6144
dn 94,36,83,2048
tr 60,221,0
dn 95,36,83,3072
s_add_u32 s83,s83,s96
s_add_u32 s84,s84,s96
s_cmp_ge_u32 s84,s34
s_cselect_b32 s89,s97,0
s_sub_u32 s84,s84,s89
ma v,16,a,24,a,216
s_mov_b32 m0,s82
s_bitcmp1_b32 s69,0
s_cbranch_scc0 LB_win46_%=
s_waitcnt vmcnt(24)
LB_win46_%=:
tr 62,221,6144
dn 52,28,80,0
cv 41,2,3
cv 42,8,9
ma v,24,a,28,a,216
dn 53,28,80,1024
tr 64,218,128
tr 66,218,6272
dn 54,28,80,2048
tr 68,219,128
dn 55,28,80,3072
s_add_u32 m0,s82,0x1000
ma v,16,a,32,a,224
s_bitcmp1_b32 s69,0
s_cbranch_scc0 LB_win47_%=
s_waitcnt vmcnt(24)
LB_win47_%=:
cv 43,10,11
ma v,24,a,36,a,224
dn 60,28,80,0
tr 70,219,6272
dn 61,28,80,1024
tr 72,220,128
dn 62,28,80,2048
tr 74,220,6272
dn 63,28,80,3072
s_add_u32 m0,s82,0x2000
s_bitcmp1_b32 s69,0
s_cbranch_scc0 LB_win48_%=
s_waitcnt vmcnt(24)
LB_win48_%=:
tr 76,221,128
dn 68,28,80,0
ma v,16,a,40,a,232
dn 69,28,80,1024
tr 78,221,6272
dn 70,28,80,2048
tr 80,218,256
dn 71,28,80,3072
s_add_u32 s80,s80,s96
ma v,24,a,44,a,232
s_add_u32 s82,s82,s96
s_cmp_ge_u32 s82,s99
s_cselect_b32 s89,s97,0
ma v,144,a,240,v,40
wl 9
s_sub_u32 s82,s82,s89
s_add_u32 s85,s85,s96
tr 82,218,6400
s_cmp_ge_u32 s85,s97
s_cselect_b32 s93,s98,s96
ma v,48,a,48,v,40
s_cselect_b32 s85,0,s85
tr 84,219,256
s_add_u32 s86,s86,s96
tr 86,219,6400
s_cmp_ge_u32 s86,s97
s_cselect_b32 s94,s98,s96
ma v,56,a,52,v,40
s_cselect_b32 s86,0,s86
tr 88,220,256
s_add_u32 s70,s70,1
tr 90,220,6400
s_cmp_lt_u32 s70,3
ma v,64,a,56,v,40
tr 92,221,256
tr 94,221,6400
ma v,72,a,60,v,40
wl 8
v_max3_f32 v240,v16,v17,v18
v_max3_f32 v240,v240,v19,v24
v_max3_f32 v240,v240,v25,v26
v_max_f32 v240,v240,v27
v_cmp_lt_f32 vcc,s87,v240
v_add_u32 v216,s93,v216
v_add_u32 v217,s93,v217
ma v,80,a,64,v,40
rd a,0,216,0
rd a,4,216,6144
rd a,8,217,0
rd a,12,217,6144
rd a,16,216,128
ma v,88,a,68,v,40
rd a,20,216,6272
rd a,24,217,128
rd a,28,217,6272
ma v,96,a,72,v,40
rd a,32,216,256
rd a,36,216,6400
rd a,40,217,256
rd a,44,217,6400
ma v,104,a,76,v,40
wl 12
v_add_u32 v218,s94,v218
ma v,112,a,80,v,40
v_add_u32 v219,s94,v219
v_add_u32 v220,s94,v220
v_add_u32 v221,s94,v221
ma v,120,a,84,v,40
ma v,128,a,88,v,40
ma v,136,a,92,v,40
s_waitcnt vmcnt(38) lgkmcnt(0)
s_cbranch_vccnz LB_a1s0i2_%=
LB_a1s0i2_ret_%=:
s_barrier
s_cbranch_scc1 LB_a1seg0_%=
mc v,0,a,0,a,192,32
s_add_u32 s85,s85,s96
v_exp_f32 v16,v16
v_exp_f32 v17,v17
mc v,8,a,4,a,192,32
s_cmp_ge_u32 s85,s97
v_exp_f32 v18,v18
ma v,0,a,8,a,200
s_cselect_b32 s93,s98,s96
v_exp_f32 v19,v19
ma v,8,a,12,a,200
s_cselect_b32 s85,0,s85
v_exp_f32 v24,v24
ma v,0,a,16,a,208
tr 48,218,0
v_exp_f32 v25,v25
v_exp_f32 v26,v26
v_exp_f32 v27,v27
cv 40,16,17
ma v,8,a,20,a,208
tr 50,218,6144
tr 52,219,0
tr 54,219,6144
ma v,0,a,24,a,216
s_add_u32 s86,s86,s96
tr 56,220,0
tr 58,220,6144
ma v,8,a,28,a,216
s_cmp_ge_u32 s86,s97
tr 60,221,0
tr 62,221,6144
cv 41,18,19
ma v,0,a,32,a,224
tr 64,218,128
)"
R"(tr 66,218,6272
cv 42,24,25
ma v,8,a,36,a,224
s_cselect_b32 s94,s98,s96
tr 68,219,128
cv 43,26,27
ma v,0,a,40,a,232
tr 70,219,6272
tr 72,220,128
tr 74,220,6272
ma v,8,a,44,a,232
s_cselect_b32 s86,0,s86
tr 76,221,128
tr 78,221,6272
ma v,144,a,240,v,40
wl 8
tr 80,218,256
tr 82,218,6400
ma v,48,a,48,v,40
tr 84,219,256
tr 86,219,6400
ma v,56,a,52,v,40
tr 88,220,256
tr 90,220,6400
ma v,64,a,56,v,40
tr 92,221,256
tr 94,221,6400
ma v,72,a,60,v,40
wl 8
v_max3_f32 v240,v0,v1,v2
v_max3_f32 v240,v240,v3,v8
v_max3_f32 v240,v240,v9,v10
v_max_f32 v240,v240,v11
v_cmp_lt_f32 vcc,s87,v240
v_add_u32 v216,s93,v216
v_add_u32 v217,s93,v217
ma v,80,a,64,v,40
rd a,0,216,0
rd a,4,216,6144
rd a,8,217,0
rd a,12,217,6144
rd a,16,216,128
ma v,88,a,68,v,40
rd a,20,216,6272
rd a,24,217,128
rd a,28,217,6272
ma v,96,a,72,v,40
rd a,32,216,256
rd a,36,216,6400
rd a,40,217,256
rd a,44,217,6400
ma v,104,a,76,v,40
wl 12
v_add_u32 v218,s94,v218
ma v,112,a,80,v,40
v_add_u32 v219,s94,v219
v_add_u32 v220,s94,v220
v_add_u32 v221,s94,v221
ma v,120,a,84,v,40
ma v,128,a,88,v,40
ma v,136,a,92,v,40
s_waitcnt vmcnt(24) lgkmcnt(0)
s_cbranch_vccnz LB_a1k0i1_%=
LB_a1k0i1_ret_%=:
s_barrier
mc v,16,a,0,a,192,32
s_mov_b32 m0,s84
v_exp_f32 v0,v0
s_bitcmp1_b32 s69,0
s_cbranch_scc0 LB_win55_%=
s_waitcnt vmcnt(24)
LB_win55_%=:
v_exp_f32 v1,v1
v_exp_f32 v2,v2
v_exp_f32 v3,v3
mc v,24,a,4,a,192,32
dn 76,36,83,0
v_exp_f32 v8,v8
ma v,16,a,8,a,200
dn 77,36,83,1024
tr 48,218,0
dn 78,36,83,2048
v_exp_f32 v9,v9
ma v,24,a,12,a,200
dn 79,36,83,3072
s_add_u32 m0,s84,0x1000
s_bitcmp1_b32 s69,0
s_cbranch_scc0 LB_win56_%=
s_waitcnt vmcnt(24)
LB_win56_%=:
tr 50,218,6144
dn 84,36,83,0
tr 52,219,0
dn 85,36,83,1024
v_exp_f32 v10,v10
ma v,16,a,16,a,208
dn 86,36,83,2048
tr 54,219,6144
dn 87,36,83,3072
s_add_u32 m0,s84,0x2000
s_bitcmp1_b32 s69,0
s_cbranch_scc0 LB_win57_%=
s_waitcnt vmcnt(24)
LB_win57_%=:
v_exp_f32 v11,v11
cv 40,0,1
ma v,24,a,20,a,208
dn 92,36,83,0
tr 56,220,0
dn 93,36,83,1024
tr 58,220,6144
dn 94,36,83,2048
tr 60,221,0
dn 95,36,83,3072
s_add_u32 s83,s83,s96
s_add_u32 s84,s84,s96
s_cmp_ge_u32 s84,s34
s_cselect_b32 s89,s97,0
s_sub_u32 s84,s84,s89
ma v,16,a,24,a,216
s_mov_b32 m0,s82
s_bitcmp1_b32 s69,0
s_cbranch_scc0 LB_win58_%=
s_waitcnt vmcnt(24)
LB_win58_%=:
tr 62,221,6144
dn 52,28,80,0
cv 41,2,3
cv 42,8,9
ma v,24,a,28,a,216
dn 53,28,80,1024
tr 64,218,128
tr 66,218,6272
dn 54,28,80,2048
tr 68,219,128
dn 55,28,80,3072
s_add_u32 m0,s82,0x1000
ma v,16,a,32,a,224
s_bitcmp1_b32 s69,0
s_cbranch_scc0 LB_win59_%=
s_waitcnt vmcnt(24)
LB_win59_%=:
cv 43,10,11
ma v,24,a,36,a,224
dn 60,28,80,0
tr 70,219,6272
dn 61,28,80,1024
tr 72,220,128
dn 62,28,80,2048
tr 74,220,6272
dn 63,28,80,3072
s_add_u32 m0,s82,0x2000
s_bitcmp1_b32 s69,0
s_cbranch_scc0 LB_win60_%=
s_waitcnt vmcnt(24)
LB_win60_%=:
tr 76,221,128
dn 68,28,80,0
ma v,16,a,40,a,232
dn 69,28,80,1024
tr 78,221,6272
dn 70,28,80,2048
tr 80,218,256
dn 71,28,80,3072
s_add_u32 s80,s80,s96
ma v,24,a,44,a,232
s_add_u32 s82,s82,s96
s_cmp_ge_u32 s82,s99
s_cselect_b32 s89,s97,0
ma v,144,a,240,v,40
wl 9
s_sub_u32 s82,s82,s89
s_add_u32 s85,s85,s96
tr 82,218,6400
s_cmp_ge_u32 s85,s97
s_cselect_b32 s93,s98,s96
ma v,48,a,48,v,40
s_cselect_b32 s85,0,s85
tr 84,219,256
s_add_u32 s86,s86,s96
tr 86,219,6400
s_cmp_ge_u32 s86,s97
s_cselect_b32 s94,s98,s96
ma v,56,a,52,v,40
s_cselect_b32 s86,0,s86
tr 88,220,256
s_add_u32 s70,s70,1
tr 90,220,6400
s_cmp_lt_u32 s70,0
ma v,64,a,56,v,40
tr 92,221,256
tr 94,221,6400
ma v,72,a,60,v,40
wl 8
v_max3_f32 v240,v16,v17,v18
v_max3_f32 v240,v240,v19,v24
v_max3_f32 v240,v240,v25,v26
v_max_f32 v240,v240,v27
v_cmp_lt_f32 vcc,s87,v240
v_add_u32 v216,s93,v216
v_add_u32 v217,s93,v217
ma v,80,a,64,v,40
rd a,0,216,0
rd a,4,216,6144
rd a,8,217,0
rd a,12,217,6144
rd a,16,216,128
ma v,88,a,68,v,40
rd a,20,216,6272
rd a,24,217,128
rd a,28,217,6272
ma v,96,a,72,v,40
rd a,32,216,256
rd a,36,216,6400
rd a,40,217,256
rd a,44,217,6400
ma v,104,a,76,v,40
wl 12
)"
R"(v_add_u32 v218,s94,v218
ma v,112,a,80,v,40
v_add_u32 v219,s94,v219
v_add_u32 v220,s94,v220
v_add_u32 v221,s94,v221
ma v,120,a,84,v,40
ma v,128,a,88,v,40
ma v,136,a,92,v,40
s_waitcnt vmcnt(24) lgkmcnt(0)
s_cbranch_vccnz LB_a1k0i2_%=
LB_a1k0i2_ret_%=:
s_barrier
LB_a1seg1_%=:
mc v,0,a,0,a,192,32
s_mov_b32 m0,s84
v_exp_f32 v16,v16
s_bitcmp1_b32 s69,0
s_cbranch_scc0 LB_win61_%=
s_waitcnt vmcnt(24)
LB_win61_%=:
v_exp_f32 v17,v17
v_exp_f32 v18,v18
v_exp_f32 v19,v19
mc v,8,a,4,a,192,32
dn 76,36,83,0
v_exp_f32 v24,v24
ma v,0,a,8,a,200
dn 77,36,83,1024
tr 48,218,0
dn 78,36,83,2048
v_exp_f32 v25,v25
ma v,8,a,12,a,200
dn 79,36,83,3072
s_add_u32 m0,s84,0x1000
s_bitcmp1_b32 s69,0
s_cbranch_scc0 LB_win62_%=
s_waitcnt vmcnt(24)
LB_win62_%=:
tr 50,218,6144
dn 84,36,83,0
tr 52,219,0
dn 85,36,83,1024
v_exp_f32 v26,v26
ma v,0,a,16,a,208
dn 86,36,83,2048
tr 54,219,6144
dn 87,36,83,3072
s_add_u32 m0,s84,0x2000
s_bitcmp1_b32 s69,0
s_cbranch_scc0 LB_win63_%=
s_waitcnt vmcnt(24)
LB_win63_%=:
v_exp_f32 v27,v27
cv 40,16,17
ma v,8,a,20,a,208
dn 92,36,83,0
tr 56,220,0
dn 93,36,83,1024
tr 58,220,6144
dn 94,36,83,2048
tr 60,221,0
dn 95,36,83,3072
s_add_u32 s83,s83,s96
s_add_u32 s84,s84,s96
s_cmp_ge_u32 s84,s34
s_cselect_b32 s89,s97,0
ma v,0,a,24,a,216
s_sub_u32 s84,s84,s89
s_mov_b32 m0,s82
s_bitcmp1_b32 s69,0
s_cbranch_scc0 LB_win64_%=
s_waitcnt vmcnt(24)
LB_win64_%=:
tr 62,221,6144
dn 52,28,80,0
ma v,8,a,28,a,216
dn 53,28,80,1024
cv 41,18,19
cv 42,24,25
cv 43,26,27
ma v,0,a,32,a,224
dn 54,28,80,2048
tr 64,218,128
tr 66,218,6272
dn 55,28,80,3072
s_add_u32 m0,s82,0x1000
s_bitcmp1_b32 s69,0
s_cbranch_scc0 LB_win65_%=
s_waitcnt vmcnt(24)
LB_win65_%=:
tr 68,219,128
dn 60,28,80,0
tr 70,219,6272
tr 72,220,128
dn 61,28,80,1024
ma v,8,a,36,a,224
dn 62,28,80,2048
tr 74,220,6272
dn 63,28,80,3072
s_add_u32 m0,s82,0x2000
ma v,0,a,40,a,232
s_bitcmp1_b32 s69,0
s_cbranch_scc0 LB_win66_%=
s_waitcnt vmcnt(24)
LB_win66_%=:
tr 76,221,128
dn 68,28,80,0
tr 78,221,6272
dn 69,28,80,1024
tr 80,218,256
dn 70,28,80,2048
ma v,8,a,44,a,232
dn 71,28,80,3072
s_add_u32 s80,s80,s96
s_add_u32 s82,s82,s96
s_cmp_ge_u32 s82,s99
ma v,144,a,240,v,40
wl 9
s_cselect_b32 s89,s97,0
s_sub_u32 s82,s82,s89
tr 82,218,6400
s_add_u32 s85,s85,s96
ma v,48,a,48,v,40
s_cmp_ge_u32 s85,s97
tr 84,219,256
s_cselect_b32 s93,s98,s96
tr 86,219,6400
s_cselect_b32 s85,0,s85
s_add_u32 s86,s86,s96
ma v,56,a,52,v,40
s_cmp_ge_u32 s86,s97
tr 88,220,256
s_cselect_b32 s94,s98,s96
tr 90,220,6400
s_cselect_b32 s86,0,s86
ma v,64,a,56,v,40
tr 92,221,256
tr 94,221,6400
ma v,72,a,60,v,40
wl 8
v_max3_f32 v240,v0,v1,v2
v_max3_f32 v240,v240,v3,v8
v_max3_f32 v240,v240,v9,v10
v_max_f32 v240,v240,v11
v_cmp_lt_f32 vcc,s87,v240
v_add_u32 v216,s93,v216
v_add_u32 v217,s93,v217
ma v,80,a,64,v,40
rd a,0,216,0
rd a,4,216,6144
rd a,8,217,0
rd a,12,217,6144
rd a,16,216,128
ma v,88,a,68,v,40
rd a,20,216,6272
rd a,24,217,128
rd a,28,217,6272
ma v,96,a,72,v,40
rd a,32,216,256
rd a,36,216,6400
rd a,40,217,256
rd a,44,217,6400
ma v,104,a,76,v,40
wl 12
v_add_u32 v218,s94,v218
ma v,112,a,80,v,40
v_add_u32 v219,s94,v219
v_add_u32 v220,s94,v220
v_add_u32 v221,s94,v221
ma v,120,a,84,v,40
ma v,128,a,88,v,40
ma v,136,a,92,v,40
s_waitcnt vmcnt(24) lgkmcnt(0)
s_cbranch_vccnz LB_a1s1i1_%=
LB_a1s1i1_ret_%=:
s_barrier
mc v,16,a,0,a,192,32
s_mov_b32 m0,s84
v_exp_f32 v0,v0
s_bitcmp1_b32 s69,0
s_cbranch_scc0 LB_win67_%=
s_waitcnt vmcnt(24)
LB_win67_%=:
v_exp_f32 v1,v1
v_exp_f32 v2,v2
v_exp_f32 v3,v3
mc v,24,a,4,a,192,32
dn 76,36,83,0
v_exp_f32 v8,v8
ma v,16,a,8,a,200
dn 77,36,83,1024
tr 48,218,0
dn 78,36,83,2048
v_exp_f32 v9,v9
ma v,24,a,12,a,200
dn 79,36,83,3072
s_add_u32 m0,s84,0x1000
s_bitcmp1_b32 s69,0
s_cbranch_scc0 LB_win68_%=
s_waitcnt vmcnt(24)
LB_win68_%=:
tr 50,218,6144
dn 84,36,83,0
tr 52,219,0
dn 85,36,83,1024
v_exp_f32 v10,v10
ma v,16,a,16,a,208
dn 86,36,83,2048
tr 54,219,6144
dn 87,36,83,3072
s_add_u32 m0,s84,0x2000
s_bitcmp1_b32 s69,0
s_cbranch_scc0 LB_win69_%=
s_waitcnt vmcnt(24)
LB_win69_%=:
v_exp_f32 v11,v11
cv 40,0,1
ma v,24,a,20,a,208
dn 92,36,83,0
tr 56,220,0
dn 93,36,83,1024
tr 58,220,6144
dn 94,36,83,2048
tr 60,221,0
dn 95,36,83,3072
s_add_u32 s83,s83,s96
s_add_u32 s84,s84,s96
)"
R"(s_cmp_ge_u32 s84,s34
s_cselect_b32 s89,s97,0
s_sub_u32 s84,s84,s89
ma v,16,a,24,a,216
s_mov_b32 m0,s82
s_bitcmp1_b32 s69,0
s_cbranch_scc0 LB_win70_%=
s_waitcnt vmcnt(24)
LB_win70_%=:
tr 62,221,6144
dn 52,28,80,0
cv 41,2,3
cv 42,8,9
ma v,24,a,28,a,216
dn 53,28,80,1024
tr 64,218,128
tr 66,218,6272
dn 54,28,80,2048
tr 68,219,128
dn 55,28,80,3072
s_add_u32 m0,s82,0x1000
ma v,16,a,32,a,224
s_bitcmp1_b32 s69,0
s_cbranch_scc0 LB_win71_%=
s_waitcnt vmcnt(24)
LB_win71_%=:
cv 43,10,11
ma v,24,a,36,a,224
dn 60,28,80,0
tr 70,219,6272
dn 61,28,80,1024
tr 72,220,128
dn 62,28,80,2048
tr 74,220,6272
dn 63,28,80,3072
s_add_u32 m0,s82,0x2000
s_bitcmp1_b32 s69,0
s_cbranch_scc0 LB_win72_%=
s_waitcnt vmcnt(24)
LB_win72_%=:
tr 76,221,128
dn 68,28,80,0
ma v,16,a,40,a,232
dn 69,28,80,1024
tr 78,221,6272
dn 70,28,80,2048
tr 80,218,256
dn 71,28,80,3072
s_add_u32 s80,s80,s96
ma v,24,a,44,a,232
s_add_u32 s82,s82,s96
s_cmp_ge_u32 s82,s99
s_cselect_b32 s89,s97,0
ma v,144,a,240,v,40
wl 9
s_sub_u32 s82,s82,s89
s_add_u32 s85,s85,s96
tr 82,218,6400
s_cmp_ge_u32 s85,s97
s_cselect_b32 s93,s98,s96
ma v,48,a,48,v,40
s_cselect_b32 s85,0,s85
tr 84,219,256
s_add_u32 s86,s86,s96
tr 86,219,6400
s_cmp_ge_u32 s86,s97
s_cselect_b32 s94,s98,s96
ma v,56,a,52,v,40
s_cselect_b32 s86,0,s86
tr 88,220,256
s_add_u32 s70,s70,1
tr 90,220,6400
s_cmp_lt_u32 s70,6
ma v,64,a,56,v,40
tr 92,221,256
tr 94,221,6400
ma v,72,a,60,v,40
wl 8
v_max3_f32 v240,v16,v17,v18
v_max3_f32 v240,v240,v19,v24
v_max3_f32 v240,v240,v25,v26
v_max_f32 v240,v240,v27
v_cmp_lt_f32 vcc,s87,v240
v_add_u32 v216,s93,v216
v_add_u32 v217,s93,v217
ma v,80,a,64,v,40
rd a,0,216,0
rd a,4,216,6144
rd a,8,217,0
rd a,12,217,6144
rd a,16,216,128
ma v,88,a,68,v,40
rd a,20,216,6272
rd a,24,217,128
rd a,28,217,6272
ma v,96,a,72,v,40
rd a,32,216,256
rd a,36,216,6400
rd a,40,217,256
rd a,44,217,6400
ma v,104,a,76,v,40
wl 12
v_add_u32 v218,s94,v218
ma v,112,a,80,v,40
v_add_u32 v219,s94,v219
v_add_u32 v220,s94,v220
v_add_u32 v221,s94,v221
ma v,120,a,84,v,40
ma v,128,a,88,v,40
ma v,136,a,92,v,40
s_waitcnt vmcnt(24) lgkmcnt(0)
s_cbranch_vccnz LB_a1s1i2_%=
LB_a1s1i2_ret_%=:
s_barrier
s_cbranch_scc1 LB_a1seg1_%=
mc v,0,a,0,a,192,32
s_add_u32 s85,s85,s96
v_exp_f32 v16,v16
v_exp_f32 v17,v17
mc v,8,a,4,a,192,32
s_cmp_ge_u32 s85,s97
v_exp_f32 v18,v18
ma v,0,a,8,a,200
s_cselect_b32 s93,s98,s96
v_exp_f32 v19,v19
ma v,8,a,12,a,200
s_cselect_b32 s85,0,s85
v_exp_f32 v24,v24
ma v,0,a,16,a,208
tr 48,218,0
v_exp_f32 v25,v25
v_exp_f32 v26,v26
v_exp_f32 v27,v27
cv 40,16,17
ma v,8,a,20,a,208
tr 50,218,6144
tr 52,219,0
tr 54,219,6144
ma v,0,a,24,a,216
s_add_u32 s86,s86,s96
tr 56,220,0
tr 58,220,6144
ma v,8,a,28,a,216
s_cmp_ge_u32 s86,s97
tr 60,221,0
tr 62,221,6144
cv 41,18,19
ma v,0,a,32,a,224
tr 64,218,128
tr 66,218,6272
cv 42,24,25
ma v,8,a,36,a,224
s_cselect_b32 s94,s98,s96
tr 68,219,128
cv 43,26,27
ma v,0,a,40,a,232
tr 70,219,6272
tr 72,220,128
tr 74,220,6272
ma v,8,a,44,a,232
s_cselect_b32 s86,0,s86
tr 76,221,128
tr 78,221,6272
ma v,144,a,240,v,40
wl 8
tr 80,218,256
tr 82,218,6400
ma v,48,a,48,v,40
tr 84,219,256
tr 86,219,6400
ma v,56,a,52,v,40
tr 88,220,256
tr 90,220,6400
ma v,64,a,56,v,40
tr 92,221,256
tr 94,221,6400
ma v,72,a,60,v,40
wl 8
v_max3_f32 v240,v0,v1,v2
v_max3_f32 v240,v240,v3,v8
v_max3_f32 v240,v240,v9,v10
v_max_f32 v240,v240,v11
v_cmp_lt_f32 vcc,s87,v240
v_add_u32 v216,s93,v216
v_add_u32 v217,s93,v217
ma v,80,a,64,v,40
rd a,0,216,0
rd a,4,216,6144
rd a,8,217,0
rd a,12,217,6144
rd a,16,216,128
ma v,88,a,68,v,40
rd a,20,216,6272
rd a,24,217,128
rd a,28,217,6272
ma v,96,a,72,v,40
rd a,32,216,256
rd a,36,216,6400
rd a,40,217,256
rd a,44,217,6400
ma v,104,a,76,v,40
wl 12
v_add_u32 v218,s94,v218
ma v,112,a,80,v,40
v_add_u32 v219,s94,v219
v_add_u32 v220,s94,v220
v_add_u32 v221,s94,v221
ma v,120,a,84,v,40
ma v,128,a,88,v,40
)"
R"(ma v,136,a,92,v,40
s_waitcnt vmcnt(0) lgkmcnt(0)
s_cbranch_vccnz LB_a1k1i1_%=
LB_a1k1i1_ret_%=:
s_barrier
mc v,16,a,0,a,192,32
s_mov_b32 m0,s84
v_exp_f32 v0,v0
s_bitcmp1_b32 s69,0
s_cbranch_scc0 LB_win79_%=
s_waitcnt vmcnt(24)
LB_win79_%=:
v_exp_f32 v1,v1
v_exp_f32 v2,v2
v_exp_f32 v3,v3
mc v,24,a,4,a,192,32
dn 76,36,83,0
v_exp_f32 v8,v8
ma v,16,a,8,a,200
dn 77,36,83,1024
tr 48,218,0
dn 78,36,83,2048
v_exp_f32 v9,v9
ma v,24,a,12,a,200
dn 79,36,83,3072
s_add_u32 m0,s84,0x1000
s_bitcmp1_b32 s69,0
s_cbranch_scc0 LB_win80_%=
s_waitcnt vmcnt(24)
LB_win80_%=:
tr 50,218,6144
dn 84,36,83,0
tr 52,219,0
dn 85,36,83,1024
v_exp_f32 v10,v10
ma v,16,a,16,a,208
dn 86,36,83,2048
tr 54,219,6144
dn 87,36,83,3072
s_add_u32 m0,s84,0x2000
s_bitcmp1_b32 s69,0
s_cbranch_scc0 LB_win81_%=
s_waitcnt vmcnt(24)
LB_win81_%=:
v_exp_f32 v11,v11
cv 40,0,1
ma v,24,a,20,a,208
dn 92,36,83,0
tr 56,220,0
dn 93,36,83,1024
tr 58,220,6144
dn 94,36,83,2048
tr 60,221,0
dn 95,36,83,3072
s_add_u32 s83,s83,s96
s_add_u32 s84,s84,s96
s_cmp_ge_u32 s84,s34
s_cselect_b32 s89,s97,0
s_sub_u32 s84,s84,s89
ma v,16,a,24,a,216
s_mov_b32 m0,s82
s_bitcmp1_b32 s69,0
s_cbranch_scc0 LB_win82_%=
s_waitcnt vmcnt(24)
LB_win82_%=:
tr 62,221,6144
dn 52,28,80,0
cv 41,2,3
cv 42,8,9
ma v,24,a,28,a,216
dn 53,28,80,1024
tr 64,218,128
tr 66,218,6272
dn 54,28,80,2048
tr 68,219,128
dn 55,28,80,3072
s_add_u32 m0,s82,0x1000
ma v,16,a,32,a,224
s_bitcmp1_b32 s69,0
s_cbranch_scc0 LB_win83_%=
s_waitcnt vmcnt(24)
LB_win83_%=:
cv 43,10,11
ma v,24,a,36,a,224
dn 60,28,80,0
tr 70,219,6272
dn 61,28,80,1024
tr 72,220,128
dn 62,28,80,2048
tr 74,220,6272
dn 63,28,80,3072
s_add_u32 m0,s82,0x2000
s_bitcmp1_b32 s69,0
s_cbranch_scc0 LB_win84_%=
s_waitcnt vmcnt(24)
LB_win84_%=:
tr 76,221,128
dn 68,28,80,0
ma v,16,a,40,a,232
dn 69,28,80,1024
tr 78,221,6272
dn 70,28,80,2048
tr 80,218,256
dn 71,28,80,3072
s_add_u32 s80,s80,s96
ma v,24,a,44,a,232
s_add_u32 s82,s82,s96
s_cmp_ge_u32 s82,s99
s_cselect_b32 s89,s97,0
ma v,144,a,240,v,40
wl 9
s_sub_u32 s82,s82,s89
s_add_u32 s85,s85,s96
tr 82,218,6400
s_cmp_ge_u32 s85,s97
s_cselect_b32 s93,s98,s96
ma v,48,a,48,v,40
s_cselect_b32 s85,0,s85
tr 84,219,256
s_add_u32 s86,s86,s96
tr 86,219,6400
s_cmp_ge_u32 s86,s97
s_cselect_b32 s94,s98,s96
ma v,56,a,52,v,40
s_cselect_b32 s86,0,s86
tr 88,220,256
s_add_u32 s70,s70,1
tr 90,220,6400
s_cmp_lt_u32 s70,0
ma v,64,a,56,v,40
tr 92,221,256
tr 94,221,6400
ma v,72,a,60,v,40
wl 8
v_max3_f32 v240,v16,v17,v18
v_max3_f32 v240,v240,v19,v24
v_max3_f32 v240,v240,v25,v26
v_max_f32 v240,v240,v27
v_cmp_lt_f32 vcc,s87,v240
v_add_u32 v216,s93,v216
v_add_u32 v217,s93,v217
ma v,80,a,64,v,40
rd a,0,216,0
rd a,4,216,6144
rd a,8,217,0
rd a,12,217,6144
rd a,16,216,128
ma v,88,a,68,v,40
rd a,20,216,6272
rd a,24,217,128
rd a,28,217,6272
ma v,96,a,72,v,40
rd a,32,216,256
rd a,36,216,6400
rd a,40,217,256
rd a,44,217,6400
ma v,104,a,76,v,40
wl 12
v_add_u32 v218,s94,v218
ma v,112,a,80,v,40
v_add_u32 v219,s94,v219
v_add_u32 v220,s94,v220
v_add_u32 v221,s94,v221
ma v,120,a,84,v,40
ma v,128,a,88,v,40
ma v,136,a,92,v,40
s_waitcnt vmcnt(0) lgkmcnt(0)
s_cbranch_vccnz LB_a1k1i2_%=
LB_a1k1i2_ret_%=:
s_barrier
s_waitcnt vmcnt(0)
s_barrier
tr 48,218,0
tr 50,218,6144
tr 52,219,0
tr 54,219,6144
tr 56,220,0
tr 58,220,6144
tr 60,221,0
tr 62,221,6144
tr 64,218,128
tr 66,218,6272
tr 68,219,128
tr 70,219,6272
v_exp_f32 v16,v16
v_exp_f32 v17,v17
v_exp_f32 v18,v18
v_exp_f32 v19,v19
v_exp_f32 v24,v24
v_exp_f32 v25,v25
v_exp_f32 v26,v26
v_exp_f32 v27,v27
cv 40,16,17
cv 41,18,19
cv 42,24,25
cv 43,26,27
tr 72,220,128
tr 74,220,6272
tr 76,221,128
tr 78,221,6272
tr 80,218,256
tr 82,218,6400
tr 84,219,256
tr 86,219,6400
tr 88,220,256
tr 90,220,6400
tr 92,221,256
tr 94,221,6400
ma v,144,a,240,v,40
wl 15
ma v,48,a,48,v,40
ma v,56,a,52,v,40
ma v,64,a,56,v,40
ma v,72,a,60,v,40
wl 14
ma v,80,a,64,v,40
wl 12
ma v,88,a,68,v,40
)"
R"(wl 10
ma v,96,a,72,v,40
wl 8
ma v,104,a,76,v,40
wl 6
ma v,112,a,80,v,40
wl 4
ma v,120,a,84,v,40
wl 2
ma v,128,a,88,v,40
wl 0
ma v,136,a,92,v,40
s_mov_b32 s42,0x78000
s_add_u32 m0,s75,0x18000
s_nop 0
dl 235,40,72,0
dl 236,40,72,1024
dl 237,40,72,2048
dl 238,40,72,3072
s_add_u32 s90,s72,0xa000
s_add_u32 m0,s75,0x19000
s_nop 0
dl 235,40,90,0
dl 236,40,90,1024
dl 237,40,90,2048
dl 238,40,90,3072
s_add_u32 s90,s72,0x14000
s_add_u32 m0,s75,0x1a000
s_nop 0
dl 235,40,90,0
dl 236,40,90,1024
dl 237,40,90,2048
dl 238,40,90,3072
s_add_u32 s90,s72,0x1e000
s_add_u32 m0,s75,0x1b000
s_nop 0
dl 235,40,90,0
dl 236,40,90,1024
dl 237,40,90,2048
s_add_u32 s72,s72,s7
s_add_u32 m0,s75,0x1e000
s_nop 0
dl 235,40,72,0
dl 236,40,72,1024
dl 237,40,72,2048
dl 238,40,72,3072
s_add_u32 s90,s72,0xa000
s_add_u32 m0,s75,0x1f000
s_nop 0
dl 235,40,90,0
dl 236,40,90,1024
dl 237,40,90,2048
dl 238,40,90,3072
s_add_u32 s90,s72,0x14000
s_add_u32 m0,s75,0x20000
s_nop 0
dl 235,40,90,0
dl 236,40,90,1024
dl 237,40,90,2048
dl 238,40,90,3072
s_add_u32 s90,s72,0x1e000
s_add_u32 m0,s75,0x21000
s_nop 0
dl 235,40,90,0
dl 236,40,90,1024
dl 237,40,90,2048
s_sub_u32 s72,s72,s8
s_cmp_ge_u32 s72,s74
s_cselect_b32 s72,s73,s72
tr 48,222,0
tr 50,222,2048
tr 52,223,0
tr 54,223,2048
tr 56,224,0
tr 58,224,2048
tr 60,225,0
tr 62,225,2048
tr 64,222,4096
tr 66,222,6144
tr 68,223,4096
tr 70,223,6144
tr 72,224,4096
tr 74,224,6144
tr 76,225,4096
tr 78,225,6144
s_barrier
wl 12
mz v,0,a,48,v,152
s_add_u32 m0,s75,0xc000
tr 80,222,8192
tr 82,222,10240
mz v,8,a,52,v,152
wl 10
dl 235,40,72,0
tr 84,223,8192
tr 86,223,10240
mz v,16,a,56,v,152
dl 236,40,72,1024
tr 88,224,8192
tr 90,224,10240
mz v,24,a,60,v,152
wl 10
v_rcp_f32 v242,v144
tr 92,225,8192
tr 94,225,10240
dl 237,40,72,2048
dl 238,40,72,3072
ma v,0,a,64,v,160
s_add_u32 s90,s72,0xa000
tr 48,222,12288
tr 50,222,14336
v_mul_f32 v48,v48,v242
s_add_u32 m0,s75,0xd000
ma v,8,a,68,v,160
wl 10
dl 235,40,90,0
tr 52,223,12288
tr 54,223,14336
v_mul_f32 v49,v49,v242
v_mul_f32 v50,v50,v242
dl 236,40,90,1024
ma v,16,a,72,v,160
dl 237,40,90,2048
tr 56,224,12288
tr 58,224,14336
v_mul_f32 v51,v51,v242
cv 48,48,49
dl 238,40,90,3072
ma v,24,a,76,v,160
s_add_u32 s90,s72,0x14000
tr 60,225,12288
tr 62,225,14336
wl 12
cv 49,50,51
v_mul_f32 v56,v56,v242
s_add_u32 m0,s75,0xe000
s_nop 0
dl 235,40,90,0
ma v,0,a,80,v,168
dl 236,40,90,1024
tr 64,222,16384
tr 66,222,18432
v_mul_f32 v57,v57,v242
v_mul_f32 v58,v58,v242
dl 237,40,90,2048
ma v,8,a,84,v,168
wl 10
dl 238,40,90,3072
tr 68,223,16384
tr 70,223,18432
v_mul_f32 v59,v59,v242
cv 50,56,57
s_add_u32 s90,s72,0x1e000
ma v,16,a,88,v,168
s_add_u32 m0,s75,0xf000
tr 72,224,16384
tr 74,224,18432
cv 51,58,59
v_mul_f32 v64,v64,v242
dl 235,40,90,0
ma v,24,a,92,v,168
wl 10
dl 236,40,90,1024
tr 76,225,16384
tr 78,225,18432
v_mul_f32 v65,v65,v242
v_mul_f32 v66,v66,v242
dl 237,40,90,2048
ma v,0,a,48,v,176
s_add_u32 s72,s72,s7
tr 80,222,20480
tr 82,222,22528
v_mul_f32 v67,v67,v242
cv 64,64,65
s_add_u32 m0,s75,0x12000
s_nop 0
dl 235,40,72,0
ma v,8,a,52,v,176
wl 10
dl 236,40,72,1024
tr 84,223,20480
tr 86,223,22528
cv 65,66,67
v_mul_f32 v72,v72,v242
dl 237,40,72,2048
ma v,16,a,56,v,176
dl 238,40,72,3072
tr 88,224,20480
tr 90,224,22528
v_mul_f32 v73,v73,v242
v_mul_f32 v74,v74,v242
s_add_u32 s90,s72,0xa000
ma v,24,a,60,v,176
s_add_u32 m0,s75,0x13000
tr 92,225,20480
tr 94,225,22528
wl 12
v_mul_f32 v75,v75,v242
cv 66,72,73
dl 235,40,90,0
)"
R"(ma v,0,a,64,v,184
dl 236,40,90,1024
tr 48,222,24576
tr 50,222,26624
cv 67,74,75
v_mul_f32 v80,v80,v242
dl 237,40,90,2048
ma v,8,a,68,v,184
wl 10
dl 238,40,90,3072
tr 52,223,24576
tr 54,223,26624
v_mul_f32 v81,v81,v242
v_mul_f32 v82,v82,v242
s_add_u32 s90,s72,0x14000
ma v,16,a,72,v,184
s_add_u32 m0,s75,0x14000
tr 56,224,24576
tr 58,224,26624
v_mul_f32 v83,v83,v242
cv 80,80,81
dl 235,40,90,0
dl 236,40,90,1024
ma v,24,a,76,v,184
wl 10
dl 237,40,90,2048
tr 60,225,24576
tr 62,225,26624
cv 81,82,83
v_mul_f32 v88,v88,v242
dl 238,40,90,3072
ma v,0,a,80,v,192
s_add_u32 s90,s72,0x1e000
tr 64,222,28672
tr 66,222,30720
v_mul_f32 v89,v89,v242
v_mul_f32 v90,v90,v242
s_add_u32 m0,s75,0x15000
ma v,8,a,84,v,192
wl 10
dl 235,40,90,0
tr 68,223,28672
tr 70,223,30720
v_mul_f32 v91,v91,v242
cv 82,88,89
dl 236,40,90,1024
ma v,16,a,88,v,192
wl 8
dl 237,40,90,2048
tr 72,224,28672
tr 74,224,30720
cv 83,90,91
s_sub_u32 s72,s72,s8
ma v,24,a,92,v,192
s_waitcnt vmcnt(36)
tr 76,225,28672
tr 78,225,30720
s_cmp_ge_u32 s72,s74
s_cselect_b32 s72,s73,s72
s_barrier
ma v,0,a,48,v,48
s_mov_b32 m0,s75
tr 80,222,32768
tr 82,222,34816
v_mul_f32 v96,v96,v242
ma v,8,a,52,v,48
wl 10
dl 235,40,72,0
tr 84,223,32768
tr 86,223,34816
v_mul_f32 v97,v97,v242
ma v,16,a,56,v,48
dl 236,40,72,1024
tr 88,224,32768
tr 90,224,34816
v_mul_f32 v98,v98,v242
v_mul_f32 v99,v99,v242
ma v,24,a,60,v,48
wl 10
dl 237,40,72,2048
tr 92,225,32768
tr 94,225,34816
cv 96,96,97
ma v,0,a,64,v,64
dl 238,40,72,3072
tr 48,222,36864
tr 50,222,38912
cv 97,98,99
v_mul_f32 v104,v104,v242
ma v,8,a,68,v,64
s_add_u32 s90,s72,0xa000
tr 52,223,36864
tr 54,223,38912
wl 12
v_mul_f32 v105,v105,v242
v_mul_f32 v106,v106,v242
ma v,16,a,72,v,64
s_add_u32 m0,s75,0x1000
tr 56,224,36864
tr 58,224,38912
v_mul_f32 v107,v107,v242
ma v,24,a,76,v,64
wl 10
dl 235,40,90,0
tr 60,225,36864
tr 62,225,38912
cv 98,104,105
cv 99,106,107
ma v,0,a,80,v,80
dl 236,40,90,1024
tr 64,222,40960
tr 66,222,43008
v_mul_f32 v112,v112,v242
ma v,8,a,84,v,80
wl 10
dl 237,40,90,2048
tr 68,223,40960
tr 70,223,43008
v_mul_f32 v113,v113,v242
v_mul_f32 v114,v114,v242
ma v,16,a,88,v,80
dl 238,40,90,3072
tr 72,224,40960
tr 74,224,43008
v_mul_f32 v115,v115,v242
cv 112,112,113
ma v,24,a,92,v,80
s_add_u32 s90,s72,0x14000
tr 76,225,40960
tr 78,225,43008
wl 12
cv 113,114,115
ma v,0,a,48,v,96
s_add_u32 m0,s75,0x2000
tr 80,222,45056
tr 82,222,47104
v_mul_f32 v120,v120,v242
v_mul_f32 v121,v121,v242
ma v,8,a,52,v,96
wl 10
dl 235,40,90,0
tr 84,223,45056
tr 86,223,47104
v_mul_f32 v122,v122,v242
v_mul_f32 v123,v123,v242
cv 114,120,121
cv 115,122,123
ma v,16,a,56,v,96
dl 236,40,90,1024
tr 88,224,45056
tr 90,224,47104
ma v,24,a,60,v,96
wl 10
dl 237,40,90,2048
tr 92,225,45056
tr 94,225,47104
v_mul_f32 v128,v128,v242
ma v,0,a,64,v,112
dl 238,40,90,3072
v_add_u32 v222,0x18000,v222
v_add_u32 v223,0x18000,v223
v_add_u32 v224,0x18000,v224
v_add_u32 v225,0x18000,v225
tr 48,222,0
tr 50,222,2048
v_mul_f32 v129,v129,v242
ma v,8,a,68,v,112
s_add_u32 s90,s72,0x1e000
tr 52,223,0
tr 54,223,2048
wl 12
v_mul_f32 v130,v130,v242
v_mul_f32 v131,v131,v242
cv 128,128,129
cv 129,130,131
v_mul_f32 v136,v136,v242
v_mul_f32 v137,v137,v242
v_mul_f32 v138,v138,v242
v_mul_f32 v139,v139,v242
cv 130,136,137
cv 131,138,139
ma v,16,a,72,v,112
s_add_u32 m0,s75,0x3000
tr 56,224,0
tr 58,224,2048
ma v,24,a,76,v,112
wl 10
dl 235,40,90,0
tr 60,225,0
tr 62,225,2048
ma v,0,a,80,v,128
dl 236,40,90,1024
tr 64,222,4096
tr 66,222,6144
ma v,8,a,84,v,128
wl 10
dl 237,40,90,2048
tr 68,223,4096
tr 70,223,6144
ma v,16,a,88,v,128
)"
R"(s_add_u32 s72,s72,s7
tr 72,224,4096
tr 74,224,6144
wl 10
ma v,24,a,92,v,128
s_waitcnt vmcnt(36)
tr 76,225,4096
tr 78,225,6144
s_barrier
mz v,56,a,48,v,152
s_lshl_b32 s88,s66,7
tr 80,222,8192
tr 82,222,10240
mz v,72,a,52,v,152
wl 10
tr 84,223,8192
tr 86,223,10240
mz v,88,a,56,v,152
tr 88,224,8192
tr 90,224,10240
v_mov_b32 v32,s88
mz v,104,a,60,v,152
wl 10
tr 92,225,8192
tr 94,225,10240
v_mov_b32 v36,s56
v_mov_b32 v37,s57
ma v,56,a,64,v,160
tr 48,222,12288
tr 50,222,14336
cv 200,0,1
v_lshrrev_b32 v246,4,v239
v_and_b32 v33,15,v246
ma v,72,a,68,v,160
wl 10
tr 52,223,12288
tr 54,223,14336
cv 201,2,3
v_mul_u32_u24 v34,0x500,v33
v_lshlrev_b32 v33,7,v33
ma v,88,a,72,v,160
tr 56,224,12288
tr 58,224,14336
cv 202,8,9
v_lshrrev_b32 v246,4,v246
v_lshl_add_u32 v34,v246,4,v34
ma v,104,a,76,v,160
s_add_u32 m0,s75,0x6000
tr 60,225,12288
tr 62,225,14336
wl 12
cv 203,10,11
ma v,56,a,80,v,168
dl 235,40,72,0
tr 64,222,16384
tr 66,222,18432
cv 204,16,17
dl 236,40,72,1024
ma v,72,a,84,v,168
wl 10
dl 237,40,72,2048
tr 68,223,16384
tr 70,223,18432
cv 205,18,19
dl 238,40,72,3072
ma v,88,a,88,v,168
s_add_u32 s90,s72,0xa000
tr 72,224,16384
tr 74,224,18432
cv 206,24,25
s_add_u32 m0,s75,0x7000
ma v,104,a,92,v,168
wl 10
dl 235,40,90,0
tr 76,225,16384
tr 78,225,18432
cv 207,26,27
dl 236,40,90,1024
ma v,56,a,48,v,176
dl 237,40,90,2048
tr 80,222,20480
tr 82,222,22528
dl 238,40,90,3072
ma v,72,a,52,v,176
s_add_u32 s90,s72,0x14000
tr 84,223,20480
tr 86,223,22528
wl 12
ma v,88,a,56,v,176
s_add_u32 m0,s75,0x8000
tr 88,224,20480
tr 90,224,22528
dl 235,40,90,0
ma v,104,a,60,v,176
wl 10
dl 236,40,90,1024
tr 92,225,20480
tr 94,225,22528
dl 237,40,90,2048
ma v,56,a,64,v,184
dl 238,40,90,3072
tr 48,222,24576
tr 50,222,26624
s_add_u32 s90,s72,0x1e000
ma v,72,a,68,v,184
s_add_u32 m0,s75,0x9000
tr 52,223,24576
tr 54,223,26624
wl 12
dl 235,40,90,0
ma v,88,a,72,v,184
dl 236,40,90,1024
tr 56,224,24576
tr 58,224,26624
ma v,104,a,76,v,184
wl 10
dl 237,40,90,2048
tr 60,225,24576
tr 62,225,26624
s_sub_u32 s72,s72,s8
ma v,56,a,80,v,192
s_cmp_ge_u32 s72,s74
tr 64,222,28672
tr 66,222,30720
s_cselect_b32 s72,s73,s72
ma v,72,a,84,v,192
wl 10
buffer_store_dwordx4 v[200:203],v239,s[44:47],s77 offen sc1
tr 68,223,28672
tr 70,223,30720
buffer_store_dwordx4 v[204:207],v239,s[44:47],s77 offen offset:1024 sc1
ma v,88,a,88,v,192
s_add_u32 s77,s77,s10
tr 72,224,28672
tr 74,224,30720
wl 10
s_cmp_ge_u32 s77,s79
ma v,104,a,92,v,192
s_waitcnt vmcnt(36)
tr 76,225,28672
tr 78,225,30720
s_cselect_b32 s77,s78,s77
s_barrier
ma v,56,a,48,v,48
s_add_u32 s88,s64,1
tr 80,222,32768
tr 82,222,34816
ma v,72,a,52,v,48
s_and_b32 s88,s88,3
tr 84,223,32768
tr 86,223,34816
wl 12
ma v,88,a,56,v,48
s_lshl_b32 s88,s88,4
tr 88,224,32768
tr 90,224,34816
ma v,104,a,60,v,48
s_add_u32 s88,s88,s66
tr 92,225,32768
tr 94,225,34816
wl 12
s_lshr_b32 s89,s88,3
ma v,56,a,64,v,64
s_and_b32 s91,s88,1
tr 48,222,36864
tr 50,222,38912
s_lshr_b32 s88,s88,1
ma v,72,a,68,v,64
s_and_b32 s88,s88,3
tr 52,223,36864
tr 54,223,38912
wl 12
s_add_u32 s88,s88,s68
ma v,88,a,72,v,64
s_add_u32 s88,s88,0
tr 56,224,36864
tr 58,224,38912
s_cmp_ge_u32 s88,10
ma v,104,a,76,v,64
s_cselect_b32 s80,10,0
tr 60,225,36864
tr 62,225,38912
wl 12
s_sub_u32 s88,s88,s80
ma v,56,a,80,v,80
s_mul_i32 s80,s89,10
tr 64,222,40960
tr 66,222,43008
s_add_u32 s80,s80,s88
ma v,72,a,84,v,80
s_lshl_b32 s80,s80,5
tr 68,223,40960
tr 70,223,43008
wl 12
s_add_u32 s80,s80,s91
ma v,88,a,88,v,80
s_lshl_b32 s80,s80,10
tr 72,224,40960
tr 74,224,43008
s_mul_i32 s83,s89,0x5000
)"
R"(ma v,104,a,92,v,80
s_lshl_b32 s88,s88,1
tr 76,225,40960
tr 78,225,43008
wl 12
s_add_u32 s88,s88,s91
ma v,56,a,48,v,96
s_lshl_b32 s88,s88,6
tr 80,222,45056
tr 82,222,47104
s_add_u32 s83,s83,s88
s_add_u32 m0,s75,0x18000
ma v,72,a,52,v,96
wl 10
dl 235,40,72,0
tr 84,223,45056
tr 86,223,47104
dl 236,40,72,1024
ma v,88,a,56,v,96
dl 237,40,72,2048
tr 88,224,45056
tr 90,224,47104
dl 238,40,72,3072
ma v,104,a,60,v,96
s_add_u32 s90,s72,0xa000
tr 92,225,45056
tr 94,225,47104
wl 12
s_add_u32 m0,s75,0x19000
ma v,56,a,64,v,112
dl 235,40,90,0
v_add_u32 v222,0xffff4000,v222
v_add_u32 v223,0xffff4000,v223
v_add_u32 v224,0xffff4000,v224
v_add_u32 v225,0xffff4000,v225
tr 48,222,0
tr 50,222,2048
dl 236,40,90,1024
ma v,72,a,68,v,112
wl 10
dl 237,40,90,2048
tr 52,223,0
tr 54,223,2048
dl 238,40,90,3072
ma v,88,a,72,v,112
s_add_u32 s90,s72,0x14000
tr 56,224,0
tr 58,224,2048
s_add_u32 m0,s75,0x1a000
ma v,104,a,76,v,112
wl 10
dl 235,40,90,0
tr 60,225,0
tr 62,225,2048
dl 236,40,90,1024
ma v,56,a,80,v,128
dl 237,40,90,2048
tr 64,222,4096
tr 66,222,6144
dl 238,40,90,3072
ma v,72,a,84,v,128
s_add_u32 s90,s72,0x1e000
tr 68,223,4096
tr 70,223,6144
wl 12
s_add_u32 m0,s75,0x1b000
ma v,88,a,88,v,128
wl 8
dl 235,40,90,0
tr 72,224,4096
tr 74,224,6144
dl 236,40,90,1024
ma v,104,a,92,v,128
tr 76,225,4096
tr 78,225,6144
dl 237,40,90,2048
s_add_u32 s72,s72,s7
s_waitcnt vmcnt(36)
s_barrier
mz v,0,a,48,v,152
s_add_u32 m0,s75,0x1e000
tr 80,222,8192
tr 82,222,10240
mz v,8,a,52,v,152
wl 10
dl 235,40,72,0
tr 84,223,8192
tr 86,223,10240
mz v,16,a,56,v,152
dl 236,40,72,1024
tr 88,224,8192
tr 90,224,10240
mz v,24,a,60,v,152
wl 10
dl 237,40,72,2048
tr 92,225,8192
tr 94,225,10240
ma v,0,a,64,v,160
dl 238,40,72,3072
tr 48,222,12288
tr 50,222,14336
cv 200,56,57
ma v,8,a,68,v,160
s_add_u32 s90,s72,0xa000
tr 52,223,12288
tr 54,223,14336
wl 12
cv 201,58,59
ma v,16,a,72,v,160
s_add_u32 m0,s75,0x1f000
tr 56,224,12288
tr 58,224,14336
cv 202,72,73
ma v,24,a,76,v,160
wl 10
dl 235,40,90,0
tr 60,225,12288
tr 62,225,14336
cv 203,74,75
dl 236,40,90,1024
ma v,0,a,80,v,168
dl 237,40,90,2048
tr 64,222,16384
tr 66,222,18432
cv 204,88,89
ma v,8,a,84,v,168
wl 10
dl 238,40,90,3072
tr 68,223,16384
tr 70,223,18432
cv 205,90,91
ma v,16,a,88,v,168
s_add_u32 s90,s72,0x14000
tr 72,224,16384
tr 74,224,18432
cv 206,104,105
s_add_u32 m0,s75,0x20000
ma v,24,a,92,v,168
wl 10
dl 235,40,90,0
tr 76,225,16384
tr 78,225,18432
cv 207,106,107
ma v,0,a,48,v,176
dl 236,40,90,1024
tr 80,222,20480
tr 82,222,22528
dl 237,40,90,2048
ma v,8,a,52,v,176
wl 10
dl 238,40,90,3072
tr 84,223,20480
tr 86,223,22528
ma v,16,a,56,v,176
s_add_u32 s90,s72,0x1e000
tr 88,224,20480
tr 90,224,22528
ma v,24,a,60,v,176
s_add_u32 m0,s75,0x21000
tr 92,225,20480
tr 94,225,22528
wl 12
dl 235,40,90,0
ma v,0,a,64,v,184
dl 236,40,90,1024
tr 48,222,24576
tr 50,222,26624
ma v,8,a,68,v,184
wl 10
dl 237,40,90,2048
tr 52,223,24576
tr 54,223,26624
ma v,16,a,72,v,184
s_sub_u32 s72,s72,s8
tr 56,224,24576
tr 58,224,26624
s_cmp_ge_u32 s72,s74
ma v,24,a,76,v,184
s_cselect_b32 s72,s73,s72
tr 60,225,24576
tr 62,225,26624
wl 12
ma v,0,a,80,v,192
buffer_store_dwordx4 v[200:203],v239,s[44:47],s77 offen sc1
tr 64,222,28672
tr 66,222,30720
ma v,8,a,84,v,192
wl 10
buffer_store_dwordx4 v[204:207],v239,s[44:47],s77 offen offset:1024 sc1
tr 68,223,28672
tr 70,223,30720
s_add_u32 s77,s77,s10
ma v,16,a,88,v,192
s_cmp_ge_u32 s77,s79
tr 72,224,28672
tr 74,224,30720
wl 10
ma v,24,a,92,v,192
s_waitcnt vmcnt(36)
tr 76,225,28672
)"
R"(tr 78,225,30720
s_cselect_b32 s77,s78,s77
s_barrier
ma v,0,a,48,v,48
s_add_u32 s88,s64,1
tr 80,222,32768
tr 82,222,34816
ma v,8,a,52,v,48
s_and_b32 s88,s88,3
tr 84,223,32768
tr 86,223,34816
wl 12
ma v,16,a,56,v,48
s_lshl_b32 s88,s88,4
tr 88,224,32768
tr 90,224,34816
ma v,24,a,60,v,48
s_add_u32 s88,s88,s66
tr 92,225,32768
tr 94,225,34816
wl 12
s_lshr_b32 s89,s88,2
ma v,0,a,64,v,64
s_and_b32 s91,s88,1
tr 48,222,36864
tr 50,222,38912
s_lshr_b32 s88,s88,1
ma v,8,a,68,v,64
s_and_b32 s88,s88,1
tr 52,223,36864
tr 54,223,38912
wl 12
s_add_u32 s88,s88,s68
ma v,16,a,72,v,64
s_add_u32 s88,s88,4
tr 56,224,36864
tr 58,224,38912
s_cmp_ge_u32 s88,10
ma v,24,a,76,v,64
s_cselect_b32 s81,10,0
tr 60,225,36864
tr 62,225,38912
wl 12
s_sub_u32 s88,s88,s81
ma v,0,a,80,v,80
s_mul_i32 s81,s89,10
tr 64,222,40960
tr 66,222,43008
s_add_u32 s81,s81,s88
ma v,8,a,84,v,80
s_lshl_b32 s81,s81,5
tr 68,223,40960
tr 70,223,43008
wl 12
s_add_u32 s81,s81,s91
ma v,16,a,88,v,80
s_lshl_b32 s81,s81,10
tr 72,224,40960
tr 74,224,43008
s_mul_i32 s84,s89,0x5000
ma v,24,a,92,v,80
s_lshl_b32 s88,s88,1
tr 76,225,40960
tr 78,225,43008
wl 12
s_add_u32 s88,s88,s91
ma v,0,a,48,v,96
s_lshl_b32 s88,s88,6
tr 80,222,45056
tr 82,222,47104
s_add_u32 s84,s84,s88
s_add_u32 m0,s75,0xc000
ma v,8,a,52,v,96
wl 10
dl 235,40,72,0
tr 84,223,45056
tr 86,223,47104
dl 236,40,72,1024
ma v,16,a,56,v,96
dl 237,40,72,2048
tr 88,224,45056
tr 90,224,47104
dl 238,40,72,3072
ma v,24,a,60,v,96
s_add_u32 s90,s72,0xa000
tr 92,225,45056
tr 94,225,47104
wl 12
s_add_u32 m0,s75,0xd000
ma v,0,a,64,v,112
dl 235,40,90,0
v_add_u32 v222,0xffff4000,v222
v_add_u32 v223,0xffff4000,v223
v_add_u32 v224,0xffff4000,v224
v_add_u32 v225,0xffff4000,v225
tr 48,222,0
tr 50,222,2048
dl 236,40,90,1024
ma v,8,a,68,v,112
wl 10
dl 237,40,90,2048
tr 52,223,0
tr 54,223,2048
dl 238,40,90,3072
ma v,16,a,72,v,112
s_add_u32 s90,s72,0x14000
tr 56,224,0
tr 58,224,2048
s_add_u32 m0,s75,0xe000
ma v,24,a,76,v,112
wl 10
dl 235,40,90,0
tr 60,225,0
tr 62,225,2048
dl 236,40,90,1024
ma v,0,a,80,v,128
dl 237,40,90,2048
tr 64,222,4096
tr 66,222,6144
dl 238,40,90,3072
ma v,8,a,84,v,128
s_add_u32 s90,s72,0x1e000
tr 68,223,4096
tr 70,223,6144
wl 12
s_add_u32 m0,s75,0xf000
ma v,16,a,88,v,128
wl 8
dl 235,40,90,0
tr 72,224,4096
tr 74,224,6144
dl 236,40,90,1024
ma v,24,a,92,v,128
tr 76,225,4096
tr 78,225,6144
dl 237,40,90,2048
s_add_u32 s72,s72,s7
s_waitcnt vmcnt(36)
s_barrier
mz v,56,a,48,v,152
s_add_u32 m0,s75,0x12000
tr 80,222,8192
tr 82,222,10240
mz v,72,a,52,v,152
wl 10
dl 235,40,72,0
tr 84,223,8192
tr 86,223,10240
mz v,88,a,56,v,152
dl 236,40,72,1024
tr 88,224,8192
tr 90,224,10240
mz v,104,a,60,v,152
wl 10
dl 237,40,72,2048
tr 92,225,8192
tr 94,225,10240
ma v,56,a,64,v,160
dl 238,40,72,3072
tr 48,222,12288
tr 50,222,14336
cv 200,0,1
ma v,72,a,68,v,160
s_add_u32 s90,s72,0xa000
tr 52,223,12288
tr 54,223,14336
wl 12
cv 201,2,3
ma v,88,a,72,v,160
s_add_u32 m0,s75,0x13000
tr 56,224,12288
tr 58,224,14336
cv 202,8,9
ma v,104,a,76,v,160
wl 10
dl 235,40,90,0
tr 60,225,12288
tr 62,225,14336
cv 203,10,11
dl 236,40,90,1024
ma v,56,a,80,v,168
dl 237,40,90,2048
tr 64,222,16384
tr 66,222,18432
cv 204,16,17
ma v,72,a,84,v,168
wl 10
dl 238,40,90,3072
tr 68,223,16384
tr 70,223,18432
cv 205,18,19
ma v,88,a,88,v,168
s_add_u32 s90,s72,0x14000
tr 72,224,16384
tr 74,224,18432
cv 206,24,25
s_add_u32 m0,s75,0x14000
ma v,104,a,92,v,168
wl 10
dl 235,40,90,0
)"
R"(tr 76,225,16384
tr 78,225,18432
cv 207,26,27
ma v,56,a,48,v,176
dl 236,40,90,1024
tr 80,222,20480
tr 82,222,22528
dl 237,40,90,2048
ma v,72,a,52,v,176
wl 10
dl 238,40,90,3072
tr 84,223,20480
tr 86,223,22528
ma v,88,a,56,v,176
s_add_u32 s90,s72,0x1e000
tr 88,224,20480
tr 90,224,22528
ma v,104,a,60,v,176
s_add_u32 m0,s75,0x15000
tr 92,225,20480
tr 94,225,22528
wl 12
dl 235,40,90,0
ma v,56,a,64,v,184
dl 236,40,90,1024
tr 48,222,24576
tr 50,222,26624
ma v,72,a,68,v,184
wl 10
dl 237,40,90,2048
tr 52,223,24576
tr 54,223,26624
ma v,88,a,72,v,184
s_sub_u32 s72,s72,s8
tr 56,224,24576
tr 58,224,26624
s_cmp_ge_u32 s72,s74
ma v,104,a,76,v,184
s_cselect_b32 s72,s73,s72
tr 60,225,24576
tr 62,225,26624
wl 12
ma v,56,a,80,v,192
buffer_store_dwordx4 v[200:203],v239,s[44:47],s77 offen sc1
tr 64,222,28672
tr 66,222,30720
ma v,72,a,84,v,192
wl 10
buffer_store_dwordx4 v[204:207],v239,s[44:47],s77 offen offset:1024 sc1
tr 68,223,28672
tr 70,223,30720
s_add_u32 s77,s77,s10
ma v,88,a,88,v,192
s_cmp_ge_u32 s77,s79
tr 72,224,28672
tr 74,224,30720
wl 10
ma v,104,a,92,v,192
s_waitcnt vmcnt(36)
tr 76,225,28672
tr 78,225,30720
s_cselect_b32 s77,s78,s77
s_barrier
ma v,56,a,48,v,48
s_add_u32 s88,s64,1
tr 80,222,32768
tr 82,222,34816
ma v,72,a,52,v,48
s_and_b32 s88,s88,3
tr 84,223,32768
tr 86,223,34816
wl 12
ma v,88,a,56,v,48
s_lshl_b32 s88,s88,4
tr 88,224,32768
tr 90,224,34816
ma v,104,a,60,v,48
s_add_u32 s88,s88,s66
tr 92,225,32768
tr 94,225,34816
wl 12
s_lshr_b32 s89,s88,2
ma v,56,a,64,v,64
s_and_b32 s91,s88,1
tr 48,222,36864
tr 50,222,38912
s_lshr_b32 s88,s88,1
ma v,72,a,68,v,64
s_and_b32 s88,s88,1
tr 52,223,36864
tr 54,223,38912
wl 12
s_add_u32 s88,s88,s68
ma v,88,a,72,v,64
s_add_u32 s88,s88,6
tr 56,224,36864
tr 58,224,38912
s_cmp_ge_u32 s88,10
ma v,104,a,76,v,64
s_cselect_b32 s82,10,0
tr 60,225,36864
tr 62,225,38912
wl 12
s_sub_u32 s88,s88,s82
ma v,56,a,80,v,80
s_mul_i32 s82,s89,10
tr 64,222,40960
tr 66,222,43008
s_add_u32 s82,s82,s88
ma v,72,a,84,v,80
s_lshl_b32 s82,s82,5
tr 68,223,40960
tr 70,223,43008
wl 12
s_add_u32 s82,s82,s91
ma v,88,a,88,v,80
s_lshl_b32 s82,s82,10
tr 72,224,40960
tr 74,224,43008
s_mul_i32 s85,s89,0x5000
ma v,104,a,92,v,80
s_lshl_b32 s88,s88,1
tr 76,225,40960
tr 78,225,43008
wl 12
s_add_u32 s88,s88,s91
ma v,56,a,48,v,96
s_lshl_b32 s88,s88,6
tr 80,222,45056
tr 82,222,47104
s_add_u32 s85,s85,s88
s_mov_b32 m0,s75
ma v,72,a,52,v,96
wl 10
dl 235,40,72,0
tr 84,223,45056
tr 86,223,47104
dl 236,40,72,1024
ma v,88,a,56,v,96
dl 237,40,72,2048
tr 88,224,45056
tr 90,224,47104
dl 238,40,72,3072
ma v,104,a,60,v,96
s_add_u32 s90,s72,0xa000
tr 92,225,45056
tr 94,225,47104
wl 12
s_add_u32 m0,s75,0x1000
ma v,56,a,64,v,112
dl 235,40,90,0
v_add_u32 v222,0x18000,v222
v_add_u32 v223,0x18000,v223
v_add_u32 v224,0x18000,v224
v_add_u32 v225,0x18000,v225
tr 48,222,0
tr 50,222,2048
dl 236,40,90,1024
ma v,72,a,68,v,112
wl 10
dl 237,40,90,2048
tr 52,223,0
tr 54,223,2048
dl 238,40,90,3072
ma v,88,a,72,v,112
s_add_u32 s90,s72,0x14000
tr 56,224,0
tr 58,224,2048
s_add_u32 m0,s75,0x2000
ma v,104,a,76,v,112
wl 10
dl 235,40,90,0
tr 60,225,0
tr 62,225,2048
dl 236,40,90,1024
ma v,56,a,80,v,128
dl 237,40,90,2048
tr 64,222,4096
tr 66,222,6144
dl 238,40,90,3072
ma v,72,a,84,v,128
s_add_u32 s90,s72,0x1e000
tr 68,223,4096
tr 70,223,6144
wl 12
s_add_u32 m0,s75,0x3000
ma v,88,a,88,v,128
wl 8
dl 235,40,90,0
tr 72,224,4096
tr 74,224,6144
dl 236,40,90,1024
ma v,104,a,92,v,128
tr 76,225,4096
tr 78,225,6144
dl 237,40,90,2048
s_add_u32 s72,s72,s7
s_waitcnt vmcnt(36)
s_barrier
mz v,0,a,48,v,152
)"
R"(s_add_u32 m0,s75,0x6000
tr 80,222,8192
tr 82,222,10240
mz v,8,a,52,v,152
wl 10
dl 235,40,72,0
tr 84,223,8192
tr 86,223,10240
mz v,16,a,56,v,152
dl 236,40,72,1024
tr 88,224,8192
tr 90,224,10240
mz v,24,a,60,v,152
wl 10
dl 237,40,72,2048
tr 92,225,8192
tr 94,225,10240
ma v,0,a,64,v,160
dl 238,40,72,3072
tr 48,222,12288
tr 50,222,14336
cv 200,56,57
ma v,8,a,68,v,160
s_add_u32 s90,s72,0xa000
tr 52,223,12288
tr 54,223,14336
wl 12
cv 201,58,59
ma v,16,a,72,v,160
s_add_u32 m0,s75,0x7000
tr 56,224,12288
tr 58,224,14336
cv 202,72,73
ma v,24,a,76,v,160
wl 10
dl 235,40,90,0
tr 60,225,12288
tr 62,225,14336
cv 203,74,75
dl 236,40,90,1024
ma v,0,a,80,v,168
dl 237,40,90,2048
tr 64,222,16384
tr 66,222,18432
cv 204,88,89
ma v,8,a,84,v,168
wl 10
dl 238,40,90,3072
tr 68,223,16384
tr 70,223,18432
cv 205,90,91
ma v,16,a,88,v,168
s_add_u32 s90,s72,0x14000
tr 72,224,16384
tr 74,224,18432
cv 206,104,105
s_add_u32 m0,s75,0x8000
ma v,24,a,92,v,168
wl 10
dl 235,40,90,0
tr 76,225,16384
tr 78,225,18432
cv 207,106,107
ma v,0,a,48,v,176
dl 236,40,90,1024
tr 80,222,20480
tr 82,222,22528
dl 237,40,90,2048
ma v,8,a,52,v,176
wl 10
dl 238,40,90,3072
tr 84,223,20480
tr 86,223,22528
ma v,16,a,56,v,176
s_add_u32 s90,s72,0x1e000
tr 88,224,20480
tr 90,224,22528
ma v,24,a,60,v,176
s_add_u32 m0,s75,0x9000
tr 92,225,20480
tr 94,225,22528
wl 12
dl 235,40,90,0
ma v,0,a,64,v,184
dl 236,40,90,1024
tr 48,222,24576
tr 50,222,26624
ma v,8,a,68,v,184
wl 10
dl 237,40,90,2048
tr 52,223,24576
tr 54,223,26624
ma v,16,a,72,v,184
s_sub_u32 s72,s72,s8
tr 56,224,24576
tr 58,224,26624
s_cmp_ge_u32 s72,s74
ma v,24,a,76,v,184
s_cselect_b32 s72,s73,s72
tr 60,225,24576
tr 62,225,26624
wl 12
ma v,0,a,80,v,192
buffer_store_dwordx4 v[200:203],v239,s[44:47],s77 offen sc1
tr 64,222,28672
tr 66,222,30720
ma v,8,a,84,v,192
wl 10
buffer_store_dwordx4 v[204:207],v239,s[44:47],s77 offen offset:1024 sc1
tr 68,223,28672
tr 70,223,30720
s_add_u32 s77,s77,s10
ma v,16,a,88,v,192
s_cmp_ge_u32 s77,s79
tr 72,224,28672
tr 74,224,30720
wl 10
ma v,24,a,92,v,192
s_waitcnt vmcnt(0)
tr 76,225,28672
tr 78,225,30720
s_cselect_b32 s77,s78,s77
s_barrier
ma v,0,a,48,v,48
s_add_u32 s88,s64,1
tr 80,222,32768
tr 82,222,34816
ma v,8,a,52,v,48
s_and_b32 s88,s88,3
tr 84,223,32768
tr 86,223,34816
wl 12
ma v,16,a,56,v,48
s_lshl_b32 s88,s88,4
tr 88,224,32768
tr 90,224,34816
ma v,24,a,60,v,48
s_add_u32 s88,s88,s66
tr 92,225,32768
tr 94,225,34816
wl 12
s_lshr_b32 s89,s88,2
ma v,0,a,64,v,64
s_and_b32 s91,s88,1
tr 48,222,36864
tr 50,222,38912
s_lshr_b32 s88,s88,1
ma v,8,a,68,v,64
s_and_b32 s88,s88,1
tr 52,223,36864
tr 54,223,38912
wl 12
s_add_u32 s88,s88,s68
ma v,16,a,72,v,64
s_add_u32 s88,s88,8
tr 56,224,36864
tr 58,224,38912
s_cmp_ge_u32 s88,10
ma v,24,a,76,v,64
s_cselect_b32 s96,10,0
tr 60,225,36864
tr 62,225,38912
wl 12
s_sub_u32 s88,s88,s96
ma v,0,a,80,v,80
s_mul_i32 s96,s89,10
tr 64,222,40960
tr 66,222,43008
s_add_u32 s96,s96,s88
ma v,8,a,84,v,80
s_lshl_b32 s96,s96,5
tr 68,223,40960
tr 70,223,43008
wl 12
s_add_u32 s96,s96,s91
ma v,16,a,88,v,80
s_lshl_b32 s96,s96,10
tr 72,224,40960
tr 74,224,43008
s_mul_i32 s97,s89,0x5000
ma v,24,a,92,v,80
s_lshl_b32 s88,s88,1
tr 76,225,40960
tr 78,225,43008
wl 12
s_add_u32 s88,s88,s91
ma v,0,a,48,v,96
s_lshl_b32 s88,s88,6
tr 80,222,45056
tr 82,222,47104
s_add_u32 s97,s97,s88
s_add_u32 m0,s75,0x18000
ma v,8,a,52,v,96
wl 10
dl 235,40,72,0
tr 84,223,45056
tr 86,223,47104
dl 236,40,72,1024
ma v,16,a,56,v,96
dl 237,40,72,2048
)"
R"(tr 88,224,45056
tr 90,224,47104
dl 238,40,72,3072
ma v,24,a,60,v,96
s_add_u32 s90,s72,0xa000
tr 92,225,45056
tr 94,225,47104
wl 12
s_add_u32 m0,s75,0x19000
ma v,0,a,64,v,112
dl 235,40,90,0
v_add_u32 v222,0xffff4000,v222
v_add_u32 v223,0xffff4000,v223
v_add_u32 v224,0xffff4000,v224
v_add_u32 v225,0xffff4000,v225
tr 48,222,0
tr 50,222,2048
dl 236,40,90,1024
ma v,8,a,68,v,112
wl 10
dl 237,40,90,2048
tr 52,223,0
tr 54,223,2048
dl 238,40,90,3072
ma v,16,a,72,v,112
s_add_u32 s90,s72,0x14000
tr 56,224,0
tr 58,224,2048
s_add_u32 m0,s75,0x1a000
ma v,24,a,76,v,112
wl 10
dl 235,40,90,0
tr 60,225,0
tr 62,225,2048
dl 236,40,90,1024
ma v,0,a,80,v,128
dl 237,40,90,2048
tr 64,222,4096
tr 66,222,6144
dl 238,40,90,3072
ma v,8,a,84,v,128
s_add_u32 s90,s72,0x1e000
tr 68,223,4096
tr 70,223,6144
wl 12
s_add_u32 m0,s75,0x1b000
ma v,16,a,88,v,128
wl 8
dl 235,40,90,0
tr 72,224,4096
tr 74,224,6144
dl 236,40,90,1024
ma v,24,a,92,v,128
tr 76,225,4096
tr 78,225,6144
dl 237,40,90,2048
s_add_u32 s72,s72,s7
s_waitcnt vmcnt(36)
s_barrier
mz v,56,a,48,v,152
s_add_u32 m0,s75,0x1e000
tr 80,222,8192
tr 82,222,10240
mz v,72,a,52,v,152
wl 10
dl 235,40,72,0
tr 84,223,8192
tr 86,223,10240
mz v,88,a,56,v,152
dl 236,40,72,1024
tr 88,224,8192
tr 90,224,10240
mz v,104,a,60,v,152
wl 10
dl 237,40,72,2048
tr 92,225,8192
tr 94,225,10240
ma v,56,a,64,v,160
dl 238,40,72,3072
tr 48,222,12288
tr 50,222,14336
cv 200,0,1
ma v,72,a,68,v,160
s_add_u32 s90,s72,0xa000
tr 52,223,12288
tr 54,223,14336
wl 12
cv 201,2,3
ma v,88,a,72,v,160
s_add_u32 m0,s75,0x1f000
tr 56,224,12288
tr 58,224,14336
cv 202,8,9
ma v,104,a,76,v,160
wl 10
dl 235,40,90,0
tr 60,225,12288
tr 62,225,14336
cv 203,10,11
dl 236,40,90,1024
ma v,56,a,80,v,168
dl 237,40,90,2048
tr 64,222,16384
tr 66,222,18432
cv 204,16,17
ma v,72,a,84,v,168
wl 10
dl 238,40,90,3072
tr 68,223,16384
tr 70,223,18432
cv 205,18,19
ma v,88,a,88,v,168
s_add_u32 s90,s72,0x14000
tr 72,224,16384
tr 74,224,18432
cv 206,24,25
s_add_u32 m0,s75,0x20000
ma v,104,a,92,v,168
wl 10
dl 235,40,90,0
tr 76,225,16384
tr 78,225,18432
cv 207,26,27
ma v,56,a,48,v,176
dl 236,40,90,1024
tr 80,222,20480
tr 82,222,22528
dl 237,40,90,2048
ma v,72,a,52,v,176
wl 10
dl 238,40,90,3072
tr 84,223,20480
tr 86,223,22528
ma v,88,a,56,v,176
s_add_u32 s90,s72,0x1e000
tr 88,224,20480
tr 90,224,22528
ma v,104,a,60,v,176
s_add_u32 m0,s75,0x21000
tr 92,225,20480
tr 94,225,22528
wl 12
dl 235,40,90,0
ma v,56,a,64,v,184
dl 236,40,90,1024
tr 48,222,24576
tr 50,222,26624
ma v,72,a,68,v,184
wl 10
dl 237,40,90,2048
tr 52,223,24576
tr 54,223,26624
ma v,88,a,72,v,184
s_sub_u32 s72,s72,s8
tr 56,224,24576
tr 58,224,26624
s_cmp_ge_u32 s72,s74
ma v,104,a,76,v,184
s_cselect_b32 s72,s73,s72
tr 60,225,24576
tr 62,225,26624
wl 12
ma v,56,a,80,v,192
buffer_store_dwordx4 v[200:203],v239,s[44:47],s77 offen sc1
tr 64,222,28672
tr 66,222,30720
ma v,72,a,84,v,192
wl 10
buffer_store_dwordx4 v[204:207],v239,s[44:47],s77 offen offset:1024 sc1
tr 68,223,28672
tr 70,223,30720
s_add_u32 s77,s77,s10
ma v,88,a,88,v,192
s_cmp_ge_u32 s77,s79
tr 72,224,28672
tr 74,224,30720
wl 10
ma v,104,a,92,v,192
s_waitcnt vmcnt(36)
tr 76,225,28672
tr 78,225,30720
s_cselect_b32 s77,s78,s77
s_barrier
ma v,56,a,48,v,48
global_load_dwordx2 v[38:39],v33,s[52:53] offset:0 sc1
tr 80,222,32768
tr 82,222,34816
ma v,72,a,52,v,48
s_add_u32 m0,s75,0xc000
tr 84,223,32768
tr 86,223,34816
wl 12
ma v,88,a,56,v,48
dl 235,40,72,0
tr 88,224,32768
tr 90,224,34816
ma v,104,a,60,v,48
wl 10
dl 236,40,72,1024
tr 92,225,32768
)"
R"(tr 94,225,34816
ma v,56,a,64,v,64
dl 237,40,72,2048
tr 48,222,36864
tr 50,222,38912
ma v,72,a,68,v,64
wl 10
dl 238,40,72,3072
tr 52,223,36864
tr 54,223,38912
ma v,88,a,72,v,64
s_add_u32 s90,s72,0xa000
tr 56,224,36864
tr 58,224,38912
ma v,104,a,76,v,64
s_add_u32 m0,s75,0xd000
tr 60,225,36864
tr 62,225,38912
wl 12
ma v,56,a,80,v,80
dl 235,40,90,0
tr 64,222,40960
tr 66,222,43008
ma v,72,a,84,v,80
wl 10
dl 236,40,90,1024
tr 68,223,40960
tr 70,223,43008
ma v,88,a,88,v,80
dl 237,40,90,2048
tr 72,224,40960
tr 74,224,43008
ma v,104,a,92,v,80
wl 10
dl 238,40,90,3072
tr 76,225,40960
tr 78,225,43008
ma v,56,a,48,v,96
s_add_u32 s90,s72,0x14000
tr 80,222,45056
tr 82,222,47104
ma v,72,a,52,v,96
s_add_u32 m0,s75,0xe000
tr 84,223,45056
tr 86,223,47104
wl 12
ma v,88,a,56,v,96
dl 235,40,90,0
tr 88,224,45056
tr 90,224,47104
ma v,104,a,60,v,96
wl 10
dl 236,40,90,1024
tr 92,225,45056
tr 94,225,47104
ma v,56,a,64,v,112
dl 237,40,90,2048
v_add_u32 v222,0xffff4000,v222
v_add_u32 v223,0xffff4000,v223
v_add_u32 v224,0xffff4000,v224
v_add_u32 v225,0xffff4000,v225
tr 48,222,0
tr 50,222,2048
ma v,72,a,68,v,112
wl 10
dl 238,40,90,3072
tr 52,223,0
tr 54,223,2048
ma v,88,a,72,v,112
s_add_u32 s90,s72,0x1e000
tr 56,224,0
tr 58,224,2048
ma v,104,a,76,v,112
s_add_u32 m0,s75,0xf000
tr 60,225,0
tr 62,225,2048
wl 12
ma v,56,a,80,v,128
dl 235,40,90,0
tr 64,222,4096
tr 66,222,6144
ma v,72,a,84,v,128
wl 10
dl 236,40,90,1024
tr 68,223,4096
tr 70,223,6144
ma v,88,a,88,v,128
wl 8
dl 237,40,90,2048
tr 72,224,4096
tr 74,224,6144
ma v,104,a,92,v,128
s_waitcnt vmcnt(15)
tr 76,225,4096
tr 78,225,6144
s_add_u32 s72,s72,s7
s_mov_b32 s70,0
LB_pl0_%=:
v_cmp_ne_u64_e64 vcc,v[38:39],s[56:57]
s_add_u32 s70,s70,1
s_cbranch_vccz LB_pk0_%=
s_sleep 1
global_load_dwordx2 v[38:39],v33,s[52:53] offset:0 sc1
s_waitcnt vmcnt(0)
s_cmp_lt_u32 s70,0x40000
s_cbranch_scc1 LB_pl0_%=
LB_pk0_%=:
s_barrier
mz v,0,a,48,v,152
s_add_u32 m0,s75,0x12000
tr 80,222,8192
tr 82,222,10240
mz v,8,a,52,v,152
wl 10
dl 235,40,72,0
tr 84,223,8192
tr 86,223,10240
mz v,16,a,56,v,152
dl 236,40,72,1024
tr 88,224,8192
tr 90,224,10240
mz v,24,a,60,v,152
wl 10
dl 237,40,72,2048
tr 92,225,8192
tr 94,225,10240
ma v,0,a,64,v,160
dl 238,40,72,3072
tr 48,222,12288
tr 50,222,14336
cv 200,56,57
ma v,8,a,68,v,160
s_add_u32 s90,s72,0xa000
tr 52,223,12288
tr 54,223,14336
wl 12
cv 201,58,59
s_add_u32 m0,s75,0x13000
ma v,16,a,72,v,160
dl 235,40,90,0
tr 56,224,12288
tr 58,224,14336
cv 202,72,73
ma v,24,a,76,v,160
wl 10
dl 236,40,90,1024
tr 60,225,12288
tr 62,225,14336
cv 203,74,75
dl 237,40,90,2048
ma v,0,a,80,v,168
dl 238,40,90,3072
tr 64,222,16384
tr 66,222,18432
cv 204,88,89
ma v,8,a,84,v,168
s_add_u32 s90,s72,0x14000
tr 68,223,16384
tr 70,223,18432
wl 12
cv 205,90,91
s_add_u32 m0,s75,0x14000
ma v,16,a,88,v,168
dl 235,40,90,0
tr 72,224,16384
tr 74,224,18432
cv 206,104,105
ma v,24,a,92,v,168
wl 10
dl 236,40,90,1024
tr 76,225,16384
tr 78,225,18432
cv 207,106,107
dl 237,40,90,2048
ma v,0,a,48,v,176
dl 238,40,90,3072
tr 80,222,20480
tr 82,222,22528
ma v,8,a,52,v,176
s_add_u32 s90,s72,0x1e000
tr 84,223,20480
tr 86,223,22528
wl 12
s_add_u32 m0,s75,0x15000
ma v,16,a,56,v,176
dl 235,40,90,0
tr 88,224,20480
tr 90,224,22528
ma v,24,a,60,v,176
wl 10
dl 236,40,90,1024
tr 92,225,20480
tr 94,225,22528
dl 237,40,90,2048
ma v,0,a,64,v,184
s_sub_u32 s72,s72,s8
tr 48,222,24576
tr 50,222,26624
ma v,8,a,68,v,184
s_cmp_ge_u32 s72,s74
)"
R"(tr 52,223,24576
tr 54,223,26624
wl 12
s_cselect_b32 s72,s73,s72
ma v,16,a,72,v,184
buffer_load_dwordx4 a[96:99],v239,s[44:47],s80 offen sc1
tr 56,224,24576
tr 58,224,26624
ma v,24,a,76,v,184
wl 10
buffer_load_dwordx4 a[100:103],v239,s[44:47],s80 offen offset:2048 sc1
tr 60,225,24576
tr 62,225,26624
s_addk_i32 s80,4096
ma v,0,a,80,v,192
buffer_store_dwordx4 v[200:203],v239,s[44:47],s77 offen sc1
tr 64,222,28672
tr 66,222,30720
ma v,8,a,84,v,192
wl 10
buffer_store_dwordx4 v[204:207],v239,s[44:47],s77 offen offset:1024 sc1
tr 68,223,28672
tr 70,223,30720
s_add_u32 s77,s77,s10
ma v,16,a,88,v,192
s_cmp_ge_u32 s77,s79
tr 72,224,28672
tr 74,224,30720
wl 10
ma v,24,a,92,v,192
s_waitcnt vmcnt(36)
tr 76,225,28672
tr 78,225,30720
s_cselect_b32 s77,s78,s77
s_barrier
ma v,0,a,48,v,48
s_mov_b32 m0,s75
tr 80,222,32768
tr 82,222,34816
ma v,8,a,52,v,48
wl 10
dl 235,40,72,0
tr 84,223,32768
tr 86,223,34816
ma v,16,a,56,v,48
dl 236,40,72,1024
tr 88,224,32768
tr 90,224,34816
ma v,24,a,60,v,48
wl 10
dl 237,40,72,2048
tr 92,225,32768
tr 94,225,34816
ma v,0,a,64,v,64
dl 238,40,72,3072
tr 48,222,36864
tr 50,222,38912
ma v,8,a,68,v,64
s_add_u32 s90,s72,0xa000
tr 52,223,36864
tr 54,223,38912
wl 12
ma v,16,a,72,v,64
s_add_u32 m0,s75,0x1000
tr 56,224,36864
tr 58,224,38912
ma v,24,a,76,v,64
wl 10
dl 235,40,90,0
tr 60,225,36864
tr 62,225,38912
ma v,0,a,80,v,80
dl 236,40,90,1024
tr 64,222,40960
tr 66,222,43008
ma v,8,a,84,v,80
wl 10
dl 237,40,90,2048
tr 68,223,40960
tr 70,223,43008
ma v,16,a,88,v,80
dl 238,40,90,3072
tr 72,224,40960
tr 74,224,43008
ma v,24,a,92,v,80
s_add_u32 s90,s72,0x14000
tr 76,225,40960
tr 78,225,43008
wl 12
ma v,0,a,48,v,96
s_add_u32 m0,s75,0x2000
tr 80,222,45056
tr 82,222,47104
dl 235,40,90,0
ma v,8,a,52,v,96
wl 10
dl 236,40,90,1024
tr 84,223,45056
tr 86,223,47104
ma v,16,a,56,v,96
dl 237,40,90,2048
tr 88,224,45056
tr 90,224,47104
ma v,24,a,60,v,96
wl 10
dl 238,40,90,3072
tr 92,225,45056
tr 94,225,47104
ma v,0,a,64,v,112
s_add_u32 s90,s72,0x1e000
v_add_u32 v222,0x18000,v222
v_add_u32 v223,0x18000,v223
v_add_u32 v224,0x18000,v224
v_add_u32 v225,0x18000,v225
tr 48,222,0
tr 50,222,2048
ma v,8,a,68,v,112
s_add_u32 m0,s75,0x3000
tr 52,223,0
tr 54,223,2048
wl 12
ma v,16,a,72,v,112
dl 235,40,90,0
tr 56,224,0
tr 58,224,2048
dl 236,40,90,1024
ma v,24,a,76,v,112
wl 10
dl 237,40,90,2048
tr 60,225,0
tr 62,225,2048
ma v,0,a,80,v,128
s_add_u32 s72,s72,s7
tr 64,222,4096
tr 66,222,6144
ma v,8,a,84,v,128
wl 10
buffer_load_dwordx4 a[104:107],v239,s[44:47],s80 offen sc1
tr 68,223,4096
tr 70,223,6144
ma v,16,a,88,v,128
wl 8
buffer_load_dwordx4 a[108:111],v239,s[44:47],s80 offen offset:2048 sc1
tr 72,224,4096
tr 74,224,6144
ma v,24,a,92,v,128
s_waitcnt vmcnt(17)
tr 76,225,4096
tr 78,225,6144
s_addk_i32 s80,4096
s_barrier
mz v,56,a,48,v,152
s_add_u32 m0,s75,0x6000
tr 80,222,8192
tr 82,222,10240
mz v,72,a,52,v,152
wl 10
dl 235,40,72,0
tr 84,223,8192
tr 86,223,10240
mz v,88,a,56,v,152
dl 236,40,72,1024
tr 88,224,8192
tr 90,224,10240
mz v,104,a,60,v,152
wl 10
dl 237,40,72,2048
tr 92,225,8192
tr 94,225,10240
ma v,56,a,64,v,160
dl 238,40,72,3072
tr 48,222,12288
tr 50,222,14336
cv 200,0,1
ma v,72,a,68,v,160
s_add_u32 s90,s72,0xa000
tr 52,223,12288
tr 54,223,14336
wl 12
cv 201,2,3
s_add_u32 m0,s75,0x7000
ma v,88,a,72,v,160
dl 235,40,90,0
tr 56,224,12288
tr 58,224,14336
cv 202,8,9
ma v,104,a,76,v,160
wl 10
dl 236,40,90,1024
tr 60,225,12288
tr 62,225,14336
cv 203,10,11
dl 237,40,90,2048
ma v,56,a,80,v,168
dl 238,40,90,3072
tr 64,222,16384
tr 66,222,18432
cv 204,16,17
ma v,72,a,84,v,168
s_add_u32 s90,s72,0x14000
)"
R"(tr 68,223,16384
tr 70,223,18432
wl 12
cv 205,18,19
s_add_u32 m0,s75,0x8000
ma v,88,a,88,v,168
dl 235,40,90,0
tr 72,224,16384
tr 74,224,18432
cv 206,24,25
ma v,104,a,92,v,168
wl 10
dl 236,40,90,1024
tr 76,225,16384
tr 78,225,18432
cv 207,26,27
dl 237,40,90,2048
ma v,56,a,48,v,176
dl 238,40,90,3072
tr 80,222,20480
tr 82,222,22528
ma v,72,a,52,v,176
s_add_u32 s90,s72,0x1e000
tr 84,223,20480
tr 86,223,22528
wl 12
s_add_u32 m0,s75,0x9000
ma v,88,a,56,v,176
dl 235,40,90,0
tr 88,224,20480
tr 90,224,22528
ma v,104,a,60,v,176
wl 10
dl 236,40,90,1024
tr 92,225,20480
tr 94,225,22528
dl 237,40,90,2048
ma v,56,a,64,v,184
s_sub_u32 s72,s72,s8
tr 48,222,24576
tr 50,222,26624
ma v,72,a,68,v,184
s_cmp_ge_u32 s72,s74
tr 52,223,24576
tr 54,223,26624
wl 12
s_cselect_b32 s72,s73,s72
ma v,88,a,72,v,184
buffer_load_dwordx4 a[112:115],v239,s[44:47],s80 offen sc1
tr 56,224,24576
tr 58,224,26624
ma v,104,a,76,v,184
wl 10
buffer_load_dwordx4 a[116:119],v239,s[44:47],s80 offen offset:2048 sc1
tr 60,225,24576
tr 62,225,26624
s_addk_i32 s80,4096
ma v,56,a,80,v,192
buffer_store_dwordx4 v[200:203],v239,s[44:47],s77 offen sc1
tr 64,222,28672
tr 66,222,30720
ma v,72,a,84,v,192
wl 10
buffer_store_dwordx4 v[204:207],v239,s[44:47],s77 offen offset:1024 sc1
tr 68,223,28672
tr 70,223,30720
s_add_u32 s77,s77,s10
ma v,88,a,88,v,192
s_cmp_ge_u32 s77,s79
tr 72,224,28672
tr 74,224,30720
wl 10
ma v,104,a,92,v,192
s_waitcnt vmcnt(36)
tr 76,225,28672
tr 78,225,30720
s_cselect_b32 s77,s78,s77
s_barrier
ma v,56,a,48,v,48
buffer_load_dwordx4 a[120:123],v239,s[44:47],s80 offen sc1
tr 80,222,32768
tr 82,222,34816
ma v,72,a,52,v,48
wl 10
tr 84,223,32768
tr 86,223,34816
ma v,88,a,56,v,48
tr 88,224,32768
tr 90,224,34816
ma v,104,a,60,v,48
wl 10
tr 92,225,32768
tr 94,225,34816
ma v,56,a,64,v,64
tr 48,222,36864
tr 50,222,38912
ma v,72,a,68,v,64
wl 10
tr 52,223,36864
tr 54,223,38912
ma v,88,a,72,v,64
tr 56,224,36864
tr 58,224,38912
ma v,104,a,76,v,64
wl 10
buffer_load_dwordx4 a[124:127],v239,s[44:47],s80 offen offset:2048 sc1
tr 60,225,36864
tr 62,225,38912
ma v,56,a,80,v,80
tr 64,222,40960
tr 66,222,43008
ma v,72,a,84,v,80
wl 10
tr 68,223,40960
tr 70,223,43008
ma v,88,a,88,v,80
tr 72,224,40960
tr 74,224,43008
ma v,104,a,92,v,80
wl 10
tr 76,225,40960
tr 78,225,43008
ma v,56,a,48,v,96
tr 80,222,45056
tr 82,222,47104
ma v,72,a,52,v,96
wl 10
tr 84,223,45056
tr 86,223,47104
ma v,88,a,56,v,96
s_addk_i32 s80,4096
tr 88,224,45056
tr 90,224,47104
ma v,104,a,60,v,96
wl 10
tr 92,225,45056
tr 94,225,47104
ma v,56,a,64,v,112
v_add_u32 v222,0xffff4000,v222
v_add_u32 v223,0xffff4000,v223
v_add_u32 v224,0xffff4000,v224
v_add_u32 v225,0xffff4000,v225
tr 48,222,0
tr 50,222,2048
ma v,72,a,68,v,112
wl 10
tr 52,223,0
tr 54,223,2048
ma v,88,a,72,v,112
tr 56,224,0
tr 58,224,2048
ma v,104,a,76,v,112
wl 10
tr 60,225,0
tr 62,225,2048
ma v,56,a,80,v,128
tr 64,222,4096
tr 66,222,6144
ma v,72,a,84,v,128
wl 10
tr 68,223,4096
tr 70,223,6144
ma v,88,a,88,v,128
wl 8
tr 72,224,4096
tr 74,224,6144
ma v,104,a,92,v,128
s_waitcnt vmcnt(36)
tr 76,225,4096
tr 78,225,6144
s_barrier
mz v,0,a,48,v,152
global_load_dwordx2 v[38:39],v33,s[52:53] offset:8 sc1
tr 80,222,8192
tr 82,222,10240
mz v,8,a,52,v,152
wl 10
tr 84,223,8192
tr 86,223,10240
mz v,16,a,56,v,152
buffer_load_dwordx4 a[128:131],v239,s[44:47],s80 offen sc1
tr 88,224,8192
tr 90,224,10240
mz v,24,a,60,v,152
wl 10
tr 92,225,8192
tr 94,225,10240
ma v,0,a,64,v,160
buffer_load_dwordx4 a[132:135],v239,s[44:47],s80 offen offset:2048 sc1
tr 48,222,12288
tr 50,222,14336
cv 200,56,57
ma v,8,a,68,v,160
wl 10
tr 52,223,12288
tr 54,223,14336
cv 201,58,59
ma v,16,a,72,v,160
tr 56,224,12288
tr 58,224,14336
)"
R"(cv 202,72,73
ma v,24,a,76,v,160
wl 10
tr 60,225,12288
tr 62,225,14336
cv 203,74,75
ma v,0,a,80,v,168
s_addk_i32 s80,4096
tr 64,222,16384
tr 66,222,18432
cv 204,88,89
ma v,8,a,84,v,168
wl 10
tr 68,223,16384
tr 70,223,18432
cv 205,90,91
ma v,16,a,88,v,168
tr 72,224,16384
tr 74,224,18432
cv 206,104,105
ma v,24,a,92,v,168
wl 10
tr 76,225,16384
tr 78,225,18432
cv 207,106,107
ma v,0,a,48,v,176
tr 80,222,20480
tr 82,222,22528
ma v,8,a,52,v,176
wl 10
buffer_store_dwordx4 v[200:203],v239,s[44:47],s77 offen sc1
tr 84,223,20480
tr 86,223,22528
ma v,16,a,56,v,176
buffer_store_dwordx4 v[204:207],v239,s[44:47],s77 offen offset:1024 sc1
tr 88,224,20480
tr 90,224,22528
ma v,24,a,60,v,176
s_add_u32 s77,s77,s10
tr 92,225,20480
tr 94,225,22528
wl 12
ma v,0,a,64,v,184
tr 48,222,24576
tr 50,222,26624
ma v,8,a,68,v,184
wl 10
tr 52,223,24576
tr 54,223,26624
ma v,16,a,72,v,184
s_cmp_ge_u32 s77,s79
tr 56,224,24576
tr 58,224,26624
ma v,24,a,76,v,184
wl 10
tr 60,225,24576
tr 62,225,26624
ma v,0,a,80,v,192
s_cselect_b32 s77,s78,s77
tr 64,222,28672
tr 66,222,30720
ma v,8,a,84,v,192
wl 10
tr 68,223,28672
tr 70,223,30720
ma v,16,a,88,v,192
wl 8
tr 72,224,28672
tr 74,224,30720
ma v,24,a,92,v,192
s_waitcnt vmcnt(4)
tr 76,225,28672
tr 78,225,30720
s_mov_b32 s70,0
LB_pl1_%=:
v_cmp_ne_u64_e64 vcc,v[38:39],s[56:57]
s_add_u32 s70,s70,1
s_cbranch_vccz LB_pk1_%=
s_sleep 1
global_load_dwordx2 v[38:39],v33,s[52:53] offset:8 sc1
s_waitcnt vmcnt(0)
s_cmp_lt_u32 s70,0x40000
s_cbranch_scc1 LB_pl1_%=
LB_pk1_%=:
s_barrier
ma v,0,a,48,v,48
buffer_load_dwordx4 a[136:139],v239,s[44:47],s80 offen sc1
tr 80,222,32768
tr 82,222,34816
ma v,8,a,52,v,48
wl 10
tr 84,223,32768
tr 86,223,34816
ma v,16,a,56,v,48
buffer_load_dwordx4 a[140:143],v239,s[44:47],s80 offen offset:2048 sc1
tr 88,224,32768
tr 90,224,34816
ma v,24,a,60,v,48
wl 10
tr 92,225,32768
tr 94,225,34816
ma v,0,a,64,v,64
s_addk_i32 s80,4096
tr 48,222,36864
tr 50,222,38912
ma v,8,a,68,v,64
wl 10
tr 52,223,36864
tr 54,223,38912
ma v,16,a,72,v,64
buffer_load_dwordx4 a[160:163],v239,s[44:47],s81 offen sc1
tr 56,224,36864
tr 58,224,38912
ma v,24,a,76,v,64
wl 10
tr 60,225,36864
tr 62,225,38912
ma v,0,a,80,v,80
buffer_load_dwordx4 a[164:167],v239,s[44:47],s81 offen offset:2048 sc1
tr 64,222,40960
tr 66,222,43008
ma v,8,a,84,v,80
wl 10
tr 68,223,40960
tr 70,223,43008
ma v,16,a,88,v,80
s_addk_i32 s81,4096
tr 72,224,40960
tr 74,224,43008
ma v,24,a,92,v,80
wl 10
tr 76,225,40960
tr 78,225,43008
ma v,0,a,48,v,96
buffer_load_dwordx4 a[168:171],v239,s[44:47],s81 offen sc1
tr 80,222,45056
tr 82,222,47104
ma v,8,a,52,v,96
wl 10
tr 84,223,45056
tr 86,223,47104
ma v,16,a,56,v,96
buffer_load_dwordx4 a[172:175],v239,s[44:47],s81 offen offset:2048 sc1
tr 88,224,45056
tr 90,224,47104
ma v,24,a,60,v,96
wl 10
tr 92,225,45056
tr 94,225,47104
ma v,0,a,64,v,112
s_addk_i32 s81,4096
v_add_u32 v222,0xffff4000,v222
v_add_u32 v223,0xffff4000,v223
v_add_u32 v224,0xffff4000,v224
v_add_u32 v225,0xffff4000,v225
tr 48,222,0
tr 50,222,2048
ma v,8,a,68,v,112
wl 10
tr 52,223,0
tr 54,223,2048
ma v,16,a,72,v,112
buffer_load_dwordx4 a[176:179],v239,s[44:47],s81 offen sc1
tr 56,224,0
tr 58,224,2048
ma v,24,a,76,v,112
wl 10
tr 60,225,0
tr 62,225,2048
ma v,0,a,80,v,128
buffer_load_dwordx4 a[180:183],v239,s[44:47],s81 offen offset:2048 sc1
tr 64,222,4096
tr 66,222,6144
ma v,8,a,84,v,128
wl 10
tr 68,223,4096
tr 70,223,6144
ma v,16,a,88,v,128
wl 8
tr 72,224,4096
tr 74,224,6144
ma v,24,a,92,v,128
s_waitcnt vmcnt(19)
tr 76,225,4096
tr 78,225,6144
s_barrier
mz v,56,a,48,v,152
buffer_load_dwordx4 a[144:147],v239,s[44:47],s80 offen sc1
tr 80,222,8192
tr 82,222,10240
mz v,72,a,52,v,152
wl 10
buffer_load_dwordx4 a[148:151],v239,s[44:47],s80 offen offset:2048 sc1
tr 84,223,8192
tr 86,223,10240
mz v,88,a,56,v,152
s_addk_i32 s80,4096
tr 88,224,8192
tr 90,224,10240
mz v,104,a,60,v,152
)"
R"(wl 10
tr 92,225,8192
tr 94,225,10240
ma v,56,a,64,v,160
s_addk_i32 s81,4096
tr 48,222,12288
tr 50,222,14336
cv 200,0,1
ma v,72,a,68,v,160
wl 10
buffer_load_dwordx4 a[184:187],v239,s[44:47],s81 offen sc1
tr 52,223,12288
tr 54,223,14336
cv 201,2,3
ma v,88,a,72,v,160
buffer_load_dwordx4 a[188:191],v239,s[44:47],s81 offen offset:2048 sc1
tr 56,224,12288
tr 58,224,14336
cv 202,8,9
ma v,104,a,76,v,160
wl 10
tr 60,225,12288
tr 62,225,14336
cv 203,10,11
ma v,56,a,80,v,168
s_addk_i32 s81,4096
tr 64,222,16384
tr 66,222,18432
cv 204,16,17
ma v,72,a,84,v,168
wl 10
buffer_load_dwordx4 a[192:195],v239,s[44:47],s81 offen sc1
tr 68,223,16384
tr 70,223,18432
cv 205,18,19
ma v,88,a,88,v,168
tr 72,224,16384
tr 74,224,18432
cv 206,24,25
ma v,104,a,92,v,168
wl 10
buffer_load_dwordx4 a[196:199],v239,s[44:47],s81 offen offset:2048 sc1
tr 76,225,16384
tr 78,225,18432
cv 207,26,27
ma v,56,a,48,v,176
s_addk_i32 s81,4096
tr 80,222,20480
tr 82,222,22528
ma v,72,a,52,v,176
wl 10
buffer_load_dwordx4 a[200:203],v239,s[44:47],s81 offen sc1
tr 84,223,20480
tr 86,223,22528
ma v,88,a,56,v,176
tr 88,224,20480
tr 90,224,22528
ma v,104,a,60,v,176
wl 10
buffer_store_dwordx4 v[200:203],v239,s[44:47],s77 offen sc1
tr 92,225,20480
tr 94,225,22528
ma v,56,a,64,v,184
buffer_store_dwordx4 v[204:207],v239,s[44:47],s77 offen offset:1024 sc1
tr 48,222,24576
tr 50,222,26624
ma v,72,a,68,v,184
wl 10
tr 52,223,24576
tr 54,223,26624
ma v,88,a,72,v,184
s_add_u32 s77,s77,s10
tr 56,224,24576
tr 58,224,26624
ma v,104,a,76,v,184
wl 10
tr 60,225,24576
tr 62,225,26624
ma v,56,a,80,v,192
s_cmp_ge_u32 s77,s79
tr 64,222,28672
tr 66,222,30720
ma v,72,a,84,v,192
s_cselect_b32 s77,s78,s77
tr 68,223,28672
tr 70,223,30720
wl 12
ma v,88,a,88,v,192
wl 8
tr 72,224,28672
tr 74,224,30720
ma v,104,a,92,v,192
s_waitcnt vmcnt(17)
tr 76,225,28672
tr 78,225,30720
s_barrier
ma v,56,a,48,v,48
buffer_load_dwordx4 a[152:155],v239,s[44:47],s80 offen sc1
tr 80,222,32768
tr 82,222,34816
ma v,72,a,52,v,48
wl 10
tr 84,223,32768
tr 86,223,34816
ma v,88,a,56,v,48
buffer_load_dwordx4 a[156:159],v239,s[44:47],s80 offen offset:2048 sc1
tr 88,224,32768
tr 90,224,34816
ma v,104,a,60,v,48
wl 10
tr 92,225,32768
tr 94,225,34816
ma v,56,a,64,v,64
buffer_load_dwordx4 a[204:207],v239,s[44:47],s81 offen offset:2048 sc1
tr 48,222,36864
tr 50,222,38912
ma v,72,a,68,v,64
wl 10
tr 52,223,36864
tr 54,223,38912
ma v,88,a,72,v,64
tr 56,224,36864
tr 58,224,38912
ma v,104,a,76,v,64
wl 10
tr 60,225,36864
tr 62,225,38912
ma v,56,a,80,v,80
s_addk_i32 s81,4096
tr 64,222,40960
tr 66,222,43008
ma v,72,a,84,v,80
wl 10
buffer_load_dwordx4 a[208:211],v239,s[44:47],s81 offen sc1
tr 68,223,40960
tr 70,223,43008
ma v,88,a,88,v,80
tr 72,224,40960
tr 74,224,43008
ma v,104,a,92,v,80
wl 10
tr 76,225,40960
tr 78,225,43008
ma v,56,a,48,v,96
buffer_load_dwordx4 a[212:215],v239,s[44:47],s81 offen offset:2048 sc1
tr 80,222,45056
tr 82,222,47104
ma v,72,a,52,v,96
wl 10
tr 84,223,45056
tr 86,223,47104
ma v,88,a,56,v,96
s_addk_i32 s81,4096
tr 88,224,45056
tr 90,224,47104
ma v,104,a,60,v,96
wl 10
tr 92,225,45056
tr 94,225,47104
ma v,56,a,64,v,112
ma v,72,a,68,v,112
wl 8
buffer_load_dwordx4 a[216:219],v239,s[44:47],s81 offen sc1
ma v,88,a,72,v,112
ma v,104,a,76,v,112
wl 4
buffer_load_dwordx4 a[220:223],v239,s[44:47],s81 offen offset:2048 sc1
ma v,56,a,80,v,128
ma v,72,a,84,v,128
wl 0
ma v,88,a,88,v,128
ma v,104,a,92,v,128
s_nop 7
cv 200,56,57
cv 201,58,59
cv 202,72,73
cv 203,74,75
cv 204,88,89
cv 205,90,91
cv 206,104,105
cv 207,106,107
buffer_store_dwordx4 v[200:203],v239,s[44:47],s77 offen sc1
buffer_store_dwordx4 v[204:207],v239,s[44:47],s77 offen offset:1024 sc1
s_add_u32 s77,s77,s10
s_cmp_ge_u32 s77,s79
s_cselect_b32 s77,s78,s77
global_load_dwordx2 v[38:39],v33,s[52:53] offset:16 sc1
s_waitcnt vmcnt(0)
s_mov_b32 s70,0
LB_pl2_%=:
v_cmp_ne_u64_e64 vcc,v[38:39],s[56:57]
s_add_u32 s70,s70,1
s_cbranch_vccz LB_pk2_%=
s_sleep 1
global_load_dwordx2 v[38:39],v33,s[52:53] offset:16 sc1
s_waitcnt vmcnt(0)
s_cmp_lt_u32 s70,0x40000
s_cbranch_scc1 LB_pl2_%=
LB_pk2_%=:
s_waitcnt vmcnt(0)
)"
R"(s_barrier
buffer_load_dwordx4 v[64:67],v239,s[44:47],s82 offen sc1
buffer_load_dwordx4 v[68:71],v239,s[44:47],s82 offen offset:2048 sc1
s_addk_i32 s82,4096
buffer_load_dwordx4 v[72:75],v239,s[44:47],s82 offen sc1
buffer_load_dwordx4 v[76:79],v239,s[44:47],s82 offen offset:2048 sc1
s_addk_i32 s82,4096
buffer_load_dwordx4 v[80:83],v239,s[44:47],s82 offen sc1
buffer_load_dwordx4 v[84:87],v239,s[44:47],s82 offen offset:2048 sc1
s_addk_i32 s82,4096
buffer_load_dwordx4 v[88:91],v239,s[44:47],s82 offen sc1
buffer_load_dwordx4 v[92:95],v239,s[44:47],s82 offen offset:2048 sc1
s_addk_i32 s82,4096
buffer_load_dwordx4 v[96:99],v239,s[44:47],s82 offen sc1
buffer_load_dwordx4 v[100:103],v239,s[44:47],s82 offen offset:2048 sc1
s_addk_i32 s82,4096
buffer_load_dwordx4 v[104:107],v239,s[44:47],s82 offen sc1
buffer_load_dwordx4 v[108:111],v239,s[44:47],s82 offen offset:2048 sc1
s_addk_i32 s82,4096
buffer_load_dwordx4 v[112:115],v239,s[44:47],s82 offen sc1
buffer_load_dwordx4 v[116:119],v239,s[44:47],s82 offen offset:2048 sc1
s_addk_i32 s82,4096
buffer_load_dwordx4 v[120:123],v239,s[44:47],s82 offen sc1
buffer_load_dwordx4 v[124:127],v239,s[44:47],s82 offen offset:2048 sc1
global_load_dwordx2 v[38:39],v33,s[52:53] offset:24 sc1
mz v,0,a,244,a,96
mz v,4,a,248,a,96
ma v,0,a,244,a,100
ma v,4,a,248,a,100
ma v,0,a,244,a,104
ma v,4,a,248,a,104
ma v,0,a,244,a,108
ma v,4,a,248,a,108
ma v,0,a,244,a,112
ma v,4,a,248,a,112
ma v,0,a,244,a,116
ma v,4,a,248,a,116
ma v,0,a,244,a,120
ma v,4,a,248,a,120
ma v,0,a,244,a,124
ma v,4,a,248,a,124
ma v,0,a,244,a,128
ma v,4,a,248,a,128
ma v,0,a,244,a,132
ma v,4,a,248,a,132
ma v,0,a,244,a,136
ma v,4,a,248,a,136
ma v,0,a,244,a,140
ma v,4,a,248,a,140
ma v,0,a,244,a,144
ma v,4,a,248,a,144
ma v,0,a,244,a,148
ma v,4,a,248,a,148
ma v,0,a,244,a,152
ma v,4,a,248,a,152
ma v,0,a,244,a,156
ma v,4,a,248,a,156
mz v,8,a,244,a,160
mz v,12,a,248,a,160
ma v,8,a,244,a,164
ma v,12,a,248,a,164
ma v,8,a,244,a,168
ma v,12,a,248,a,168
ma v,8,a,244,a,172
ma v,12,a,248,a,172
ma v,8,a,244,a,176
ma v,12,a,248,a,176
ma v,8,a,244,a,180
ma v,12,a,248,a,180
ma v,8,a,244,a,184
ma v,12,a,248,a,184
ma v,8,a,244,a,188
ma v,12,a,248,a,188
ma v,8,a,244,a,192
ma v,12,a,248,a,192
ma v,8,a,244,a,196
ma v,12,a,248,a,196
ma v,8,a,244,a,200
ma v,12,a,248,a,200
ma v,8,a,244,a,204
ma v,12,a,248,a,204
ma v,8,a,244,a,208
ma v,12,a,248,a,208
ma v,8,a,244,a,212
ma v,12,a,248,a,212
ma v,8,a,244,a,216
ma v,12,a,248,a,216
ma v,8,a,244,a,220
ma v,12,a,248,a,220
s_waitcnt vmcnt(0)
s_mov_b32 s70,0
LB_pl3_%=:
v_cmp_ne_u64_e64 vcc,v[38:39],s[56:57]
s_add_u32 s70,s70,1
s_cbranch_vccz LB_pk3_%=
s_nop 0
global_load_dwordx2 v[38:39],v33,s[52:53] offset:24 sc1
s_waitcnt vmcnt(0)
s_cmp_lt_u32 s70,0x40000
s_cbranch_scc1 LB_pl3_%=
LB_pk3_%=:
s_cmp_lg_u32 s66,0
s_cbranch_scc1 LB_nob_%=
v_mov_b32 v246,0
s_mov_b64 exec,1
global_store_dwordx2 v246,v[36:37],s[54:55] sc1
s_mov_b64 exec,-1
LB_nob_%=:
s_barrier
buffer_load_dwordx4 v[128:131],v239,s[44:47],s96 offen sc1
buffer_load_dwordx4 v[132:135],v239,s[44:47],s96 offen offset:2048 sc1
s_addk_i32 s96,4096
buffer_load_dwordx4 v[136:139],v239,s[44:47],s96 offen sc1
buffer_load_dwordx4 v[140:143],v239,s[44:47],s96 offen offset:2048 sc1
s_addk_i32 s96,4096
buffer_load_dwordx4 v[144:147],v239,s[44:47],s96 offen sc1
buffer_load_dwordx4 v[148:151],v239,s[44:47],s96 offen offset:2048 sc1
s_addk_i32 s96,4096
buffer_load_dwordx4 v[152:155],v239,s[44:47],s96 offen sc1
buffer_load_dwordx4 v[156:159],v239,s[44:47],s96 offen offset:2048 sc1
s_addk_i32 s96,4096
buffer_load_dwordx4 v[160:163],v239,s[44:47],s96 offen sc1
buffer_load_dwordx4 v[164:167],v239,s[44:47],s96 offen offset:2048 sc1
s_addk_i32 s96,4096
buffer_load_dwordx4 v[168:171],v239,s[44:47],s96 offen sc1
buffer_load_dwordx4 v[172:175],v239,s[44:47],s96 offen offset:2048 sc1
s_addk_i32 s96,4096
buffer_load_dwordx4 v[176:179],v239,s[44:47],s96 offen sc1
buffer_load_dwordx4 v[180:183],v239,s[44:47],s96 offen offset:2048 sc1
s_addk_i32 s96,4096
buffer_load_dwordx4 v[184:187],v239,s[44:47],s96 offen sc1
buffer_load_dwordx4 v[188:191],v239,s[44:47],s96 offen offset:2048 sc1
cv 40,0,1
cv 41,2,3
cv 42,4,5
cv 43,6,7
buffer_store_dwordx4 v[40:43],v34,s[48:51],s83 offen
cv 44,8,9
cv 45,10,11
cv 46,12,13
cv 47,14,15
buffer_store_dwordx4 v[44:47],v34,s[48:51],s84 offen
mz v,16,a,244,v,64
mz v,20,a,248,v,64
ma v,16,a,244,v,68
ma v,20,a,248,v,68
ma v,16,a,244,v,72
ma v,20,a,248,v,72
ma v,16,a,244,v,76
ma v,20,a,248,v,76
ma v,16,a,244,v,80
ma v,20,a,248,v,80
ma v,16,a,244,v,84
ma v,20,a,248,v,84
ma v,16,a,244,v,88
ma v,20,a,248,v,88
ma v,16,a,244,v,92
ma v,20,a,248,v,92
ma v,16,a,244,v,96
ma v,20,a,248,v,96
ma v,16,a,244,v,100
ma v,20,a,248,v,100
ma v,16,a,244,v,104
ma v,20,a,248,v,104
ma v,16,a,244,v,108
ma v,20,a,248,v,108
ma v,16,a,244,v,112
ma v,20,a,248,v,112
ma v,16,a,244,v,116
ma v,20,a,248,v,116
ma v,16,a,244,v,120
ma v,20,a,248,v,120
ma v,16,a,244,v,124
ma v,20,a,248,v,124
s_nop 6
cv 200,16,17
cv 201,18,19
cv 202,20,21
cv 203,22,23
buffer_store_dwordx4 v[200:203],v34,s[48:51],s85 offen
s_waitcnt vmcnt(17)
mz v,24,a,244,v,128
mz v,28,a,248,v,128
ma v,24,a,244,v,132
ma v,28,a,248,v,132
s_waitcnt vmcnt(15)
ma v,24,a,244,v,136
ma v,28,a,248,v,136
ma v,24,a,244,v,140
ma v,28,a,248,v,140
s_waitcnt vmcnt(13)
ma v,24,a,244,v,144
ma v,28,a,248,v,144
ma v,24,a,244,v,148
ma v,28,a,248,v,148
s_waitcnt vmcnt(11)
ma v,24,a,244,v,152
ma v,28,a,248,v,152
ma v,24,a,244,v,156
ma v,28,a,248,v,156
)"
R"(s_waitcnt vmcnt(9)
ma v,24,a,244,v,160
ma v,28,a,248,v,160
ma v,24,a,244,v,164
ma v,28,a,248,v,164
s_waitcnt vmcnt(7)
ma v,24,a,244,v,168
ma v,28,a,248,v,168
ma v,24,a,244,v,172
ma v,28,a,248,v,172
s_waitcnt vmcnt(5)
ma v,24,a,244,v,176
ma v,28,a,248,v,176
ma v,24,a,244,v,180
ma v,28,a,248,v,180
s_waitcnt vmcnt(3)
ma v,24,a,244,v,184
ma v,28,a,248,v,184
ma v,24,a,244,v,188
ma v,28,a,248,v,188
s_nop 6
cv 204,24,25
cv 205,26,27
cv 206,28,29
cv 207,30,31
buffer_store_dwordx4 v[204:207],v34,s[48:51],s97 offen
s_branch Lend_%=
LB_a0i0_%=:
s_getpc_b64 s[60:61]
s_branch LB_rare1_%=
s_branch LB_a0i0_ret_%=
LB_a0i1_%=:
s_getpc_b64 s[60:61]
s_branch LB_rare0_%=
s_branch LB_a0i1_ret_%=
LB_a0i2_%=:
s_getpc_b64 s[60:61]
s_branch LB_rare1_%=
s_branch LB_a0i2_ret_%=
LB_a1i0_%=:
s_getpc_b64 s[60:61]
s_branch LB_rare1_%=
s_branch LB_a1i0_ret_%=
LB_a1s0i1_%=:
s_getpc_b64 s[60:61]
s_branch LB_rare0_%=
s_branch LB_a1s0i1_ret_%=
LB_a1s0i2_%=:
s_getpc_b64 s[60:61]
s_branch LB_rare1_%=
s_branch LB_a1s0i2_ret_%=
LB_a1k0i1_%=:
s_getpc_b64 s[60:61]
s_branch LB_rare0_%=
s_branch LB_a1k0i1_ret_%=
LB_a1k0i2_%=:
s_getpc_b64 s[60:61]
s_branch LB_rare1_%=
s_branch LB_a1k0i2_ret_%=
LB_a1s1i1_%=:
s_getpc_b64 s[60:61]
s_branch LB_rare0_%=
s_branch LB_a1s1i1_ret_%=
LB_a1s1i2_%=:
s_getpc_b64 s[60:61]
s_branch LB_rare1_%=
s_branch LB_a1s1i2_ret_%=
LB_a1k1i1_%=:
s_getpc_b64 s[60:61]
s_branch LB_rare0_%=
s_branch LB_a1k1i1_ret_%=
LB_a1k1i2_%=:
s_getpc_b64 s[60:61]
s_branch LB_rare1_%=
s_branch LB_a1k1i2_ret_%=
LB_rare0_%=:
s_cselect_b32 s92,1,0
s_nop 7
v_mov_b32 v242,v240
s_nop 1
v_permlane32_swap_b32 v240,v242
v_max_f32 v240,v240,v242
v_mov_b32 v242,v240
s_nop 1
v_permlane16_swap_b32 v240,v242
v_max_f32 v240,v240,v242
v_max_f32 v240,0,v240
v_exp_f32 v248,-v240
v_sub_f32 v32,v32,v240
v_sub_f32 v33,v33,v240
v_sub_f32 v34,v34,v240
v_sub_f32 v35,v35,v240
v_sub_f32 v0,v0,v240
v_sub_f32 v1,v1,v240
v_sub_f32 v2,v2,v240
v_sub_f32 v3,v3,v240
v_sub_f32 v8,v8,v240
v_sub_f32 v9,v9,v240
v_sub_f32 v10,v10,v240
v_sub_f32 v11,v11,v240
v_pk_mul_f32 v[48:49],v[48:49],v[248:249] op_sel_hi:[1,0]
v_pk_mul_f32 v[50:51],v[50:51],v[248:249] op_sel_hi:[1,0]
v_pk_mul_f32 v[56:57],v[56:57],v[248:249] op_sel_hi:[1,0]
v_pk_mul_f32 v[58:59],v[58:59],v[248:249] op_sel_hi:[1,0]
v_pk_mul_f32 v[64:65],v[64:65],v[248:249] op_sel_hi:[1,0]
v_pk_mul_f32 v[66:67],v[66:67],v[248:249] op_sel_hi:[1,0]
v_pk_mul_f32 v[72:73],v[72:73],v[248:249] op_sel_hi:[1,0]
v_pk_mul_f32 v[74:75],v[74:75],v[248:249] op_sel_hi:[1,0]
v_pk_mul_f32 v[80:81],v[80:81],v[248:249] op_sel_hi:[1,0]
v_pk_mul_f32 v[82:83],v[82:83],v[248:249] op_sel_hi:[1,0]
v_pk_mul_f32 v[88:89],v[88:89],v[248:249] op_sel_hi:[1,0]
v_pk_mul_f32 v[90:91],v[90:91],v[248:249] op_sel_hi:[1,0]
v_pk_mul_f32 v[96:97],v[96:97],v[248:249] op_sel_hi:[1,0]
v_pk_mul_f32 v[98:99],v[98:99],v[248:249] op_sel_hi:[1,0]
v_pk_mul_f32 v[104:105],v[104:105],v[248:249] op_sel_hi:[1,0]
v_pk_mul_f32 v[106:107],v[106:107],v[248:249] op_sel_hi:[1,0]
v_pk_mul_f32 v[112:113],v[112:113],v[248:249] op_sel_hi:[1,0]
v_pk_mul_f32 v[114:115],v[114:115],v[248:249] op_sel_hi:[1,0]
v_pk_mul_f32 v[120:121],v[120:121],v[248:249] op_sel_hi:[1,0]
v_pk_mul_f32 v[122:123],v[122:123],v[248:249] op_sel_hi:[1,0]
v_pk_mul_f32 v[128:129],v[128:129],v[248:249] op_sel_hi:[1,0]
v_pk_mul_f32 v[130:131],v[130:131],v[248:249] op_sel_hi:[1,0]
v_pk_mul_f32 v[136:137],v[136:137],v[248:249] op_sel_hi:[1,0]
v_pk_mul_f32 v[138:139],v[138:139],v[248:249] op_sel_hi:[1,0]
v_pk_mul_f32 v[144:145],v[144:145],v[248:249] op_sel_hi:[1,0]
v_pk_mul_f32 v[146:147],v[146:147],v[248:249] op_sel_hi:[1,0]
s_add_u32 s60,s60,4
s_addc_u32 s61,s61,0
s_cmp_eq_u32 s92,1
s_setpc_b64 s[60:61]
LB_rare1_%=:
s_cselect_b32 s92,1,0
s_nop 7
v_mov_b32 v242,v240
s_nop 1
v_permlane32_swap_b32 v240,v242
v_max_f32 v240,v240,v242
v_mov_b32 v242,v240
s_nop 1
v_permlane16_swap_b32 v240,v242
v_max_f32 v240,v240,v242
v_max_f32 v240,0,v240
v_exp_f32 v248,-v240
v_sub_f32 v32,v32,v240
v_sub_f32 v33,v33,v240
v_sub_f32 v34,v34,v240
v_sub_f32 v35,v35,v240
v_sub_f32 v16,v16,v240
v_sub_f32 v17,v17,v240
v_sub_f32 v18,v18,v240
v_sub_f32 v19,v19,v240
v_sub_f32 v24,v24,v240
v_sub_f32 v25,v25,v240
v_sub_f32 v26,v26,v240
v_sub_f32 v27,v27,v240
v_pk_mul_f32 v[48:49],v[48:49],v[248:249] op_sel_hi:[1,0]
v_pk_mul_f32 v[50:51],v[50:51],v[248:249] op_sel_hi:[1,0]
v_pk_mul_f32 v[56:57],v[56:57],v[248:249] op_sel_hi:[1,0]
v_pk_mul_f32 v[58:59],v[58:59],v[248:249] op_sel_hi:[1,0]
v_pk_mul_f32 v[64:65],v[64:65],v[248:249] op_sel_hi:[1,0]
v_pk_mul_f32 v[66:67],v[66:67],v[248:249] op_sel_hi:[1,0]
v_pk_mul_f32 v[72:73],v[72:73],v[248:249] op_sel_hi:[1,0]
v_pk_mul_f32 v[74:75],v[74:75],v[248:249] op_sel_hi:[1,0]
v_pk_mul_f32 v[80:81],v[80:81],v[248:249] op_sel_hi:[1,0]
v_pk_mul_f32 v[82:83],v[82:83],v[248:249] op_sel_hi:[1,0]
v_pk_mul_f32 v[88:89],v[88:89],v[248:249] op_sel_hi:[1,0]
v_pk_mul_f32 v[90:91],v[90:91],v[248:249] op_sel_hi:[1,0]
v_pk_mul_f32 v[96:97],v[96:97],v[248:249] op_sel_hi:[1,0]
v_pk_mul_f32 v[98:99],v[98:99],v[248:249] op_sel_hi:[1,0]
v_pk_mul_f32 v[104:105],v[104:105],v[248:249] op_sel_hi:[1,0]
v_pk_mul_f32 v[106:107],v[106:107],v[248:249] op_sel_hi:[1,0]
v_pk_mul_f32 v[112:113],v[112:113],v[248:249] op_sel_hi:[1,0]
v_pk_mul_f32 v[114:115],v[114:115],v[248:249] op_sel_hi:[1,0]
v_pk_mul_f32 v[120:121],v[120:121],v[248:249] op_sel_hi:[1,0]
v_pk_mul_f32 v[122:123],v[122:123],v[248:249] op_sel_hi:[1,0]
v_pk_mul_f32 v[128:129],v[128:129],v[248:249] op_sel_hi:[1,0]
v_pk_mul_f32 v[130:131],v[130:131],v[248:249] op_sel_hi:[1,0]
v_pk_mul_f32 v[136:137],v[136:137],v[248:249] op_sel_hi:[1,0]
v_pk_mul_f32 v[138:139],v[138:139],v[248:249] op_sel_hi:[1,0]
v_pk_mul_f32 v[144:145],v[144:145],v[248:249] op_sel_hi:[1,0]
v_pk_mul_f32 v[146:147],v[146:147],v[248:249] op_sel_hi:[1,0]
s_add_u32 s60,s60,4
s_addc_u32 s61,s61,0
s_cmp_eq_u32 s92,1
s_setpc_b64 s[60:61]
Lend_%=:
.purgem mz
.purgem ma
.purgem mc
.purgem tr
.purgem rd
.purgem dl
.purgem dn
.purgem wl
.purgem cv
)"
    :
    : "s"(__builtin_amdgcn_kernarg_segment_ptr()), "s"(blockIdx.x), "s"(wid), "s"(ldsbase)
    : "v0", "v1", "v2", "v3", "v4", "v5", "v6", "v7", "v8", "v9", "v10", "v11", "v12", "v13", "v14", "v15", "v16", "v17", "v18", "v19", "v20", "v21", "v22", "v23", "v24", "v25", "v26", "v27", "v28", "v29", "v30", "v31", "v32", "v33", "v34", "v35", "v36", "v37", "v38", "v39", "v40", "v41", "v42", "v43", "v44", "v45", "v46", "v47", "v48", "v49", "v50", "v51", "v52", "v53", "v54", "v55", "v56", "v57", "v58", "v59", "v60", "v61", "v62", "v63", "v64", "v65", "v66", "v67", "v68", "v69", "v70", "v71", "v72", "v73", "v74", "v75", "v76", "v77", "v78", "v79", "v80", "v81", "v82", "v83", "v84", "v85", "v86", "v87", "v88", "v89", "v90", "v91", "v92", "v93", "v94", "v95", "v96", "v97", "v98", "v99", "v100", "v101", "v102", "v103", "v104", "v105", "v106", "v107", "v108", "v109", "v110", "v111", "v112", "v113", "v114", "v115", "v116", "v117", "v118", "v119", "v120", "v121", "v122", "v123", "v124", "v125", "v126", "v127", "v128", "v129", "v130", "v131", "v132", "v133", "v134", "v135", "v136", "v137", "v138", "v139", "v140", "v141", "v142", "v143", "v144", "v145", "v146", "v147", "v148", "v149", "v150", "v151", "v152", "v153", "v154", "v155", "v156", "v157", "v158", "v159", "v160", "v161", "v162", "v163", "v164", "v165", "v166", "v167", "v168", "v169", "v170", "v171", "v172", "v173", "v174", "v175", "v176", "v177", "v178", "v179", "v180", "v181", "v182", "v183", "v184", "v185", "v186", "v187", "v188", "v189", "v190", "v191", "v192", "v193", "v194", "v195", "v196", "v197", "v198", "v199", "v200", "v201", "v202", "v203", "v204", "v205", "v206", "v207", "v208", "v209", "v210", "v211", "v212", "v213", "v214", "v215", "v216", "v217", "v218", "v219", "v220", "v221", "v222", "v223", "v224", "v225", "v226", "v227", "v228", "v229", "v230", "v231", "v232", "v233", "v234", "v235", "v236", "v237", "v238", "v239", "v240", "v241", "v242", "v243", "v244", "v245", "v246", "v247", "v248", "v249", "v250", "v251", "v252", "v253", "v254", "v255", "a0", "a1", "a2", "a3", "a4", "a5", "a6", "a7", "a8", "a9", "a10", "a11", "a12", "a13", "a14", "a15", "a16", "a17", "a18", "a19", "a20", "a21", "a22", "a23", "a24", "a25", "a26", "a27", "a28", "a29", "a30", "a31", "a32", "a33", "a34", "a35", "a36", "a37", "a38", "a39", "a40", "a41", "a42", "a43", "a44", "a45", "a46", "a47", "a48", "a49", "a50", "a51", "a52", "a53", "a54", "a55", "a56", "a57", "a58", "a59", "a60", "a61", "a62", "a63", "a64", "a65", "a66", "a67", "a68", "a69", "a70", "a71", "a72", "a73", "a74", "a75", "a76", "a77", "a78", "a79", "a80", "a81", "a82", "a83", "a84", "a85", "a86", "a87", "a88", "a89", "a90", "a91", "a92", "a93", "a94", "a95", "a96", "a97", "a98", "a99", "a100", "a101", "a102", "a103", "a104", "a105", "a106", "a107", "a108", "a109", "a110", "a111", "a112", "a113", "a114", "a115", "a116", "a117", "a118", "a119", "a120", "a121", "a122", "a123", "a124", "a125", "a126", "a127", "a128", "a129", "a130", "a131", "a132", "a133", "a134", "a135", "a136", "a137", "a138", "a139", "a140", "a141", "a142", "a143", "a144", "a145", "a146", "a147", "a148", "a149", "a150", "a151", "a152", "a153", "a154", "a155", "a156", "a157", "a158", "a159", "a160", "a161", "a162", "a163", "a164", "a165", "a166", "a167", "a168", "a169", "a170", "a171", "a172", "a173", "a174", "a175", "a176", "a177", "a178", "a179", "a180", "a181", "a182", "a183", "a184", "a185", "a186", "a187", "a188", "a189", "a190", "a191", "a192", "a193", "a194", "a195", "a196", "a197", "a198", "a199", "a200", "a201", "a202", "a203", "a204", "a205", "a206", "a207", "a208", "a209", "a210", "a211", "a212", "a213", "a214", "a215", "a216", "a217", "a218", "a219", "a220", "a221", "a222", "a223", "a224", "a225", "a226", "a227", "a228", "a229", "a230", "a231", "a232", "a233", "a234", "a235", "a236", "a237", "a238", "a239", "a240", "a241", "a242", "a243", "a244", "a245", "a246", "a247", "a248", "a249", "a250", "a251", "a252", "a253", "a254", "a255", "s4", "s5", "s6", "s7", "s8", "s9", "s10", "s11", "s12", "s13", "s14", "s15", "s16", "s17", "s20", "s21", "s22", "s23", "s24", "s25", "s26", "s27", "s28", "s29", "s30", "s31", "s34", "s35", "s36", "s37", "s38", "s39", "s40", "s41", "s42", "s43", "s44", "s45", "s46", "s47", "s48", "s49", "s50", "s51", "s52", "s53", "s54", "s55", "s56", "s57", "s58", "s59", "s60", "s61", "s62", "s63", "s64", "s65", "s66", "s67", "s68", "s69", "s70", "s71", "s72", "s73", "s74", "s75", "s76", "s77", "s78", "s79", "s80", "s81", "s82", "s83", "s84", "s85", "s86", "s87", "s88", "s89", "s90", "s91", "s92", "s93", "s94", "s95", "s96", "s97", "s98", "s99", "vcc", "scc", "memory");
}

extern "C" void kernel_launch(void* const* d_in, const int* in_sizes, int n_in, void* d_out, int out_size, void* d_ws,
                              size_t ws_size, hipStream_t stream) {
  mha_block_fused<<<256, 256, 0, stream>>>(d_in[0], d_in[1], d_in[2], d_in[3], d_in[4], d_out, (char*)d_ws + ((ws_size - (40u << 20)) & ~(size_t)4095));
}
```
